```python
import torch

def _softmax(x, axis=-1):
    return torch.softmax(x, dim=axis)
def _gelu(x):
    return torch.nn.functional.gelu(x, approximate="tanh")
def _silu(x):
    return torch.nn.functional.silu(x)
def _sigmoid(x):
    return torch.sigmoid(x)
def _layernorm(x, axis=-1, eps=1e-5):
    m = x.mean(dim=axis, keepdim=True)
    v = x.var(dim=axis, keepdim=True, unbiased=False)
    return (x - m) / torch.sqrt(v + eps)
def _scatter_add(tgt, idx, src, axis=0):
    return tgt.clone().index_add_(axis, idx, src)
def _segment_sum(src, seg, n):
    out = torch.zeros((n,) + src.shape[1:], dtype=src.dtype)
    return out.index_add_(0, seg, src)
def _pad(x, pw):
    flat = tuple(v for pair in reversed(pw) for v in pair)
    return torch.nn.functional.pad(x, flat)

kernel_name = 'concat_heads__compressed_kv_cla__sliding_block_cached__decode'

def setup_inputs():

    _w = (lambda a: (lambda t: t.to(torch.bfloat16) if t.is_floating_point() else t)(torch.as_tensor(a)))
    def _A_setup():
        B, H, Sq, Sk, D, M = 2, 32, 1, 8192, 128, 512
        Dc = 4 * D
        return {
            "x": _w((lambda *s: torch.randn(*s, dtype=torch.bfloat16) * 3.2)(B, Sq, M)),
            "c_kv": _w((lambda *s: torch.randn(*s, dtype=torch.bfloat16) * 0.1)(B, Sk, Dc)),
            "w_q": _w((lambda *s: torch.randn(*s, dtype=torch.bfloat16) * 6.4)(M, H, D) * (M ** -0.5)),
            "w_uk": _w((lambda *s: torch.randn(*s, dtype=torch.bfloat16) * 6.4)(Dc, H, D) * (Dc ** -0.5)),
            "w_uv": _w((lambda *s: torch.randn(*s, dtype=torch.bfloat16) * 0.1)(Dc, H, D) * (Dc ** -0.5)),
            "w_o": _w((lambda *s: torch.randn(*s, dtype=torch.bfloat16) * 0.1)(H, D, M) * ((H * D) ** -0.5)),
        }
    _A_in = _A_setup()

    def _B_setup():
        B, Sq, D = 2, 1, 512
        Hq, Hkv, Sk, Dh = 32, 8, 8192, 128
        assert Hq % Hkv == 0, (Hq, Hkv)
        return {
            "x": _w((lambda *s: torch.randn(*s, dtype=torch.bfloat16) * 1.6)(B, Sq, D)),
            "k_cache": _w((lambda *s: torch.randn(*s, dtype=torch.bfloat16) * 1.6)(B, Hkv, Sk, Dh)),
            "v_cache": _w((lambda *s: torch.randn(*s, dtype=torch.bfloat16) * 0.05)(B, Hkv, Sk, Dh)),
            "wq": _w((lambda *s: torch.randn(*s, dtype=torch.bfloat16) * 1.6)(D, Hq * Dh) * (D ** -0.5)),
            "wo": _w((lambda *s: torch.randn(*s, dtype=torch.bfloat16) * 0.05)(Hq * Dh, D) * ((Hq * Dh) ** -0.5)),
        }
    _B_in = _B_setup()

    return {
        "A_x": _A_in["x"],
        "A_c_kv": _A_in["c_kv"],
        "A_w_q": _A_in["w_q"],
        "A_w_uk": _A_in["w_uk"],
        "A_w_uv": _A_in["w_uv"],
        "A_w_o": _A_in["w_o"],
        "B_x": _B_in["x"],
        "B_k_cache": _B_in["k_cache"],
        "B_v_cache": _B_in["v_cache"],
        "B_wq": _B_in["wq"],
        "B_wo": _B_in["wo"],
    }

def reference(A_x, A_c_kv, A_w_q, A_w_uk, A_w_uv, A_w_o, B_x, B_k_cache, B_v_cache, B_wq, B_wo):
    def _A_ref(x, c_kv, w_q, w_uk, w_uv, w_o):
        B, Sq, M = x.shape[0], x.shape[1], x.shape[2]
        Sk = c_kv.shape[1]
        H, D = w_q.shape[1], w_q.shape[2]
        q = torch.einsum("bqm,mhd->bhqd", x, w_q)
        k = torch.einsum("bkc,chd->bhkd", c_kv, w_uk)
        v = torch.einsum("bkc,chd->bhkd", c_kv, w_uv)
        scores = torch.einsum("bhqd,bhkd->bhqk", q, k) * (D ** -0.5)
        off = Sk - Sq
        qi = torch.arange(Sq)[:, None]
        kj = torch.arange(Sk)[None, :]
        cmask = kj <= (qi + off)
        scores = torch.where(cmask[None, None], scores, -1e9)
        p = _softmax(scores, axis=-1)
        ctx = torch.einsum("bhqk,bhkd->bhqd", p, v)
        return torch.einsum("bhqd,hdm->bqm", ctx, w_o)

    def _B_ref(x, k_cache, v_cache, wq, wo):
        B, Sq, D = x.shape
        Hq, Dh = 32, 128
        Hkv, Sk = k_cache.shape[1], k_cache.shape[2]
        W = min(4096, max(256, Sk // 4))
        rep = Hq // Hkv
        q = (lambda x, *d: x.permute(*d))((x @ wq).reshape(B, Sq, Hq, Dh), 0, 2, 1, 3)
        k = (lambda x, n, axis: torch.repeat_interleave(x, n, dim=axis))(k_cache[:, :, Sk - W:, :], rep, axis=1)
        v = (lambda x, n, axis: torch.repeat_interleave(x, n, dim=axis))(v_cache[:, :, Sk - W:, :], rep, axis=1)
        scores = torch.einsum("bhqd,bhkd->bhqk", q, k) * (Dh ** -0.5)
        p = _softmax(scores, axis=-1)
        o = torch.einsum("bhqk,bhkd->bhqd", p, v)
        return (lambda x, *d: x.permute(*d))(o, 0, 2, 1, 3).reshape(B, Sq, Hq * Dh) @ wo

    a = _A_ref(A_x, A_c_kv, A_w_q, A_w_uk, A_w_uv, A_w_o)
    b = _B_ref(B_x, B_k_cache, B_v_cache, B_wq, B_wo)
    return torch.concatenate([a, b], axis=-1)
```

```cpp
#include <hip/hip_runtime.h>
#include <hip/hip_bf16.h>
typedef unsigned short u16;
__device__ __forceinline__ float bf2f(u16 v){ return __uint_as_float(((unsigned)v)<<16); }
__device__ __forceinline__ u16 f2bf(float f){ unsigned u=__float_as_uint(f); u += 0x7fffu + ((u>>16)&1u); return (u16)(u>>16); }

__global__ void k_q(const u16* xA, const u16* wqA, const u16* xB, const u16* wqB, float* q){
  int which = blockIdx.x>>6, cb = blockIdx.x&63; int t=threadIdx.x; int col = cb*64 + (t&63), mg=t>>6;
  const u16* x = which? xB: xA; const u16* w = which? wqB: wqA;
  float a0=0,a1=0;
  for(int m=mg*128;m<mg*128+128;m++){ float wv=bf2f(w[(size_t)m*4096+col]); a0+=bf2f(x[m])*wv; a1+=bf2f(x[512+m])*wv; }
  __shared__ float s[2][4][64]; s[0][mg][t&63]=a0; s[1][mg][t&63]=a1; __syncthreads();
  if(t<128){ int b=t>>6,c=t&63; float v=s[b][0][c]+s[b][1][c]+s[b][2][c]+s[b][3][c]; q[(which*2+b)*4096+cb*64+c]=bf2f(f2bf(v)); }
}
__global__ void k_qabs(const float* q, const u16* wuk, float* qabs){
  int wv = blockIdx.x*4 + (threadIdx.x>>6); int l=threadIdx.x&63; int c = wv>>5, h = wv&31;
  const u16* w = wuk + ((size_t)c*32+h)*128 + l*2; float w0=bf2f(w[0]), w1=bf2f(w[1]);
  for(int b=0;b<2;b++){ const float* qq=q+b*4096+h*128+l*2; float v=qq[0]*w0+qq[1]*w1; for(int o=32;o>0;o>>=1) v+=__shfl_xor(v,o); if(l==0) qabs[(b*32+h)*512+c]=v; }
}
__global__ void k_sA(const float* qabs, const u16* ckv, float* s){
  int wv = blockIdx.x*4 + (threadIdx.x>>6); int l=threadIdx.x&63; int b=wv>>13, k=wv&8191;
  float cv[8]; const u16* c=ckv+((size_t)b*8192+k)*512+l*8; for(int i=0;i<8;i++) cv[i]=bf2f(c[i]);
  for(int h=0;h<32;h++){ const float* qq=qabs+(b*32+h)*512+l*8; float v=0; for(int i=0;i<8;i++) v+=cv[i]*qq[i]; for(int o=32;o>0;o>>=1) v+=__shfl_xor(v,o); if(l==0) s[((size_t)b*32+h)*8192+k]=v*0.08838834764831845f; }
}
__global__ void k_sB(const float* q, const u16* kc, float* s){
  int wv = blockIdx.x*4 + (threadIdx.x>>6); int l=threadIdx.x&63; int k=wv&2047, kvh=(wv>>11)&7, b=wv>>14;
  const u16* c=kc+(((size_t)b*8+kvh)*8192+6144+k)*128+l*2; float c0=bf2f(c[0]),c1=bf2f(c[1]);
  for(int g=0;g<4;g++){ int h=kvh*4+g; const float* qq=q+(2+b)*4096+h*128+l*2; float v=c0*qq[0]+c1*qq[1]; for(int o=32;o>0;o>>=1) v+=__shfl_xor(v,o); if(l==0) s[((size_t)b*32+h)*2048+k]=v*0.08838834764831845f; }
}
__global__ void k_softmax(float* s, int n){
  float* r=s+(size_t)blockIdx.x*n; int t=threadIdx.x; __shared__ float sh[256];
  float m=-INFINITY; for(int i=t;i<n;i+=256) m=fmaxf(m,r[i]); sh[t]=m; __syncthreads();
  for(int o=128;o>0;o>>=1){ if(t<o) sh[t]=fmaxf(sh[t],sh[t+o]); __syncthreads(); } m=sh[0]; __syncthreads();
  float sum=0; for(int i=t;i<n;i+=256){ float e=__expf(r[i]-m); r[i]=e; sum+=e; } sh[t]=sum; __syncthreads();
  for(int o=128;o>0;o>>=1){ if(t<o) sh[t]+=sh[t+o]; __syncthreads(); } float inv=1.f/sh[0];
  for(int i=t;i<n;i+=256) r[i]*=inv;
}
__global__ void k_pvA(const float* p, const u16* ckv, float* olat){
  int ch=blockIdx.x&7, h=(blockIdx.x>>3)&31, b=blockIdx.x>>8; int c=threadIdx.x; float a=0;
  const float* pp=p+((size_t)b*32+h)*8192+ch*1024; const u16* cc=ckv+((size_t)b*8192+ch*1024)*512+c;
  for(int k=0;k<1024;k++) a+=pp[k]*bf2f(cc[(size_t)k*512]);
  olat[(((size_t)b*32+h)*8+ch)*512+c]=a;
}
__global__ void k_ctxA(const float* olat, const u16* wuv, float* ctx){
  int h=blockIdx.x&31, b=blockIdx.x>>5; int d=threadIdx.x; __shared__ float o[512];
  for(int c=d;c<512;c+=128){ float a=0; for(int ch=0;ch<8;ch++) a+=olat[(((size_t)b*32+h)*8+ch)*512+c]; o[c]=a; } __syncthreads();
  float a=0; for(int c=0;c<512;c++) a+=o[c]*bf2f(wuv[((size_t)c*32+h)*128+d]);
  ctx[b*4096+h*128+d]=bf2f(f2bf(a));
}
__global__ void k_pvB(const float* p, const u16* vc, float* ctx){
  int h=blockIdx.x&31, b=blockIdx.x>>5; int d=threadIdx.x&127, g=threadIdx.x>>7; float a=0; int kvh=h>>2;
  const float* pp=p+((size_t)b*32+h)*2048; const u16* vv=vc+(((size_t)b*8+kvh)*8192+6144)*128+d;
  for(int k=g*512;k<g*512+512;k++) a+=pp[k]*bf2f(vv[(size_t)k*128]);
  __shared__ float sh[4][128]; sh[g][d]=a; __syncthreads();
  if(g==0) ctx[(2+b)*4096+h*128+d]=bf2f(f2bf(sh[0][d]+sh[1][d]+sh[2][d]+sh[3][d]));
}
__global__ void k_out(const float* ctx, const u16* woA, const u16* woB, u16* out){
  int mb=blockIdx.x&7, b=(blockIdx.x>>3)&1, which=blockIdx.x>>4; int t=threadIdx.x; int m=mb*64+(t&63), g=t>>6;
  const u16* w=which?woB:woA; const float* c=ctx+(which*2+b)*4096; float a=0;
  for(int k=g*1024;k<g*1024+1024;k++) a+=c[k]*bf2f(w[(size_t)k*512+m]);
  __shared__ float sh[4][64]; sh[g][t&63]=a; __syncthreads();
  if(g==0) out[b*1024+which*512+m]=f2bf(sh[0][t]+sh[1][t]+sh[2][t]+sh[3][t]);
}
extern "C" void kernel_launch(void* const* d_in, const int* in_sizes, int n_in, void* d_out, int out_size, void* d_ws, size_t ws_size, hipStream_t stream){
  const u16* xA=(const u16*)d_in[0]; const u16* ckv=(const u16*)d_in[1]; const u16* wqA=(const u16*)d_in[2]; const u16* wuk=(const u16*)d_in[3];
  const u16* wuv=(const u16*)d_in[4]; const u16* woA=(const u16*)d_in[5]; const u16* xB=(const u16*)d_in[6]; const u16* kc=(const u16*)d_in[7];
  const u16* vc=(const u16*)d_in[8]; const u16* wqB=(const u16*)d_in[9]; const u16* woB=(const u16*)d_in[10];
  float* ws=(float*)d_ws; float* q=ws; float* qabs=q+4*4096; float* sA=qabs+2*32*512; float* sB=sA+2*32*8192; float* olat=sB+2*32*2048; float* ctx=olat+2*32*8*512;
  k_q<<<128,256,0,stream>>>(xA,wqA,xB,wqB,q);
  k_qabs<<<512*32/4,256,0,stream>>>(q,wuk,qabs);
  k_sA<<<16384/4,256,0,stream>>>(qabs,ckv,sA);
  k_sB<<<32768/4,256,0,stream>>>(q,kc,sB);
  k_softmax<<<64,256,0,stream>>>(sA,8192);
  k_softmax<<<64,256,0,stream>>>(sB,2048);
  k_pvA<<<512,512,0,stream>>>(sA,ckv,olat);
  k_ctxA<<<64,128,0,stream>>>(olat,wuv,ctx);
  k_pvB<<<64,512,0,stream>>>(sB,vc,ctx);
  k_out<<<32,256,0,stream>>>(ctx,woA,woB,(u16*)d_out);
}
```

```cpp
#include <hip/hip_runtime.h>
#include <hip/hip_bf16.h>

typedef unsigned u32;
typedef unsigned short u16;
typedef unsigned long long u64;
typedef u32 v4u __attribute__((ext_vector_type(4)));
typedef u32 v2u __attribute__((ext_vector_type(2)));
typedef short s16x4 __attribute__((ext_vector_type(4)));
typedef short bf16x8 __attribute__((ext_vector_type(8)));
typedef __bf16 bf16x2 __attribute__((ext_vector_type(2)));
typedef float f32x4 __attribute__((ext_vector_type(4)));
typedef float f32x16 __attribute__((ext_vector_type(16)));
#define GAS __attribute__((address_space(1)))
#define LAS __attribute__((address_space(3)))
#define DI __device__ __forceinline__
#define SB() __builtin_amdgcn_sched_barrier(0)

#define WIN0 32
#define WINA0 16
#define NA0 2
#define WINA 44
#define WINB 32
#define ORDER 0
#define QW 8
#define CODE_BYTES 24576

constexpr u32 WS_EPOCH = 0;
constexpr u32 WS_H1 = 1835008;
constexpr u32 WS_QABS = 65536;
constexpr u32 WS_CHK = 131072;
constexpr u32 WS_QF0 = 139264;
constexpr u32 WS_QB = 1048576 + 65536;
constexpr u32 WS_ASTAT = 262144;
constexpr u32 WS_H3B = 524288;
constexpr u32 WS_BIN = 786432;
constexpr u32 WS_H4 = 1310720;
constexpr u32 WS_APART = 2097152;
constexpr u32 WS_BYTES = 6291456;
constexpr u32 WS_STAMP = 8388608;

constexpr u32 L_C = 0, L_K = 0, L_V = 65536, L_WQ = 0, L_UK = 32768, L_WQB = 98304;
constexpr u32 L_Q = 131072;
constexpr u32 L_X = L_Q + 16384, L_QL = L_Q + 18432, L_QBL = L_Q + 22528, L_PB = L_Q + 24576;
constexpr u32 L_P = L_Q, L_SCR = L_Q + 8192, L_LSUM = L_Q + 28672;
constexpr u32 L_MAXX = L_Q + 31744, L_SYNC = L_MAXX + 512;
constexpr u32 L_W1 = 0, L_W2 = 32768, L_WGT = 65536, L_CK = 66048, L_B1 = 67584, L_B2 = 73728;
enum { S_PUB = 0, S_WQ, S_UK, S_WQB, S_QBISS, S_QISS, S_BAD, S_QGO, S_QST, S_LAND, S_MAX, S_P, S_PV, S_LGO, S_P2DONE, S_QBRDY, S_VLAND, S_N };

constexpr float KSC = 0.08838834764831845f * 1.4426950408889634f;
constexpr int SPIN_CAP = 1 << 15;

#define STAMP(i) do { } while (0)

DI void dma16(const void* sbase, u32 voff, u32 lds_dst) {
  u32 keep;
  asm volatile("s_mov_b32 %0, m0\n\ts_mov_b32 m0, %3\n\ts_nop 0\n\tglobal_load_lds_dwordx4 %1, %2 nt\n\ts_mov_b32 m0, %0"
               : "=&s"(keep) : "v"(voff), "s"(sbase), "s"(lds_dst) : "memory");
}
template <int W, u32 LDS> DI void dma4(const void* sb, u32 v0, u32 v1, u32 v2, u32 v3) {
  u32 keep;
  asm volatile("s_mov_b32 %0, m0\n\ts_mov_b32 m0, %6\n\ts_waitcnt vmcnt(%7)\n\t"
               "global_load_lds_dwordx4 %1, %5 nt\n\tglobal_load_lds_dwordx4 %2, %5 offset:1024 nt\n\ts_waitcnt vmcnt(%7)\n\t"
               "global_load_lds_dwordx4 %3, %5 offset:2048 nt\n\tglobal_load_lds_dwordx4 %4, %5 offset:3072 nt\n\ts_mov_b32 m0, %0"
               : "=&s"(keep) : "v"(v0), "v"(v1), "v"(v2), "v"(v3), "s"(sb), "i"(LDS), "i"(W) : "memory");
}
template <int I> struct IC { static constexpr int v = I; };
template <int B, int E, class F> DI void sfor(F&& f) { if constexpr (B < E) { f(IC<B>{}); sfor<B + 1, E>(f); } }
template <int N> DI void vmwait() { asm volatile("s_waitcnt vmcnt(%0)" :: "i"(N) : "memory"); }
DI u32 pk(float a, float b) { bf16x2 v = {(__bf16)a, (__bf16)b}; return __builtin_bit_cast(u32, v); }
DI float blo(u32 x) { return __builtin_bit_cast(float, x << 16); }
DI float bhi(u32 x) { return __builtin_bit_cast(float, x & 0xffff0000u); }
template <class T> DI LAS T* lp(u32 a) { return (LAS T*)(size_t)a; }
DI v4u lds128(u32 a) { return *lp<v4u>(a); }
DI void lds128w(u32 a, v4u v) { *lp<v4u>(a) = v; }
DI void lds64w(u32 a, u32 x, u32 y) { *lp<v2u>(a) = v2u{x, y}; }
DI v4u ldstr(u32 a0, u32 a1) {
  s16x4 lo = __builtin_amdgcn_ds_read_tr16_b64_v4i16(lp<s16x4>(a0));
  s16x4 hi = __builtin_amdgcn_ds_read_tr16_b64_v4i16(lp<s16x4>(a1));
  v2u l = __builtin_bit_cast(v2u, lo), h = __builtin_bit_cast(v2u, hi);
  return v4u{l.x, l.y, h.x, h.y};
}
DI f32x4 mfma16(v4u a, v4u b, f32x4 c) { return __builtin_amdgcn_mfma_f32_16x16x32_bf16(__builtin_bit_cast(bf16x8, a), __builtin_bit_cast(bf16x8, b), c, 0, 0, 0); }
DI f32x16 mfma32(v4u a, v4u b, f32x16 c) { return __builtin_amdgcn_mfma_f32_32x32x16_bf16(__builtin_bit_cast(bf16x8, a), __builtin_bit_cast(bf16x8, b), c, 0, 0, 0); }
template <int CTRL> DI u32 dppu(u32 x) { return (u32)__builtin_amdgcn_mov_dpp((int)x, CTRL, 0xf, 0xf, true); }
DI float f_(u32 x) { return __builtin_bit_cast(float, x); }
DI u32 u_(float x) { return __builtin_bit_cast(u32, x); }
template <int CTRL> DI float dpp(float x) { return f_(dppu<CTRL>(u_(x))); }
constexpr int X1 = 0xB1, X2 = 0x4E, X7 = 0x141, X8 = 0x128;
DI float sum32(float x) { auto t = __builtin_amdgcn_permlane32_swap(u_(x), u_(x), false, false); return f_(t[0]) + f_(t[1]); }
DI float max32(float x) { auto t = __builtin_amdgcn_permlane32_swap(u_(x), u_(x), false, false); return fmaxf(f_(t[0]), f_(t[1])); }
DI float sum16(float x) { auto t = __builtin_amdgcn_permlane16_swap(u_(x), u_(x), false, false); return f_(t[0]) + f_(t[1]); }
DI float max16(float x) { auto t = __builtin_amdgcn_permlane16_swap(u_(x), u_(x), false, false); return fmaxf(f_(t[0]), f_(t[1])); }
DI void sig(int word, int lane) {
  asm volatile("" ::: "memory");
  if (lane == 0) __hip_atomic_fetch_add(lp<u32>(L_SYNC + 4 * word), 1u, __ATOMIC_RELAXED, __HIP_MEMORY_SCOPE_WORKGROUP);
  asm volatile("" ::: "memory");
}
template <bool SLEEP> DI void waitw(int word, u32 n) {
  while ((u32)__builtin_amdgcn_readfirstlane(__hip_atomic_load(lp<u32>(L_SYNC + 4 * word), __ATOMIC_RELAXED, __HIP_MEMORY_SCOPE_WORKGROUP)) < n) {
    if (SLEEP) __builtin_amdgcn_s_sleep(2);
  }
  asm volatile("" ::: "memory");
}
DI void bar() { asm volatile("s_waitcnt lgkmcnt(0)\n\ts_barrier" ::: "memory"); }
#define LD1(off) __builtin_amdgcn_raw_buffer_load_b128(rs, (off), 0, 16)
#define ST1(v, off) __builtin_amdgcn_raw_buffer_store_b128((v), rs, (off), 0, 16)
#define TAGLOAD() __builtin_amdgcn_raw_buffer_load_b32(rs, WS_EPOCH, 0, 16)
DI v4u ldnt(const void* p, u32 off) { return __builtin_nontemporal_load((const GAS v4u*)((const GAS unsigned char*)p + off)); }
DI void stage_w(u32 base, const v4u (&park)[32], u32 g, u32 n) {
#pragma unroll
  for (int i = 0; i < 32; ++i) lds128w(base + g * 256 + i * 1024 + 16 * (n ^ ((g | (((i >> 1) & 1) << 2)) << 1)), park[i]);
}

__global__ void __launch_bounds__(512, 2)
fused_decode(const u16* __restrict__ xA, const u16* __restrict__ wqA, const u16* __restrict__ wuk, unsigned char* ws,
             const u16* __restrict__ ckv, const u16* __restrict__ kc, const u16* __restrict__ vc, const u16* __restrict__ xB,
             const u16* __restrict__ wqB, const u16* __restrict__ wuv, const u16* __restrict__ woA, const u16* __restrict__ woB,
             u16* __restrict__ out) {
  u64 pc0; asm volatile("s_getpc_b64 %0" : "=s"(pc0));
  const int tid = threadIdx.x, lane = tid & 63, wave = __builtin_amdgcn_readfirstlane(tid >> 6);
  STAMP(0);
  const u32 gid = blockIdx.x, x = gid & 7, r = gid >> 3;
  const bool isA = (r & 1) == 0;
  const u32 l16 = r >> 1;
  const u32 ht = 4 * x + (l16 >> 2), q4 = l16 & 3;
  const u32 g = lane >> 4, n = lane & 15, tq = n >> 2, tp = lane & 3;
  const __amdgpu_buffer_rsrc_t rs = __builtin_amdgcn_make_buffer_rsrc((void*)ws, 0, WS_BYTES, 0x00020000);
  u32 tag;

  if (wave == 4) {
    bar();
    tag = __builtin_amdgcn_readfirstlane(TAGLOAD()) + 1;
  } else if (wave == 5) {
    __builtin_amdgcn_s_setprio(1);
#define SIG_AT(ISSUED, W, NEED, WORD) if constexpr ((ISSUED) - (W) >= (NEED) && (ISSUED) - 4 - (W) < (NEED)) { vmwait<W>(); sig(WORD, lane); }
    const u32 rw = lane >> 3, sw = ((rw >> 1) & 1) << 1;
    const u32 ce = ((lane & 7) ^ sw) * 16, cd = ((lane & 7) ^ sw ^ 4) * 16 + 64512;
    if (isA) {
      dma16(xB, lane * 16, L_X);
      dma16(xB, lane * 16 + 1024, L_X + 1024);
      const u32 vb = (256 * (l16 >> 3) + rw) * 8192 + x * 1024 + (l16 & 7) * 128;
      sfor<0, 8>([&](auto I) {
        constexpr int G = I.v;
        dma4<WINA0, L_WQB + G * 4096>(wqB, vb + ce + G * 262144, vb + cd + G * 262144, vb + ce + G * 262144 + 129024, vb + cd + G * 262144 + 129024);
        if constexpr (G == 1) bar();
      });
      const unsigned char* src = (const unsigned char*)ckv + (size_t)(x * 16 + l16) * 131072;
      u32 cv[16];
#pragma unroll
      for (int i = 0; i < 16; ++i) cv[i] = (lane ^ (((i & 3) << 2) | (i >> 2))) * 16;
      sfor<0, 32>([&](auto I) {
        constexpr int G = I.v;
        if constexpr (G == 24) waitw<true>(S_P2DONE, 4);
        dma4<(G < NA0 ? WINA0 : WINA), L_C + G * 4096>(src + G * 4096, cv[(4 * G) & 15], cv[(4 * G + 1) & 15], cv[(4 * G + 2) & 15], cv[(4 * G + 3) & 15]);
        if constexpr (G + 1 == NA0) { vmwait<4 * NA0>(); sig(S_WQB, lane); }
      });
      STAMP(1);
      if constexpr (ORDER == 0) { vmwait<QW>(); sig(S_QGO, lane); vmwait<0>(); sig(S_LAND, lane); STAMP(2); waitw<false>(S_QISS, 4); }
      v4u park[32];
      const u32 vo = (128 * q4 + g) * 8192 + ht * 256 + n * 16;
      sfor<0, 8>([&](auto I) {
        constexpr int G = I.v;
        vmwait<WINA>(); SB();
        park[4 * G] = ldnt(wuv, vo + (4 * G) * 32768); park[4 * G + 1] = ldnt(wuv, vo + (4 * G + 1) * 32768);
        SB(); vmwait<WINA>(); SB();
        park[4 * G + 2] = ldnt(wuv, vo + (4 * G + 2) * 32768); park[4 * G + 3] = ldnt(wuv, vo + (4 * G + 3) * 32768);
        SB();
      });
      if constexpr (ORDER != 1) sig(S_LGO, lane);
      STAMP(3);
      if constexpr (ORDER != 0) { vmwait<32>(); sig(S_LAND, lane); STAMP(2); }
      if constexpr (ORDER == 1) { vmwait<QW>(); sig(S_QGO, lane); }
      waitw<true>(S_PV, 4);
      const u32 tv = TAGLOAD();
      stage_w(L_W1, park, g, n);
      tag = __builtin_amdgcn_readfirstlane(tv) + 1;
    } else {
      dma16(xA, lane * 16, L_X);
      dma16(xA, lane * 16 + 1024, L_X + 1024);
      const u32 va = (256 * (q4 >> 1) + rw) * 8192 + ht * 256 + (q4 & 1) * 128;
      sfor<0, 8>([&](auto I) {
        constexpr int G = I.v;
        dma4<63, L_WQ + G * 4096>(wqA, va + ce + G * 262144, va + cd + G * 262144, va + ce + G * 262144 + 129024, va + cd + G * 262144 + 129024);
      });
      STAMP(15);
      bar();
      const u32 uo = (128 * q4 + g) * 8192 + ht * 256;
      sfor<0, 8>([&](auto I) {
        constexpr int G = I.v;
        const u32 u0 = uo + G * 131072 + ((n ^ (g | (((2 * G) & 3) << 2))) * 16), u1 = uo + G * 131072 + ((n ^ (g | (((2 * G + 1) & 3) << 2))) * 16);
        dma4<WIN0, L_UK + G * 4096>(wuk, u0, u0 + 31744, u1 + 63488, u1 + 95232);
        SIG_AT(38 + 4 * G, WIN0, 34, S_WQ)
      });
      STAMP(14);
      vmwait<0>(); sig(S_UK, lane);
      tag = __builtin_amdgcn_readfirstlane(TAGLOAD()) + 1;
      waitw<false>(S_QBISS, 1);
      STAMP(13);
      const size_t base = (((size_t)(l16 >> 3) * 8 + x) * 8192 + 6144 + (l16 & 7) * 256) * 256;
      const unsigned char* ks = (const unsigned char*)kc + base;
      const unsigned char* vs = (const unsigned char*)vc + base;
      {
        u32 kv[4];
#pragma unroll
        for (int i = 0; i < 4; ++i) kv[i] = g * 256 + ((n ^ (4 * i + g)) * 16);
        sfor<0, 16>([&](auto I) { constexpr int G = I.v; dma4<WINB, L_K + G * 4096>(ks + G * 4096, kv[0], kv[1], kv[2], kv[3]); });
      }
      STAMP(1);
      {
        const u32 v0 = g * 256 + ((n ^ (g << 1)) * 16), v1 = g * 256 + ((n ^ ((4 + g) << 1)) * 16);
        sfor<0, 16>([&](auto I) {
          constexpr int G = I.v;
          dma4<WINB, L_V + G * 4096>(vs + G * 4096, v0, v1, v0, v1);
          SIG_AT(4 + 4 * G, WINB, 0, S_LAND)
          SIG_AT(4 + 4 * G, WINB, 16, S_VLAND)
          SIG_AT(4 + 4 * G, WINB, 32, S_VLAND)
          SIG_AT(4 + 4 * G, WINB, 48, S_VLAND)
        });
      }
      STAMP(2);
      waitw<false>(S_P, 4);
      {
        const u32 vo = (ht * 128 + g) * 1024 + q4 * 256, w0 = vo + ((n ^ (g << 1)) * 16), w1 = vo + ((n ^ ((g | 4) << 1)) * 16) + 6144;
        sfor<0, 8>([&](auto I) {
          constexpr int G = I.v;
          dma4<WINB, L_W1 + G * 4096>(woB, w0 + G * 16384, w0 + G * 16384 + 3072, w1 + G * 16384, w1 + G * 16384 + 3072);
          SIG_AT(68 + 4 * G, WINB, 48, S_VLAND)
          SIG_AT(68 + 4 * G, WINB, 64, S_VLAND)
        });
      }
      STAMP(3);
      vmwait<0>();
    }
    STAMP(4);
  } else if (wave == 6) {
    bar();
    const u32 tv = TAGLOAD();
    if (isA) {
      __builtin_amdgcn_s_setprio(1);
      v4u park[32];
      if constexpr (ORDER == 1) waitw<true>(S_QISS, 4); else waitw<true>(S_LGO, 1);
      const u32 vo = (ht * 128 + g) * 1024 + q4 * 256 + n * 16;
#pragma unroll
      for (int i = 0; i < 32; i += 2) {
        park[i] = ldnt(woA, vo + i * 4096); park[i + 1] = ldnt(woA, vo + (i + 1) * 4096);
        SB();
      }
      if constexpr (ORDER == 2) { vmwait<QW>(); sig(S_QGO, lane); }
      STAMP(3);
      waitw<true>(S_PV, 4);
      tag = __builtin_amdgcn_readfirstlane(tv) + 1;
      stage_w(L_W2, park, g, n);
      STAMP(4);
    } else {
      __builtin_amdgcn_s_setprio(2);
      tag = __builtin_amdgcn_readfirstlane(tv) + 1;
      waitw<false>(S_PUB, 4);
      STAMP(2);
      v4u e[8];
      for (int spin = 0; spin < SPIN_CAP; ++spin) {
        bool ok = true;
#pragma unroll
        for (int i = 0; i < 8; ++i) e[i] = LD1(WS_QB + x * 16384 + (l16 >> 3) * 8192 + i * 1024 + lane * 16);
        SB();
        if (spin == 0) sig(S_QBISS, lane);
        asm volatile("" ::: "memory");
#pragma unroll
        for (int i = 0; i < 8; ++i) ok = ok && e[i].y == tag && e[i].w == tag;
        if (__all(ok)) break;
      }
#pragma unroll
      for (int i = 0; i < 4; ++i) *lp<u32>(L_QBL + i * 256 + lane * 4) = pk(f_(e[i].x) + f_(e[4 + i].x), f_(e[i].z) + f_(e[4 + i].z));
      sig(S_QBRDY, lane);
      STAMP(3);
      __builtin_amdgcn_s_setprio(0);
    }
  } else if (wave == 7) {
    bar();
    const u32 tv = TAGLOAD();
    if (r < 2) {
      const GAS u32* c = (const GAS u32*)(pc0 & ~127ull);
      u32 s = 0;
#pragma unroll
      for (int i = 0; i < (CODE_BYTES / 128 + 127) / 128; ++i) {
        const u32 ln = (2 * i + r) * 64 + lane;
        if (ln < CODE_BYTES / 128) s += c[ln * 32];
      }
      asm volatile("" :: "v"(s));
    }
    if (lane < 6) { u32 w = __builtin_amdgcn_raw_buffer_load_b32(rs, lane * 1048576 + 1048576 - 64, 0, 16); asm volatile("" :: "v"(w)); }
    tag = __builtin_amdgcn_readfirstlane(tv) + 1;
  } else {
    if (wave == 0 && lane < S_N) *lp<u32>(L_SYNC + 4 * lane) = 0;
    bar();
    tag = __builtin_amdgcn_readfirstlane(TAGLOAD()) + 1;
    const u32 w = wave;
    const u32 a0 = (8 * g + tq) * 128 + 16 * ((2 * w + (tp >> 1)) ^ ((((tq >> 1) & 1) | ((g & 1) << 1)) << 1)) + 8 * (tp & 1);
    if (isA) {
      const u32 bs = x * 16 + l16, b = bs >> 6, hh = lane >> 5, hd = lane & 31;
      __builtin_amdgcn_s_setprio(2);
      {
        waitw<false>(S_WQB, 1);
        v4u xf[8], af[8];
#pragma unroll
        for (int s = 0; s < 8; ++s) { af[s] = ldstr(L_WQB + a0 + s * 4096, L_WQB + a0 + s * 4096 + 512); xf[s] = lds128(L_X + (lane & 1) * 1024 + (l16 >> 3) * 512 + g * 16 + s * 64); }
        sig(S_P2DONE, lane);
        SB();
        f32x4 c0 = {0.f, 0.f, 0.f, 0.f}, c1 = c0;
#pragma unroll
        for (int s = 0; s < 8; s += 2) { c0 = mfma16(af[s], xf[s], c0); c1 = mfma16(af[s + 1], xf[s + 1], c1); }
        c0 += c1;
        if (n < 4) ST1((v4u{u_((n & 2) ? c0[2] : c0[0]), tag, u_((n & 2) ? c0[3] : c0[1]), tag}),
                       WS_QB + x * 16384 + (n & 1) * 8192 + l16 * 512 + (8 * w + 2 * g + (n >> 1)) * 16);
        STAMP(13);
      }
      waitw<true>(S_QGO, 1);
      STAMP(14);
      v4u qr[8], z0, z1;
      for (int spin = 0; spin < SPIN_CAP; ++spin) {
#pragma unroll
        for (int i = 0; i < 8; ++i) qr[i] = LD1(WS_QABS + b * 32768 + (8 * w + i) * 1024 + lane * 16);
        const u32 co = WS_CHK + (8 * w + 4 * ((lane >> 1) & 1) + 2 * (lane & 1)) * 128 + (lane >> 2) * 8 + b * 4;
        const u32 ck0 = __builtin_amdgcn_raw_buffer_load_b32(rs, co, 0, 16), ck1 = __builtin_amdgcn_raw_buffer_load_b32(rs, co + 128, 0, 16);
        z0 = LD1(WS_QF0 + b * 2048 + hd * 64 + hh * 32);
        z1 = LD1(WS_QF0 + b * 2048 + hd * 64 + hh * 32 + 16);
        SB();
        if (spin == 0) sig(S_QISS, lane);
        asm volatile("" ::: "memory");
        u32 c8[8];
#pragma unroll
        for (int i = 0; i < 8; ++i) c8[i] = (qr[i].x + qr[i].y) + (qr[i].z + qr[i].w);
        u32 c4[4], c2[2];
#pragma unroll
        for (int i = 0; i < 4; ++i) { const u32 p = c8[i] + dppu<X2>(c8[i]), q = c8[i + 4] + dppu<X2>(c8[i + 4]); c4[i] = (lane & 2) ? q : p; }
#pragma unroll
        for (int i = 0; i < 2; ++i) { const u32 p = c4[i] + dppu<X1>(c4[i]), q = c4[i + 2] + dppu<X1>(c4[i + 2]); c2[i] = (lane & 1) ? q : p; }
        if (__all(c2[0] + tag == ck0 && c2[1] + tag == ck1 && z0.y == tag && z0.w == tag && z1.y == tag && z1.w == tag)) break;
      }
      STAMP(1);
      if (lane >= 2) {
#pragma unroll
        for (int i = 0; i < 8; ++i)
          lds128w(L_Q - 1024 + (lane >> 1) * 1024 + ((((lane & 1) * 32 + 8 * w + i) ^ (lane & 7)) * 16), qr[i]);
      }
      sig(S_QST, lane);
      STAMP(2);
      waitw<false>(S_QST, 4);
      waitw<false>(S_LAND, 1);
      STAMP(3);
      f32x16 s;
#pragma unroll
      for (int i = 0; i < 16; ++i) s[i] = 0.f;
      {
        const u32 fr = ((hd & 3) << 2) | ((hd >> 2) & 3);
        const u32 ca = L_C + (32 * w + hd) * 1024 + 16 * (hh ^ fr), qa = L_Q - 1024 + ((lane ^ hh) * 16);
        v4u af[32], bf[32];
        bf[0] = v4u{z0.x, z0.z, z1.x, z1.z};
        constexpr int PD = 6;
#define S_RD(k) do { af[k] = lds128((ca ^ (32 * ((k) & 7))) + 256 * ((k) >> 3)); if ((k) > 0) bf[k] = lds128((qa ^ (32 * ((k) & 3))) + (k) * 1024); } while (0)
#pragma unroll
        for (int k = 0; k < PD; ++k) S_RD(k);
        SB();
#pragma unroll
        for (int k = 0; k < 32; ++k) {
          if (k + PD < 32) S_RD(k + PD);
          s = mfma32(af[k], bf[k], s);
          SB();
        }
      }
      float m = s[0];
#pragma unroll
      for (int i = 1; i < 16; ++i) m = fmaxf(m, s[i]);
      m = max32(m);
      if (hh == 0) *lp<float>(L_MAXX + hd * 16 + w * 4) = m;
      sig(S_MAX, lane);
      STAMP(4);
      waitw<false>(S_MAX, 4);
      const v4u mm = lds128(L_MAXX + hd * 16);
      const float mk = fmaxf(fmaxf(f_(mm.x), f_(mm.y)), fmaxf(f_(mm.z), f_(mm.w))) * KSC;
      float ls = 0.f;
#pragma unroll
      for (int i = 0; i < 16; ++i) { s[i] = __builtin_amdgcn_exp2f(fmaf(s[i], KSC, -mk)); ls += s[i]; }
      lds128w(L_P + (2 * w) * 1024 + lane * 16, v4u{pk(s[0], s[1]), pk(s[2], s[3]), pk(s[4], s[5]), pk(s[6], s[7])});
      lds128w(L_P + (2 * w + 1) * 1024 + lane * 16, v4u{pk(s[8], s[9]), pk(s[10], s[11]), pk(s[12], s[13]), pk(s[14], s[15])});
      ls = sum32(ls);
      if (hh == 0) *lp<float>(L_LSUM + hd * 16 + w * 4) = ls;
      sig(S_P, lane);
      const u32 blk = (lane >> 4) & 1;
      u32 ta[2];
#pragma unroll
      for (int t = 0; t < 2; ++t)
        ta[t] = L_C + (4 * hh + 8 * t + tq) * 1024 + 256 * w + 16 * ((2 * blk + (tp >> 1)) ^ ((tq << 2) | (hh + 2 * t))) + 8 * (tp & 1);
      v4u cf[32];
      constexpr int PV_PD = 5;
#define PV_RD(kk) cf[kk] = ldstr((ta[0] ^ (64 * ((kk) >> 3))) + ((kk) & 7) * 16384, (ta[1] ^ (64 * ((kk) >> 3))) + ((kk) & 7) * 16384)
#pragma unroll
      for (int k = 0; k < PV_PD; ++k) PV_RD(k);
      SB();
      waitw<false>(S_P, 4);
      STAMP(5);
      v4u pf[8];
#pragma unroll
      for (int k = 0; k < 8; ++k) pf[k] = lds128(L_P + k * 1024 + lane * 16);
      const v4u lv = lds128(L_LSUM + hd * 16);
      SB();
      const u32 scw = L_SCR + w * 4096 + hd * 64 + 8 * hh, scx = 16 * ((hd >> 1) & 3);
      const u32 scr = L_SCR + w * 4096 + (lane >> 2) * 64 + 16 * (tp ^ ((lane >> 3) & 3));
      const u32 sto = WS_APART + (((lane >> 2) * 4 + w) * 128 + bs) * 256 + 16 * tp;
      f32x16 o[4];
      v4u ov[2];
      u32 cs = tag;
#define EPI_W(c) _Pragma("unroll") for (int gg = 0; gg < 4; ++gg) { \
        const u32 p0 = pk(o[c][4 * gg], o[c][4 * gg + 1]), p1 = pk(o[c][4 * gg + 2], o[c][4 * gg + 3]); \
        lds64w(scw + ((c) & 1) * 2048 + ((16 * gg) ^ scx), p0, p1); cs += p0 + p1; }
#define EPI_R(c) do { ov[0] = lds128(scr + ((c) & 1) * 2048); ov[1] = lds128(scr + ((c) & 1) * 2048 + 1024); } while (0)
#define EPI_S(c) do { ST1(ov[0], sto + 64 * (c)); ST1(ov[1], sto + 64 * (c) + 16 * 4 * 128 * 256); } while (0)
#pragma unroll
      for (int kk = 0; kk < 32; ++kk) {
        const int c = kk >> 3, k = kk & 7;
        if (kk + PV_PD < 32) PV_RD(kk + PV_PD);
        if (k == 0) {
          f32x16 z;
#pragma unroll
          for (int i = 0; i < 16; ++i) z[i] = 0.f;
          o[c] = mfma32(cf[kk], pf[k], z);
        } else o[c] = mfma32(cf[kk], pf[k], o[c]);
        if (c > 0 && k == 1) { EPI_W(c - 1) EPI_R(c - 1); }
        if (c > 0 && k == 5) { EPI_S(c - 1); }
        SB();
      }
      sig(S_PV, lane);
      EPI_W(3) EPI_R(3);
      const float lsum = (f_(lv.x) + f_(lv.y)) + (f_(lv.z) + f_(lv.w));
      { auto t = __builtin_amdgcn_permlane32_swap(cs, cs, false, false); cs = t[0] + t[1] - tag + u_(mk) + u_(lsum); }
      EPI_S(3);
      if (hh == 0) ST1((v4u{u_(mk), u_(lsum), cs, tag}), WS_ASTAT + ((hd * 4 + w) * 128 + bs) * 16);
      STAMP(7);
    } else {
      __builtin_amdgcn_s_setprio(2);
      {
        waitw<false>(S_WQ, 1);
        STAMP(10);
        v4u xf[8], af[8];
#pragma unroll
        for (int s = 0; s < 8; ++s) { af[s] = ldstr(L_WQ + a0 + s * 4096, L_WQ + a0 + s * 4096 + 512); xf[s] = lds128(L_X + (lane & 1) * 1024 + (q4 >> 1) * 512 + g * 16 + s * 64); }
        SB();
        f32x4 c0 = {0.f, 0.f, 0.f, 0.f}, c1 = c0;
#pragma unroll
        for (int s = 0; s < 8; s += 2) { c0 = mfma16(af[s], xf[s], c0); c1 = mfma16(af[s + 1], xf[s + 1], c1); }
        c0 += c1;
        if (n < 4) ST1((v4u{u_((n & 2) ? c0[2] : c0[0]), tag, u_((n & 2) ? c0[3] : c0[1]), tag}),
                       WS_H1 + ht * 4096 + q4 * 1024 + (n & 1) * 512 + (8 * w + 2 * g + (n >> 1)) * 16);
        STAMP(11);
        waitw<false>(S_UK, 1);
        v4u uk[8];
#pragma unroll
        for (int t = 0; t < 2; ++t)
#pragma unroll
          for (int s = 0; s < 4; ++s) uk[t * 4 + s] = lds128(L_UK + (32 * w + 8 * (n >> 2) + 4 * t + (n & 3)) * 256 + 16 * ((4 * s + g) ^ n));
        v4u e[4];
        for (int spin = 0; spin < SPIN_CAP; ++spin) {
          bool ok = true;
#pragma unroll
          for (int i = 0; i < 4; ++i) e[i] = LD1(WS_H1 + ht * 4096 + i * 1024 + lane * 16);
          asm volatile("" ::: "memory");
#pragma unroll
          for (int i = 0; i < 4; ++i) ok = ok && e[i].y == tag && e[i].w == tag;
          if (__all(ok)) break;
        }
        STAMP(14);
        const u32 ql = L_QL + w * 512;
#pragma unroll
        for (int d = 0; d < 2; ++d)
          *lp<u32>(ql + (lane >> 5) * 256 + d * 128 + (lane & 31) * 4) = pk(f_(e[d].x) + f_(e[2 + d].x), f_(e[d].z) + f_(e[2 + d].z));
        v4u qb[4];
#pragma unroll
        for (int s = 0; s < 4; ++s) qb[s] = lds128(ql + (lane & 1) * 256 + g * 16 + s * 64);
        SB();
        f32x4 u0 = {0.f, 0.f, 0.f, 0.f}, u1 = u0;
#pragma unroll
        for (int s = 0; s < 4; ++s) { u0 = mfma16(uk[s], qb[s], u0); u1 = mfma16(uk[4 + s], qb[s], u1); }
        const v4u pv = {pk(u0[0], u0[1]), pk(u0[2], u0[3]), pk(u1[0], u1[1]), pk(u1[2], u1[3])};
        if (n < 2) ST1(pv, WS_QABS + n * 32768 + ht * 1024 + q4 * 256 + w * 64 + g * 16);
        if (q4 == 0 && w == 0 && n < 2 && g < 2) {
          ST1((v4u{pv.x, tag, pv.y, tag}), WS_QF0 + n * 2048 + ht * 64 + g * 32);
          ST1((v4u{pv.z, tag, pv.w, tag}), WS_QF0 + n * 2048 + ht * 64 + g * 32 + 16);
        }
        u32 cs = (pv.x + pv.y) + (pv.z + pv.w);
        { auto t = __builtin_amdgcn_permlane16_swap(cs, cs, false, false); cs = t[0] + t[1]; }
        { auto t = __builtin_amdgcn_permlane32_swap(cs, cs, false, false); cs = t[0] + t[1]; }
        cs += tag;
        const u32 cs1 = dppu<X1>(cs);
        if (lane == 0) __builtin_amdgcn_raw_buffer_store_b64(v2u{cs, cs1}, rs, WS_CHK + ht * 128 + (4 * q4 + w) * 8, 0, 16);
        sig(S_PUB, lane);
        STAMP(15);
      }
      waitw<true>(S_QBRDY, 1);
      v4u qf[4];
#pragma unroll
      for (int s = 0; s < 4; ++s) qf[s] = lds128(L_QBL + (lane & 3) * 256 + g * 16 + s * 64);
      waitw<true>(S_LAND, 1);
      STAMP(3);
      f32x4 sc[4];
      {
        v4u kf[16];
        const u32 ka = L_K + (64 * w + n) * 256 + 16 * (g ^ n);
#pragma unroll
        for (int i = 0; i < 16; ++i) kf[i] = lds128((ka ^ (64 * (i & 3))) + (i >> 2) * 4096);
        SB();
#pragma unroll
        for (int t = 0; t < 4; ++t) {
          sc[t] = f32x4{0.f, 0.f, 0.f, 0.f};
#pragma unroll
          for (int s = 0; s < 4; ++s) sc[t] = mfma16(kf[4 * t + s], qf[s], sc[t]);
        }
      }
      float m = sc[0][0];
#pragma unroll
      for (int i = 1; i < 16; ++i) m = fmaxf(m, sc[i >> 2][i & 3]);
      m = max32(max16(m));
      if (lane < 4) *lp<float>(L_MAXX + lane * 16 + w * 4) = m;
      sig(S_MAX, lane);
      waitw<false>(S_MAX, 4);
      const v4u mm = lds128(L_MAXX + (lane & 3) * 16);
      const float mk = fmaxf(fmaxf(f_(mm.x), f_(mm.y)), fmaxf(f_(mm.z), f_(mm.w))) * KSC;
      float ls = 0.f;
#pragma unroll
      for (int i = 0; i < 16; ++i) { const float p = __builtin_amdgcn_exp2f(fmaf(sc[i >> 2][i & 3], KSC, -mk)); sc[i >> 2][i & 3] = p; ls += p; }
      ls = sum32(sum16(ls));
      if (n < 4) {
#pragma unroll
        for (int u = 0; u < 2; ++u)
          lds128w(L_PB + (2 * w + u) * 256 + g * 64 + n * 16,
                  v4u{pk(sc[2 * u][0], sc[2 * u][1]), pk(sc[2 * u][2], sc[2 * u][3]), pk(sc[2 * u + 1][0], sc[2 * u + 1][1]), pk(sc[2 * u + 1][2], sc[2 * u + 1][3])});
      }
      if (lane < 4) *lp<float>(L_LSUM + lane * 16 + w * 4) = ls;
      sig(S_P, lane);
      waitw<false>(S_P, 4);
      STAMP(5);
      v4u pf[8];
#pragma unroll
      for (int k = 0; k < 8; ++k) pf[k] = lds128(L_PB + k * 256 + g * 64 + (lane & 3) * 16);
      if (w == 0) {
        const v4u lv = lds128(L_LSUM + (lane & 3) * 16);
        const float lsum = (f_(lv.x) + f_(lv.y)) + (f_(lv.z) + f_(lv.w));
        if (lane < 4) ST1((v4u{u_(mk), tag, u_(lsum), tag}), WS_BIN + (4 * x + lane) * 9216 + 8192 + l16 * 16);
      }
      f32x4 o0 = {0.f, 0.f, 0.f, 0.f}, o1 = o0;
      const u32 va = L_V + (4 * g + tq) * 256 + 16 * ((4 * w + (tp >> 1)) ^ ((4 * (g & 1) + tq) << 1)) + 8 * (tp & 1);
#pragma unroll
      for (int k2 = 0; k2 < 4; ++k2) {
        waitw<false>(S_VLAND, k2 + 1);
        v4u vf[4];
#pragma unroll
        for (int i = 0; i < 4; ++i) { const u32 a = (va ^ (32 * (i & 1))) + (2 * k2 + (i >> 1)) * 8192; vf[i] = ldstr(a, a + 4096); }
        SB();
        o0 = mfma16(vf[0], pf[2 * k2], o0); o1 = mfma16(vf[1], pf[2 * k2], o1);
        o0 = mfma16(vf[2], pf[2 * k2 + 1], o0); o1 = mfma16(vf[3], pf[2 * k2 + 1], o1);
      }
      STAMP(6);
      const f32x4 o = (n & 4) ? o1 : o0;
      if (n < 8) ST1((v4u{pk(o[0], o[1]), tag, pk(o[2], o[3]), tag}), WS_BIN + (4 * x + (n & 3)) * 9216 + l16 * 512 + (4 * (2 * w + (n >> 2)) + g) * 16);
      STAMP(7);
    }
  }

  const u32 v = wave;
  const u32 ab = (8 * g + tq) * 256 + 16 * ((2 * v + (tp >> 1)) ^ ((tq | ((g & 1) << 2)) << 1)) + 8 * (tp & 1);
  if (isA) {
    v4u a1[4], a2[4], bq[4];
    if (wave == 7) waitw<true>(S_PV, 4);
    for (u32 it = 1; it < 64; ++it) {
      v4u st0, st1;
      if (wave == 7) {
        for (int spin = 0; spin < SPIN_CAP; ++spin) {
          st0 = LD1(WS_ASTAT + (ht * 4 + q4) * 2048 + lane * 16);
          st1 = LD1(WS_ASTAT + (ht * 4 + q4) * 2048 + 1024 + lane * 16);
          asm volatile("" ::: "memory");
          if (__all(st0.w == tag && st1.w == tag)) break;
        }
        STAMP(2);
      }
      bar();
      STAMP(8);
      v4u pd[4];
#pragma unroll
      for (int i = 0; i < 4; ++i) pd[i] = LD1(WS_APART + (ht * 4 + q4) * 32768 + v * 4096 + i * 1024 + lane * 16);
      SB();
      if (wave == 7) {
#pragma unroll
        for (int i = 0; i < 2; ++i) {
          const v4u st = i ? st1 : st0;
          float M = f_(st.x);
          M = fmaxf(M, dpp<X1>(M)); M = fmaxf(M, dpp<X2>(M)); M = fmaxf(M, dpp<X7>(M)); M = fmaxf(M, dpp<X8>(M)); M = max16(M); M = max32(M);
          const float e = __builtin_amdgcn_exp2f(f_(st.x) - M);
          float S = e * f_(st.y);
          S += dpp<X1>(S); S += dpp<X2>(S); S += dpp<X7>(S); S += dpp<X8>(S); S = sum16(S); S = sum32(S);
          *lp<float>(L_WGT + i * 256 + lane * 4) = e / S;
          *lp<u32>(L_CK + i * 256 + lane * 4) = st.z - tag - st.x - st.y;
        }
      }
#pragma unroll
      for (int s = 0; s < 4; ++s) { a1[s] = ldstr(L_W1 + ab + s * 8192, L_W1 + ab + s * 8192 + 1024); a2[s] = ldstr(L_W2 + ab + s * 8192, L_W2 + ab + s * 8192 + 1024); }
      bar();
      float acc[8], wt[4];
#pragma unroll
      for (int i = 0; i < 4; ++i) wt[i] = *lp<float>(L_WGT + (v * 16 + 4 * i + g) * 4);
      const u32 ck = *lp<u32>(L_CK + (v * 16 + 4 * (lane & 3) + g) * 4);
#pragma unroll
      for (int k = 0; k < 4; ++k) { acc[2 * k] = wt[0] * blo(pd[0][k]); acc[2 * k + 1] = wt[0] * bhi(pd[0][k]); }
#pragma unroll
      for (int i = 1; i < 4; ++i)
#pragma unroll
        for (int k = 0; k < 4; ++k) { acc[2 * k] = fmaf(wt[i], blo(pd[i][k]), acc[2 * k]); acc[2 * k + 1] = fmaf(wt[i], bhi(pd[i][k]), acc[2 * k + 1]); }
      STAMP(9);
      {
        float y[4];
#pragma unroll
        for (int k = 0; k < 4; ++k) { auto t = __builtin_amdgcn_permlane32_swap(u_(acc[k]), u_(acc[k + 4]), false, false); y[k] = f_(t[0]) + f_(t[1]); }
        float z[2];
#pragma unroll
        for (int k = 0; k < 2; ++k) { auto t = __builtin_amdgcn_permlane16_swap(u_(y[k]), u_(y[k + 2]), false, false); z[k] = f_(t[0]) + f_(t[1]); }
        const u32 hi = pk(z[0], z[1]), lo = pk(z[0] - blo(hi), z[1] - bhi(hi));
        *lp<u32>(L_B1 + v * 272 + (8 * n + 2 * g) * 2) = hi;
        *lp<u32>(L_B1 + (8 + v) * 272 + (8 * n + 2 * g) * 2) = lo;
        u32 c4[4], c2[2];
#pragma unroll
        for (int i = 0; i < 4; ++i) c4[i] = (pd[i].x + pd[i].y) + (pd[i].z + pd[i].w);
#pragma unroll
        for (int i = 0; i < 2; ++i) { const u32 p = c4[i] + dppu<X2>(c4[i]), q = c4[i + 2] + dppu<X2>(c4[i + 2]); c2[i] = (lane & 2) ? q : p; }
        const u32 p1 = c2[0] + dppu<X1>(c2[0]), q1 = c2[1] + dppu<X1>(c2[1]);
        u32 c1 = (lane & 1) ? q1 : p1;
        c1 += dppu<X8>(c1);
        c1 += dppu<0x124>(c1);
        if (c1 != ck) *lp<u32>(L_SYNC + 4 * S_BAD) = it;
      }
      bar();
      const u32 bad = *lp<u32>(L_SYNC + 4 * S_BAD);
#pragma unroll
      for (int s = 0; s < 4; ++s) bq[s] = lds128(L_B1 + n * 272 + g * 16 + s * 64);
      SB();
      if (__builtin_amdgcn_readfirstlane(bad) != it) break;
    }
    {
      f32x4 d = {0.f, 0.f, 0.f, 0.f};
#pragma unroll
      for (int s = 0; s < 4; ++s) d = mfma16(a1[s], bq[s], d);
#pragma unroll
      for (int k = 0; k < 4; ++k) { float t = d[k]; t += dpp<X1>(t); t += dpp<X2>(t); t += dpp<X8>(t); d[k] = t; }
      if ((n & 11) == 0) ST1((v4u{pk(d[0], d[1]), tag, pk(d[2], d[3]), tag}), WS_H3B + ht * 4096 + q4 * 1024 + (n >> 2) * 512 + (4 * v + g) * 16);
    }
    STAMP(10);
    if (wave < 4) {
      v4u e;
      for (int spin = 0; spin < SPIN_CAP; ++spin) {
        e = LD1(WS_H3B + ht * 4096 + v * 1024 + lane * 16);
        asm volatile("" ::: "memory");
        if (__all(e.y == tag && e.w == tag)) break;
      }
      lds64w(L_B2 + ((lane >> 5) * 4 + v) * 272 + (lane & 31) * 8, e.x, e.z);
    }
    bar();
    STAMP(11);
    {
#pragma unroll
      for (int s = 0; s < 4; ++s) bq[s] = lds128(L_B2 + (n & 7) * 272 + g * 16 + s * 64);
      SB();
      f32x4 d = {0.f, 0.f, 0.f, 0.f};
#pragma unroll
      for (int s = 0; s < 4; ++s) d = mfma16(a2[s], bq[s], d);
#pragma unroll
      for (int k = 0; k < 4; ++k) { float t = d[k]; t += dpp<X1>(t); t += dpp<X2>(t); d[k] = t; }
      const bool k1 = (n & 1) != 0;
      if ((n & 10) == 0) ST1((v4u{u_(k1 ? d[2] : d[0]), tag, u_(k1 ? d[3] : d[1]), tag}),
                             WS_H4 + (((q4 * 8 + v) * 2 + (n >> 2)) * 32 + ht) * 128 + g * 32 + (n & 1) * 16);
    }
    STAMP(12);
  } else {
    bar();
    STAMP(8);
    v4u a1[4];
#pragma unroll
    for (int s = 0; s < 4; ++s) a1[s] = ldstr(L_W1 + ab + s * 8192, L_W1 + ab + s * 8192 + 1024);
    v4u e, st;
    for (int spin = 0; spin < SPIN_CAP; ++spin) {
      e = LD1(WS_BIN + ht * 9216 + v * 1024 + lane * 16);
      st = LD1(WS_BIN + ht * 9216 + 8192 + n * 16);
      asm volatile("" ::: "memory");
      if (__all(e.y == tag && e.w == tag && st.y == tag && st.w == tag)) break;
    }
    STAMP(9);
    lds64w(L_B1 + (2 * v + (lane >> 5)) * 272 + (lane & 31) * 8, e.x, e.z);
    float M = f_(st.x);
    M = fmaxf(M, dpp<X1>(M)); M = fmaxf(M, dpp<X2>(M)); M = fmaxf(M, dpp<X7>(M));
    const float ee = __builtin_amdgcn_exp2f(f_(st.x) - M);
    float S = ee * f_(st.z);
    S += dpp<X1>(S); S += dpp<X2>(S); S += dpp<X7>(S);
    const float wt = ee / S;
    bar();
    {
      v4u bq[4];
#pragma unroll
      for (int s = 0; s < 4; ++s) bq[s] = lds128(L_B1 + n * 272 + g * 16 + s * 64);
      SB();
      f32x4 d = {0.f, 0.f, 0.f, 0.f};
#pragma unroll
      for (int s = 0; s < 4; ++s) d = mfma16(a1[s], bq[s], d);
#pragma unroll
      for (int k = 0; k < 4; ++k) { float t = d[k] * wt; t += dpp<X1>(t); t += dpp<X2>(t); t += dpp<X7>(t); d[k] = t; }
      const bool k1 = (n & 1) != 0;
      if ((n & 6) == 0) ST1((v4u{u_(k1 ? d[2] : d[0]), tag, u_(k1 ? d[3] : d[1]), tag}),
                            WS_H4 + 262144 + (((q4 * 8 + v) * 2 + (n >> 3)) * 32 + ht) * 128 + g * 32 + (n & 1) * 16);
    }
    STAMP(12);
  }
  const u32 rho = x * 8 + l16;
  if (wave == 0 && l16 < 8) {
    const u32 part = isA ? 0u : 1u;
    v4u e[4];
    for (int spin = 0; spin < SPIN_CAP; ++spin) {
      bool ok = true;
#pragma unroll
      for (int i = 0; i < 4; ++i) e[i] = LD1(WS_H4 + part * 262144 + rho * 4096 + i * 1024 + lane * 16);
      asm volatile("" ::: "memory");
#pragma unroll
      for (int i = 0; i < 4; ++i) ok = ok && e[i].y == tag && e[i].w == tag;
      if (__all(ok)) break;
    }
    float s0 = (f_(e[0].x) + f_(e[1].x)) + (f_(e[2].x) + f_(e[3].x)), s1 = (f_(e[0].z) + f_(e[1].z)) + (f_(e[2].z) + f_(e[3].z));
    s0 += dpp<X8>(s0); s1 += dpp<X8>(s1);
    s0 = sum32(sum16(s0)); s1 = sum32(sum16(s1));
    if (lane < 8) *(GAS u32*)(out + (rho & 1) * 1024 + part * 512 + (rho >> 1) * 16 + 2 * lane) = pk(s0, s1);
    if (gid == 0 && lane == 0) __builtin_amdgcn_raw_buffer_store_b32(tag, rs, WS_EPOCH, 0, 16);
    STAMP(13);
  }
}

extern "C" void kernel_launch(void* const* d_in, const int* in_sizes, int n_in, void* d_out, int out_size, void* d_ws, size_t ws_size,
                              hipStream_t stream) {
  (void)hipFuncSetAttribute((const void*)fused_decode, hipFuncAttributeMaxDynamicSharedMemorySize, 163840);
  fused_decode<<<256, 512, 163840, stream>>>((const u16*)d_in[0], (const u16*)d_in[2], (const u16*)d_in[3], (unsigned char*)d_ws,
                                             (const u16*)d_in[1], (const u16*)d_in[7], (const u16*)d_in[8], (const u16*)d_in[6],
                                             (const u16*)d_in[9], (const u16*)d_in[4], (const u16*)d_in[5], (const u16*)d_in[10],
                                             (u16*)d_out);
}
```

```cpp
#define STAMPS
#define XMODE 0
#define XT 0
#define XW 0
#define XS 0
#define XK 0
#include <hip/hip_runtime.h>
#include <hip/hip_bf16.h>

typedef unsigned u32;
typedef unsigned short u16;
typedef unsigned long long u64;
typedef u32 v4u __attribute__((ext_vector_type(4)));
typedef u32 v2u __attribute__((ext_vector_type(2)));
typedef short s16x4 __attribute__((ext_vector_type(4)));
typedef short bf16x8 __attribute__((ext_vector_type(8)));
typedef __bf16 bf16x2 __attribute__((ext_vector_type(2)));
typedef float f32x4 __attribute__((ext_vector_type(4)));
typedef float f32x16 __attribute__((ext_vector_type(16)));
#define GAS __attribute__((address_space(1)))
#define LAS __attribute__((address_space(3)))
#define DI __device__ __forceinline__
#define SB() __builtin_amdgcn_sched_barrier(0)

#define WIN0 32
#define WINA0 16
#define NA0 2
#define WINA 44
#define WINB 32
#define ORDER 0
#define QW 8
#define CODE_BYTES 24576

constexpr u32 WS_EPOCH = 0;
constexpr u32 WS_H1 = 1835008;
constexpr u32 WS_QABS = 65536;
constexpr u32 WS_CHK = 131072;
constexpr u32 WS_QF0 = 139264;
constexpr u32 WS_QB = 1048576 + 65536;
constexpr u32 WS_ASTAT = 262144;
constexpr u32 WS_H3B = 524288;
constexpr u32 WS_BIN = 786432;
constexpr u32 WS_H4 = 1310720;
constexpr u32 WS_APART = 2097152;
constexpr u32 WS_BYTES = 6291456;
constexpr u32 WS_STAMP = 8388608;

constexpr u32 L_C = 0, L_K = 0, L_V = 65536, L_WQ = 0, L_UK = 32768, L_WQB = 98304;
constexpr u32 L_Q = 131072;
constexpr u32 L_X = L_Q + 16384, L_QL = L_Q + 18432, L_QBL = L_Q + 22528, L_PB = L_Q + 24576;
constexpr u32 L_P = L_Q, L_SCR = L_Q + 8192, L_LSUM = L_Q + 28672;
constexpr u32 L_MAXX = L_Q + 31744, L_SYNC = L_MAXX + 512;
constexpr u32 L_W1 = 0, L_W2 = 32768, L_WGT = 65536, L_CK = 66048, L_B1 = 67584, L_B2 = 73728;
enum { S_PUB = 0, S_WQ, S_UK, S_WQB, S_QBISS, S_QISS, S_BAD, S_QGO, S_QST, S_LAND, S_MAX, S_P, S_PV, S_LGO, S_P2DONE, S_QBRDY, S_VLAND, S_N };

constexpr float KSC = 0.08838834764831845f * 1.4426950408889634f;
constexpr int SPIN_CAP = 1 << 15;

#define STAMP(i) do { u64 t_; SB(); asm volatile("s_memrealtime %0\n\ts_waitcnt lgkmcnt(0)" : "=s"(t_) :: "memory"); SB(); if (lane == (i)) tsv = (u32)t_; } while (0)

DI void dma16(const void* sbase, u32 voff, u32 lds_dst) {
  u32 keep;
  asm volatile("s_mov_b32 %0, m0\n\ts_mov_b32 m0, %3\n\ts_nop 0\n\tglobal_load_lds_dwordx4 %1, %2 nt\n\ts_mov_b32 m0, %0"
               : "=&s"(keep) : "v"(voff), "s"(sbase), "s"(lds_dst) : "memory");
}
template <int W, u32 LDS> DI void dma4(const void* sb, u32 v0, u32 v1, u32 v2, u32 v3) {
  u32 keep;
  asm volatile("s_mov_b32 %0, m0\n\ts_mov_b32 m0, %6\n\ts_waitcnt vmcnt(%7)\n\t"
               "global_load_lds_dwordx4 %1, %5 nt\n\tglobal_load_lds_dwordx4 %2, %5 offset:1024 nt\n\ts_waitcnt vmcnt(%7)\n\t"
               "global_load_lds_dwordx4 %3, %5 offset:2048 nt\n\tglobal_load_lds_dwordx4 %4, %5 offset:3072 nt\n\ts_mov_b32 m0, %0"
               : "=&s"(keep) : "v"(v0), "v"(v1), "v"(v2), "v"(v3), "s"(sb), "i"(LDS), "i"(W) : "memory");
}
template <int I> struct IC { static constexpr int v = I; };
template <int B, int E, class F> DI void sfor(F&& f) { if constexpr (B < E) { f(IC<B>{}); sfor<B + 1, E>(f); } }
template <int N> DI void vmwait() { asm volatile("s_waitcnt vmcnt(%0)" :: "i"(N) : "memory"); }
DI u32 pk(float a, float b) { bf16x2 v = {(__bf16)a, (__bf16)b}; return __builtin_bit_cast(u32, v); }
DI float blo(u32 x) { return __builtin_bit_cast(float, x << 16); }
DI float bhi(u32 x) { return __builtin_bit_cast(float, x & 0xffff0000u); }
template <class T> DI LAS T* lp(u32 a) { return (LAS T*)(size_t)a; }
DI v4u lds128(u32 a) { return *lp<v4u>(a); }
DI void lds128w(u32 a, v4u v) { *lp<v4u>(a) = v; }
DI void lds64w(u32 a, u32 x, u32 y) { *lp<v2u>(a) = v2u{x, y}; }
DI v4u ldstr(u32 a0, u32 a1) {
  s16x4 lo = __builtin_amdgcn_ds_read_tr16_b64_v4i16(lp<s16x4>(a0));
  s16x4 hi = __builtin_amdgcn_ds_read_tr16_b64_v4i16(lp<s16x4>(a1));
  v2u l = __builtin_bit_cast(v2u, lo), h = __builtin_bit_cast(v2u, hi);
  return v4u{l.x, l.y, h.x, h.y};
}
DI f32x4 mfma16(v4u a, v4u b, f32x4 c) { return __builtin_amdgcn_mfma_f32_16x16x32_bf16(__builtin_bit_cast(bf16x8, a), __builtin_bit_cast(bf16x8, b), c, 0, 0, 0); }
DI f32x16 mfma32(v4u a, v4u b, f32x16 c) { return __builtin_amdgcn_mfma_f32_32x32x16_bf16(__builtin_bit_cast(bf16x8, a), __builtin_bit_cast(bf16x8, b), c, 0, 0, 0); }
template <int CTRL> DI u32 dppu(u32 x) { return (u32)__builtin_amdgcn_mov_dpp((int)x, CTRL, 0xf, 0xf, true); }
DI float f_(u32 x) { return __builtin_bit_cast(float, x); }
DI u32 u_(float x) { return __builtin_bit_cast(u32, x); }
template <int CTRL> DI float dpp(float x) { return f_(dppu<CTRL>(u_(x))); }
constexpr int X1 = 0xB1, X2 = 0x4E, X7 = 0x141, X8 = 0x128;
DI float sum32(float x) { auto t = __builtin_amdgcn_permlane32_swap(u_(x), u_(x), false, false); return f_(t[0]) + f_(t[1]); }
DI float max32(float x) { auto t = __builtin_amdgcn_permlane32_swap(u_(x), u_(x), false, false); return fmaxf(f_(t[0]), f_(t[1])); }
DI float sum16(float x) { auto t = __builtin_amdgcn_permlane16_swap(u_(x), u_(x), false, false); return f_(t[0]) + f_(t[1]); }
DI float max16(float x) { auto t = __builtin_amdgcn_permlane16_swap(u_(x), u_(x), false, false); return fmaxf(f_(t[0]), f_(t[1])); }
DI void sig(int word, int lane) {
  asm volatile("" ::: "memory");
  if (lane == 0) __hip_atomic_fetch_add(lp<u32>(L_SYNC + 4 * word), 1u, __ATOMIC_RELAXED, __HIP_MEMORY_SCOPE_WORKGROUP);
  asm volatile("" ::: "memory");
}
template <bool SLEEP> DI void waitw(int word, u32 n) {
  while ((u32)__builtin_amdgcn_readfirstlane(__hip_atomic_load(lp<u32>(L_SYNC + 4 * word), __ATOMIC_RELAXED, __HIP_MEMORY_SCOPE_WORKGROUP)) < n) {
    if (SLEEP) __builtin_amdgcn_s_sleep(2);
  }
  asm volatile("" ::: "memory");
}
DI void bar() { asm volatile("s_waitcnt lgkmcnt(0)\n\ts_barrier" ::: "memory"); }
#define LD1(off) __builtin_amdgcn_raw_buffer_load_b128(rs, (off), 0, 16)
#define ST1(v, off) __builtin_amdgcn_raw_buffer_store_b128((v), rs, (off), 0, 16)
#define TAGLOAD() __builtin_amdgcn_raw_buffer_load_b32(rs, WS_EPOCH, 0, 16)
DI v4u ldnt(const void* p, u32 off) { return __builtin_nontemporal_load((const GAS v4u*)((const GAS unsigned char*)p + off)); }
DI void stage_w(u32 base, const v4u (&park)[32], u32 g, u32 n) {
#pragma unroll
  for (int i = 0; i < 32; ++i) lds128w(base + g * 256 + i * 1024 + 16 * (n ^ ((g | (((i >> 1) & 1) << 2)) << 1)), park[i]);
}

__global__ void __launch_bounds__(512, 2)
fused_decode(const u16* __restrict__ xA, const u16* __restrict__ wqA, const u16* __restrict__ wuk, unsigned char* ws,
             const u16* __restrict__ ckv, const u16* __restrict__ kc, const u16* __restrict__ vc, const u16* __restrict__ xB,
             const u16* __restrict__ wqB, const u16* __restrict__ wuv, const u16* __restrict__ woA, const u16* __restrict__ woB,
             u16* __restrict__ out) {
  u64 pc0; asm volatile("s_getpc_b64 %0" : "=s"(pc0));
  const int tid = threadIdx.x, lane = tid & 63, wave = __builtin_amdgcn_readfirstlane(tid >> 6);
  u32 tsv = 0;
  STAMP(0);
  const u32 gid = blockIdx.x, x = gid & 7, r = gid >> 3;
  const bool isA = (r & 1) == 0;
  const u32 l16 = r >> 1;
  const u32 ht = 4 * x + (l16 >> 2), q4 = l16 & 3;
  const u32 g = lane >> 4, n = lane & 15, tq = n >> 2, tp = lane & 3;
  const __amdgpu_buffer_rsrc_t rs = __builtin_amdgcn_make_buffer_rsrc((void*)ws, 0, WS_BYTES, 0x00020000);
  u32 tag;

  if (wave == 4) {
    bar();
    tag = __builtin_amdgcn_readfirstlane(TAGLOAD()) + 1;
  } else if (wave == 5) {
    __builtin_amdgcn_s_setprio(1);
#define SIG_AT(ISSUED, W, NEED, WORD) if constexpr ((ISSUED) - (W) >= (NEED) && (ISSUED) - 4 - (W) < (NEED)) { vmwait<W>(); sig(WORD, lane); }
    const u32 rw = lane >> 3, sw = ((rw >> 1) & 1) << 1;
    const u32 ce = ((lane & 7) ^ sw) * 16, cd = ((lane & 7) ^ sw ^ 4) * 16 + 64512;
    if (isA) {
      dma16(xB, lane * 16, L_X);
      dma16(xB, lane * 16 + 1024, L_X + 1024);
      const u32 vb = (256 * (l16 >> 3) + rw) * 8192 + x * 1024 + (l16 & 7) * 128;
      sfor<0, 8>([&](auto I) {
        constexpr int G = I.v;
        dma4<WINA0, L_WQB + G * 4096>(wqB, vb + ce + G * 262144, vb + cd + G * 262144, vb + ce + G * 262144 + 129024, vb + cd + G * 262144 + 129024);
        if constexpr (G == 1) bar();
      });
      const unsigned char* src = (const unsigned char*)ckv + (size_t)(x * 16 + l16) * 131072;
      u32 cv[16];
#pragma unroll
      for (int i = 0; i < 16; ++i) cv[i] = (lane ^ (((i & 3) << 2) | (i >> 2))) * 16;
      sfor<0, 32>([&](auto I) {
        constexpr int G = I.v;
        if constexpr (G == 24) waitw<true>(S_P2DONE, 4);
        dma4<(G < NA0 ? WINA0 : WINA), L_C + G * 4096>(src + G * 4096, cv[(4 * G) & 15], cv[(4 * G + 1) & 15], cv[(4 * G + 2) & 15], cv[(4 * G + 3) & 15]);
        if constexpr (G + 1 == NA0) { vmwait<4 * NA0>(); sig(S_WQB, lane); }
      });
      STAMP(1);
      if constexpr (ORDER == 0) { vmwait<QW>(); sig(S_QGO, lane); vmwait<0>(); sig(S_LAND, lane); STAMP(2); waitw<false>(S_QISS, 4); }
      v4u park[32];
      const u32 vo = (128 * q4 + g) * 8192 + ht * 256 + n * 16;
      sfor<0, 8>([&](auto I) {
        constexpr int G = I.v;
        vmwait<WINA>(); SB();
        park[4 * G] = ldnt(wuv, vo + (4 * G) * 32768); park[4 * G + 1] = ldnt(wuv, vo + (4 * G + 1) * 32768);
        SB(); vmwait<WINA>(); SB();
        park[4 * G + 2] = ldnt(wuv, vo + (4 * G + 2) * 32768); park[4 * G + 3] = ldnt(wuv, vo + (4 * G + 3) * 32768);
        SB();
      });
      if constexpr (ORDER != 1) sig(S_LGO, lane);
      STAMP(3);
      if constexpr (ORDER != 0) { vmwait<32>(); sig(S_LAND, lane); STAMP(2); }
      if constexpr (ORDER == 1) { vmwait<QW>(); sig(S_QGO, lane); }
      waitw<true>(S_PV, 4);
      const u32 tv = TAGLOAD();
      stage_w(L_W1, park, g, n);
      tag = __builtin_amdgcn_readfirstlane(tv) + 1;
    } else {
      dma16(xA, lane * 16, L_X);
      dma16(xA, lane * 16 + 1024, L_X + 1024);
      const u32 va = (256 * (q4 >> 1) + rw) * 8192 + ht * 256 + (q4 & 1) * 128;
      sfor<0, 8>([&](auto I) {
        constexpr int G = I.v;
        dma4<63, L_WQ + G * 4096>(wqA, va + ce + G * 262144, va + cd + G * 262144, va + ce + G * 262144 + 129024, va + cd + G * 262144 + 129024);
      });
      STAMP(15);
      bar();
      const u32 uo = (128 * q4 + g) * 8192 + ht * 256;
      sfor<0, 8>([&](auto I) {
        constexpr int G = I.v;
        const u32 u0 = uo + G * 131072 + ((n ^ (g | (((2 * G) & 3) << 2))) * 16), u1 = uo + G * 131072 + ((n ^ (g | (((2 * G + 1) & 3) << 2))) * 16);
        dma4<WIN0, L_UK + G * 4096>(wuk, u0, u0 + 31744, u1 + 63488, u1 + 95232);
        SIG_AT(38 + 4 * G, WIN0, 34, S_WQ)
      });
      STAMP(14);
      vmwait<0>(); sig(S_UK, lane);
      tag = __builtin_amdgcn_readfirstlane(TAGLOAD()) + 1;
      waitw<false>(S_QBISS, 1);
      STAMP(13);
      const size_t base = (((size_t)(l16 >> 3) * 8 + x) * 8192 + 6144 + (l16 & 7) * 256) * 256;
      const unsigned char* ks = (const unsigned char*)kc + base;
      const unsigned char* vs = (const unsigned char*)vc + base;
      {
        u32 kv[4];
#pragma unroll
        for (int i = 0; i < 4; ++i) kv[i] = g * 256 + ((n ^ (4 * i + g)) * 16);
        sfor<0, 16>([&](auto I) { constexpr int G = I.v; dma4<WINB, L_K + G * 4096>(ks + G * 4096, kv[0], kv[1], kv[2], kv[3]); });
      }
      STAMP(1);
      {
        const u32 v0 = g * 256 + ((n ^ (g << 1)) * 16), v1 = g * 256 + ((n ^ ((4 + g) << 1)) * 16);
        sfor<0, 16>([&](auto I) {
          constexpr int G = I.v;
          dma4<WINB, L_V + G * 4096>(vs + G * 4096, v0, v1, v0, v1);
          SIG_AT(4 + 4 * G, WINB, 0, S_LAND)
          SIG_AT(4 + 4 * G, WINB, 16, S_VLAND)
          SIG_AT(4 + 4 * G, WINB, 32, S_VLAND)
          SIG_AT(4 + 4 * G, WINB, 48, S_VLAND)
        });
      }
      STAMP(2);
      waitw<false>(S_P, 4);
      {
        const u32 vo = (ht * 128 + g) * 1024 + q4 * 256, w0 = vo + ((n ^ (g << 1)) * 16), w1 = vo + ((n ^ ((g | 4) << 1)) * 16) + 6144;
        sfor<0, 8>([&](auto I) {
          constexpr int G = I.v;
          dma4<WINB, L_W1 + G * 4096>(woB, w0 + G * 16384, w0 + G * 16384 + 3072, w1 + G * 16384, w1 + G * 16384 + 3072);
          SIG_AT(68 + 4 * G, WINB, 48, S_VLAND)
          SIG_AT(68 + 4 * G, WINB, 64, S_VLAND)
        });
      }
      STAMP(3);
      vmwait<0>();
    }
    STAMP(4);
  } else if (wave == 6) {
    bar();
    const u32 tv = TAGLOAD();
    if (isA) {
      __builtin_amdgcn_s_setprio(1);
      v4u park[32];
      if constexpr (ORDER == 1) waitw<true>(S_QISS, 4); else waitw<true>(S_LGO, 1);
      const u32 vo = (ht * 128 + g) * 1024 + q4 * 256 + n * 16;
#pragma unroll
      for (int i = 0; i < 32; i += 2) {
        park[i] = ldnt(woA, vo + i * 4096); park[i + 1] = ldnt(woA, vo + (i + 1) * 4096);
        SB();
      }
      if constexpr (ORDER == 2) { vmwait<QW>(); sig(S_QGO, lane); }
      STAMP(3);
      waitw<true>(S_PV, 4);
      tag = __builtin_amdgcn_readfirstlane(tv) + 1;
      stage_w(L_W2, park, g, n);
      STAMP(4);
    } else {
      __builtin_amdgcn_s_setprio(2);
      tag = __builtin_amdgcn_readfirstlane(tv) + 1;
      waitw<false>(S_PUB, 4);
      STAMP(2);
      v4u e[8];
      for (int spin = 0; spin < SPIN_CAP; ++spin) {
        bool ok = true;
#pragma unroll
        for (int i = 0; i < 8; ++i) e[i] = LD1(WS_QB + x * 16384 + (l16 >> 3) * 8192 + i * 1024 + lane * 16);
        SB();
        if (spin == 0) sig(S_QBISS, lane);
        asm volatile("" ::: "memory");
#pragma unroll
        for (int i = 0; i < 8; ++i) ok = ok && e[i].y == tag && e[i].w == tag;
        if (__all(ok)) break;
      }
#pragma unroll
      for (int i = 0; i < 4; ++i) *lp<u32>(L_QBL + i * 256 + lane * 4) = pk(f_(e[i].x) + f_(e[4 + i].x), f_(e[i].z) + f_(e[4 + i].z));
      sig(S_QBRDY, lane);
      STAMP(3);
      __builtin_amdgcn_s_setprio(0);
    }
  } else if (wave == 7) {
    bar();
    const u32 tv = TAGLOAD();
    if (r < 2) {
      const GAS u32* c = (const GAS u32*)(pc0 & ~127ull);
      u32 s = 0;
#pragma unroll
      for (int i = 0; i < (CODE_BYTES / 128 + 127) / 128; ++i) {
        const u32 ln = (2 * i + r) * 64 + lane;
        if (ln < CODE_BYTES / 128) s += c[ln * 32];
      }
      asm volatile("" :: "v"(s));
    }
    if (lane < 6) { u32 w = __builtin_amdgcn_raw_buffer_load_b32(rs, lane * 1048576 + 1048576 - 64, 0, 16); asm volatile("" :: "v"(w)); }
    tag = __builtin_amdgcn_readfirstlane(tv) + 1;
  } else {
    if (wave == 0 && lane < S_N) *lp<u32>(L_SYNC + 4 * lane) = 0;
    bar();
    tag = __builtin_amdgcn_readfirstlane(TAGLOAD()) + 1;
    const u32 w = wave;
    const u32 a0 = (8 * g + tq) * 128 + 16 * ((2 * w + (tp >> 1)) ^ ((((tq >> 1) & 1) | ((g & 1) << 1)) << 1)) + 8 * (tp & 1);
    if (isA) {
      const u32 bs = x * 16 + l16, b = bs >> 6, hh = lane >> 5, hd = lane & 31;
      __builtin_amdgcn_s_setprio(2);
      {
        waitw<false>(S_WQB, 1);
        v4u xf[8], af[8];
#pragma unroll
        for (int s = 0; s < 8; ++s) { af[s] = ldstr(L_WQB + a0 + s * 4096, L_WQB + a0 + s * 4096 + 512); xf[s] = lds128(L_X + (lane & 1) * 1024 + (l16 >> 3) * 512 + g * 16 + s * 64); }
        sig(S_P2DONE, lane);
        SB();
        f32x4 c0 = {0.f, 0.f, 0.f, 0.f}, c1 = c0;
#pragma unroll
        for (int s = 0; s < 8; s += 2) { c0 = mfma16(af[s], xf[s], c0); c1 = mfma16(af[s + 1], xf[s + 1], c1); }
        c0 += c1;
        if (n < 4) ST1((v4u{u_((n & 2) ? c0[2] : c0[0]), tag, u_((n & 2) ? c0[3] : c0[1]), tag}),
                       WS_QB + x * 16384 + (n & 1) * 8192 + l16 * 512 + (8 * w + 2 * g + (n >> 1)) * 16);
        STAMP(13);
      }
      waitw<true>(S_QGO, 1);
      STAMP(14);
      v4u qr[8], z0, z1;
      for (int spin = 0; spin < SPIN_CAP; ++spin) {
#pragma unroll
        for (int i = 0; i < 8; ++i) qr[i] = LD1(WS_QABS + b * 32768 + (8 * w + i) * 1024 + lane * 16);
        const u32 co = WS_CHK + (8 * w + 4 * ((lane >> 1) & 1) + 2 * (lane & 1)) * 128 + (lane >> 2) * 8 + b * 4;
        const u32 ck0 = __builtin_amdgcn_raw_buffer_load_b32(rs, co, 0, 16), ck1 = __builtin_amdgcn_raw_buffer_load_b32(rs, co + 128, 0, 16);
        z0 = LD1(WS_QF0 + b * 2048 + hd * 64 + hh * 32);
        z1 = LD1(WS_QF0 + b * 2048 + hd * 64 + hh * 32 + 16);
        SB();
        if (spin == 0) sig(S_QISS, lane);
        asm volatile("" ::: "memory");
        u32 c8[8];
#pragma unroll
        for (int i = 0; i < 8; ++i) c8[i] = (qr[i].x + qr[i].y) + (qr[i].z + qr[i].w);
        u32 c4[4], c2[2];
#pragma unroll
        for (int i = 0; i < 4; ++i) { const u32 p = c8[i] + dppu<X2>(c8[i]), q = c8[i + 4] + dppu<X2>(c8[i + 4]); c4[i] = (lane & 2) ? q : p; }
#pragma unroll
        for (int i = 0; i < 2; ++i) { const u32 p = c4[i] + dppu<X1>(c4[i]), q = c4[i + 2] + dppu<X1>(c4[i + 2]); c2[i] = (lane & 1) ? q : p; }
        if (__all(c2[0] + tag == ck0 && c2[1] + tag == ck1 && z0.y == tag && z0.w == tag && z1.y == tag && z1.w == tag)) break;
        if (lane == 15) tsv += 1;
      }
      STAMP(1);
      if (lane >= 2) {
#pragma unroll
        for (int i = 0; i < 8; ++i)
          lds128w(L_Q - 1024 + (lane >> 1) * 1024 + ((((lane & 1) * 32 + 8 * w + i) ^ (lane & 7)) * 16), qr[i]);
      }
      sig(S_QST, lane);
      STAMP(2);
      waitw<false>(S_QST, 4);
      waitw<false>(S_LAND, 1);
      STAMP(3);
      f32x16 s;
#pragma unroll
      for (int i = 0; i < 16; ++i) s[i] = 0.f;
      {
        const u32 fr = ((hd & 3) << 2) | ((hd >> 2) & 3);
        const u32 ca = L_C + (32 * w + hd) * 1024 + 16 * (hh ^ fr), qa = L_Q - 1024 + ((lane ^ hh) * 16);
        v4u af[32], bf[32];
        bf[0] = v4u{z0.x, z0.z, z1.x, z1.z};
        constexpr int PD = 6;
#define S_RD(k) do { af[k] = lds128((ca ^ (32 * ((k) & 7))) + 256 * ((k) >> 3)); if ((k) > 0) bf[k] = lds128((qa ^ (32 * ((k) & 3))) + (k) * 1024); } while (0)
#pragma unroll
        for (int k = 0; k < PD; ++k) S_RD(k);
        SB();
#pragma unroll
        for (int k = 0; k < 32; ++k) {
          if (k + PD < 32) S_RD(k + PD);
          s = mfma32(af[k], bf[k], s);
          SB();
        }
      }
      float m = s[0];
#pragma unroll
      for (int i = 1; i < 16; ++i) m = fmaxf(m, s[i]);
      m = max32(m);
      if (hh == 0) *lp<float>(L_MAXX + hd * 16 + w * 4) = m;
      sig(S_MAX, lane);
      STAMP(4);
      waitw<false>(S_MAX, 4);
      const v4u mm = lds128(L_MAXX + hd * 16);
      const float mk = fmaxf(fmaxf(f_(mm.x), f_(mm.y)), fmaxf(f_(mm.z), f_(mm.w))) * KSC;
      float ls = 0.f;
#pragma unroll
      for (int i = 0; i < 16; ++i) { s[i] = __builtin_amdgcn_exp2f(fmaf(s[i], KSC, -mk)); ls += s[i]; }
      lds128w(L_P + (2 * w) * 1024 + lane * 16, v4u{pk(s[0], s[1]), pk(s[2], s[3]), pk(s[4], s[5]), pk(s[6], s[7])});
      lds128w(L_P + (2 * w + 1) * 1024 + lane * 16, v4u{pk(s[8], s[9]), pk(s[10], s[11]), pk(s[12], s[13]), pk(s[14], s[15])});
      ls = sum32(ls);
      if (hh == 0) *lp<float>(L_LSUM + hd * 16 + w * 4) = ls;
      sig(S_P, lane);
      const u32 blk = (lane >> 4) & 1;
      u32 ta[2];
#pragma unroll
      for (int t = 0; t < 2; ++t)
        ta[t] = L_C + (4 * hh + 8 * t + tq) * 1024 + 256 * w + 16 * ((2 * blk + (tp >> 1)) ^ ((tq << 2) | (hh + 2 * t))) + 8 * (tp & 1);
      v4u cf[32];
      constexpr int PV_PD = 5;
#define PV_RD(kk) cf[kk] = ldstr((ta[0] ^ (64 * ((kk) >> 3))) + ((kk) & 7) * 16384, (ta[1] ^ (64 * ((kk) >> 3))) + ((kk) & 7) * 16384)
#pragma unroll
      for (int k = 0; k < PV_PD; ++k) PV_RD(k);
      SB();
      waitw<false>(S_P, 4);
      STAMP(5);
      v4u pf[8];
#pragma unroll
      for (int k = 0; k < 8; ++k) pf[k] = lds128(L_P + k * 1024 + lane * 16);
      const v4u lv = lds128(L_LSUM + hd * 16);
      SB();
      const u32 scw = L_SCR + w * 4096 + hd * 64 + 8 * hh, scx = 16 * ((hd >> 1) & 3);
      const u32 scr = L_SCR + w * 4096 + (lane >> 2) * 64 + 16 * (tp ^ ((lane >> 3) & 3));
      const u32 sto = WS_APART + (((lane >> 2) * 4 + w) * 128 + bs) * 256 + 16 * tp;
      f32x16 o[4];
      v4u ov[2];
      u32 cs = tag;
#define EPI_W(c) _Pragma("unroll") for (int gg = 0; gg < 4; ++gg) { \
        const u32 p0 = pk(o[c][4 * gg], o[c][4 * gg + 1]), p1 = pk(o[c][4 * gg + 2], o[c][4 * gg + 3]); \
        lds64w(scw + ((c) & 1) * 2048 + ((16 * gg) ^ scx), p0, p1); cs += p0 + p1; }
#define EPI_R(c) do { ov[0] = lds128(scr + ((c) & 1) * 2048); ov[1] = lds128(scr + ((c) & 1) * 2048 + 1024); } while (0)
#define EPI_S(c) do { ST1(ov[0], sto + 64 * (c)); ST1(ov[1], sto + 64 * (c) + 16 * 4 * 128 * 256); } while (0)
#pragma unroll
      for (int kk = 0; kk < 32; ++kk) {
        const int c = kk >> 3, k = kk & 7;
        if (kk + PV_PD < 32) PV_RD(kk + PV_PD);
        if (k == 0) {
          f32x16 z;
#pragma unroll
          for (int i = 0; i < 16; ++i) z[i] = 0.f;
          o[c] = mfma32(cf[kk], pf[k], z);
        } else o[c] = mfma32(cf[kk], pf[k], o[c]);
        if (c > 0 && k == 1) { EPI_W(c - 1) EPI_R(c - 1); }
        if (c > 0 && k == 5) { EPI_S(c - 1); }
        SB();
      }
      sig(S_PV, lane);
      EPI_W(3) EPI_R(3);
      const float lsum = (f_(lv.x) + f_(lv.y)) + (f_(lv.z) + f_(lv.w));
      { auto t = __builtin_amdgcn_permlane32_swap(cs, cs, false, false); cs = t[0] + t[1] - tag + u_(mk) + u_(lsum); }
      EPI_S(3);
      if (hh == 0) ST1((v4u{u_(mk), u_(lsum), cs, tag}), WS_ASTAT + ((hd * 4 + w) * 128 + bs) * 16);
      STAMP(7);
    } else {
      __builtin_amdgcn_s_setprio(2);
      {
        waitw<false>(S_WQ, 1);
        STAMP(10);
        v4u xf[8], af[8];
#pragma unroll
        for (int s = 0; s < 8; ++s) { af[s] = ldstr(L_WQ + a0 + s * 4096, L_WQ + a0 + s * 4096 + 512); xf[s] = lds128(L_X + (lane & 1) * 1024 + (q4 >> 1) * 512 + g * 16 + s * 64); }
        SB();
        f32x4 c0 = {0.f, 0.f, 0.f, 0.f}, c1 = c0;
#pragma unroll
        for (int s = 0; s < 8; s += 2) { c0 = mfma16(af[s], xf[s], c0); c1 = mfma16(af[s + 1], xf[s + 1], c1); }
        c0 += c1;
        if (n < 4) ST1((v4u{u_((n & 2) ? c0[2] : c0[0]), tag, u_((n & 2) ? c0[3] : c0[1]), tag}),
                       WS_H1 + ht * 4096 + q4 * 1024 + (n & 1) * 512 + (8 * w + 2 * g + (n >> 1)) * 16);
        STAMP(11);
        waitw<false>(S_UK, 1);
        v4u uk[8];
#pragma unroll
        for (int t = 0; t < 2; ++t)
#pragma unroll
          for (int s = 0; s < 4; ++s) uk[t * 4 + s] = lds128(L_UK + (32 * w + 8 * (n >> 2) + 4 * t + (n & 3)) * 256 + 16 * ((4 * s + g) ^ n));
        v4u e[4];
        for (int spin = 0; spin < SPIN_CAP; ++spin) {
          bool ok = true;
#pragma unroll
          for (int i = 0; i < 4; ++i) e[i] = LD1(WS_H1 + ht * 4096 + i * 1024 + lane * 16);
          asm volatile("" ::: "memory");
#pragma unroll
          for (int i = 0; i < 4; ++i) ok = ok && e[i].y == tag && e[i].w == tag;
          if (__all(ok)) break;
        }
        STAMP(14);
        const u32 ql = L_QL + w * 512;
#pragma unroll
        for (int d = 0; d < 2; ++d)
          *lp<u32>(ql + (lane >> 5) * 256 + d * 128 + (lane & 31) * 4) = pk(f_(e[d].x) + f_(e[2 + d].x), f_(e[d].z) + f_(e[2 + d].z));
        v4u qb[4];
#pragma unroll
        for (int s = 0; s < 4; ++s) qb[s] = lds128(ql + (lane & 1) * 256 + g * 16 + s * 64);
        SB();
        f32x4 u0 = {0.f, 0.f, 0.f, 0.f}, u1 = u0;
#pragma unroll
        for (int s = 0; s < 4; ++s) { u0 = mfma16(uk[s], qb[s], u0); u1 = mfma16(uk[4 + s], qb[s], u1); }
        const v4u pv = {pk(u0[0], u0[1]), pk(u0[2], u0[3]), pk(u1[0], u1[1]), pk(u1[2], u1[3])};
        if (n < 2) ST1(pv, WS_QABS + n * 32768 + ht * 1024 + q4 * 256 + w * 64 + g * 16);
        if (q4 == 0 && w == 0 && n < 2 && g < 2) {
          ST1((v4u{pv.x, tag, pv.y, tag}), WS_QF0 + n * 2048 + ht * 64 + g * 32);
          ST1((v4u{pv.z, tag, pv.w, tag}), WS_QF0 + n * 2048 + ht * 64 + g * 32 + 16);
        }
        u32 cs = (pv.x + pv.y) + (pv.z + pv.w);
        { auto t = __builtin_amdgcn_permlane16_swap(cs, cs, false, false); cs = t[0] + t[1]; }
        { auto t = __builtin_amdgcn_permlane32_swap(cs, cs, false, false); cs = t[0] + t[1]; }
        cs += tag;
        const u32 cs1 = dppu<X1>(cs);
        if (lane == 0) __builtin_amdgcn_raw_buffer_store_b64(v2u{cs, cs1}, rs, WS_CHK + ht * 128 + (4 * q4 + w) * 8, 0, 16);
        sig(S_PUB, lane);
        STAMP(15);
      }
      waitw<true>(S_QBRDY, 1);
      v4u qf[4];
#pragma unroll
      for (int s = 0; s < 4; ++s) qf[s] = lds128(L_QBL + (lane & 3) * 256 + g * 16 + s * 64);
      waitw<true>(S_LAND, 1);
      STAMP(3);
      f32x4 sc[4];
      {
        v4u kf[16];
        const u32 ka = L_K + (64 * w + n) * 256 + 16 * (g ^ n);
#pragma unroll
        for (int i = 0; i < 16; ++i) kf[i] = lds128((ka ^ (64 * (i & 3))) + (i >> 2) * 4096);
        SB();
#pragma unroll
        for (int t = 0; t < 4; ++t) {
          sc[t] = f32x4{0.f, 0.f, 0.f, 0.f};
#pragma unroll
          for (int s = 0; s < 4; ++s) sc[t] = mfma16(kf[4 * t + s], qf[s], sc[t]);
        }
      }
      float m = sc[0][0];
#pragma unroll
      for (int i = 1; i < 16; ++i) m = fmaxf(m, sc[i >> 2][i & 3]);
      m = max32(max16(m));
      if (lane < 4) *lp<float>(L_MAXX + lane * 16 + w * 4) = m;
      sig(S_MAX, lane);
      waitw<false>(S_MAX, 4);
      const v4u mm = lds128(L_MAXX + (lane & 3) * 16);
      const float mk = fmaxf(fmaxf(f_(mm.x), f_(mm.y)), fmaxf(f_(mm.z), f_(mm.w))) * KSC;
      float ls = 0.f;
#pragma unroll
      for (int i = 0; i < 16; ++i) { const float p = __builtin_amdgcn_exp2f(fmaf(sc[i >> 2][i & 3], KSC, -mk)); sc[i >> 2][i & 3] = p; ls += p; }
      ls = sum32(sum16(ls));
      if (n < 4) {
#pragma unroll
        for (int u = 0; u < 2; ++u)
          lds128w(L_PB + (2 * w + u) * 256 + g * 64 + n * 16,
                  v4u{pk(sc[2 * u][0], sc[2 * u][1]), pk(sc[2 * u][2], sc[2 * u][3]), pk(sc[2 * u + 1][0], sc[2 * u + 1][1]), pk(sc[2 * u + 1][2], sc[2 * u + 1][3])});
      }
      if (lane < 4) *lp<float>(L_LSUM + lane * 16 + w * 4) = ls;
      sig(S_P, lane);
      waitw<false>(S_P, 4);
      STAMP(5);
      v4u pf[8];
#pragma unroll
      for (int k = 0; k < 8; ++k) pf[k] = lds128(L_PB + k * 256 + g * 64 + (lane & 3) * 16);
      if (w == 0) {
        const v4u lv = lds128(L_LSUM + (lane & 3) * 16);
        const float lsum = (f_(lv.x) + f_(lv.y)) + (f_(lv.z) + f_(lv.w));
        if (lane < 4) ST1((v4u{u_(mk), tag, u_(lsum), tag}), WS_BIN + (4 * x + lane) * 9216 + 8192 + l16 * 16);
      }
      f32x4 o0 = {0.f, 0.f, 0.f, 0.f}, o1 = o0;
      const u32 va = L_V + (4 * g + tq) * 256 + 16 * ((4 * w + (tp >> 1)) ^ ((4 * (g & 1) + tq) << 1)) + 8 * (tp & 1);
#pragma unroll
      for (int k2 = 0; k2 < 4; ++k2) {
        waitw<false>(S_VLAND, k2 + 1);
        v4u vf[4];
#pragma unroll
        for (int i = 0; i < 4; ++i) { const u32 a = (va ^ (32 * (i & 1))) + (2 * k2 + (i >> 1)) * 8192; vf[i] = ldstr(a, a + 4096); }
        SB();
        o0 = mfma16(vf[0], pf[2 * k2], o0); o1 = mfma16(vf[1], pf[2 * k2], o1);
        o0 = mfma16(vf[2], pf[2 * k2 + 1], o0); o1 = mfma16(vf[3], pf[2 * k2 + 1], o1);
      }
      STAMP(6);
      const f32x4 o = (n & 4) ? o1 : o0;
      if (n < 8) ST1((v4u{pk(o[0], o[1]), tag, pk(o[2], o[3]), tag}), WS_BIN + (4 * x + (n & 3)) * 9216 + l16 * 512 + (4 * (2 * w + (n >> 2)) + g) * 16);
      STAMP(7);
    }
  }

  const u32 v = wave;
  const u32 ab = (8 * g + tq) * 256 + 16 * ((2 * v + (tp >> 1)) ^ ((tq | ((g & 1) << 2)) << 1)) + 8 * (tp & 1);
  if (isA) {
    v4u a1[4], a2[4], bq[4];
    if (wave == 7) waitw<true>(S_PV, 4);
    for (u32 it = 1; it < 64; ++it) {
      v4u st0, st1;
      if (wave == 7) {
        for (int spin = 0; spin < SPIN_CAP; ++spin) {
          st0 = LD1(WS_ASTAT + (ht * 4 + q4) * 2048 + lane * 16);
          st1 = LD1(WS_ASTAT + (ht * 4 + q4) * 2048 + 1024 + lane * 16);
          asm volatile("" ::: "memory");
          if (__all(st0.w == tag && st1.w == tag)) break;
        }
        STAMP(2);
      }
      bar();
      STAMP(8);
      v4u pd[4];
#pragma unroll
      for (int i = 0; i < 4; ++i) pd[i] = LD1(WS_APART + (ht * 4 + q4) * 32768 + v * 4096 + i * 1024 + lane * 16);
      SB();
      if (wave == 7) {
#pragma unroll
        for (int i = 0; i < 2; ++i) {
          const v4u st = i ? st1 : st0;
          float M = f_(st.x);
          M = fmaxf(M, dpp<X1>(M)); M = fmaxf(M, dpp<X2>(M)); M = fmaxf(M, dpp<X7>(M)); M = fmaxf(M, dpp<X8>(M)); M = max16(M); M = max32(M);
          const float e = __builtin_amdgcn_exp2f(f_(st.x) - M);
          float S = e * f_(st.y);
          S += dpp<X1>(S); S += dpp<X2>(S); S += dpp<X7>(S); S += dpp<X8>(S); S = sum16(S); S = sum32(S);
          *lp<float>(L_WGT + i * 256 + lane * 4) = e / S;
          *lp<u32>(L_CK + i * 256 + lane * 4) = st.z - tag - st.x - st.y;
        }
      }
#pragma unroll
      for (int s = 0; s < 4; ++s) { a1[s] = ldstr(L_W1 + ab + s * 8192, L_W1 + ab + s * 8192 + 1024); a2[s] = ldstr(L_W2 + ab + s * 8192, L_W2 + ab + s * 8192 + 1024); }
      bar();
      float acc[8], wt[4];
#pragma unroll
      for (int i = 0; i < 4; ++i) wt[i] = *lp<float>(L_WGT + (v * 16 + 4 * i + g) * 4);
      const u32 ck = *lp<u32>(L_CK + (v * 16 + 4 * (lane & 3) + g) * 4);
#pragma unroll
      for (int k = 0; k < 4; ++k) { acc[2 * k] = wt[0] * blo(pd[0][k]); acc[2 * k + 1] = wt[0] * bhi(pd[0][k]); }
#pragma unroll
      for (int i = 1; i < 4; ++i)
#pragma unroll
        for (int k = 0; k < 4; ++k) { acc[2 * k] = fmaf(wt[i], blo(pd[i][k]), acc[2 * k]); acc[2 * k + 1] = fmaf(wt[i], bhi(pd[i][k]), acc[2 * k + 1]); }
      STAMP(9);
      {
        float y[4];
#pragma unroll
        for (int k = 0; k < 4; ++k) { auto t = __builtin_amdgcn_permlane32_swap(u_(acc[k]), u_(acc[k + 4]), false, false); y[k] = f_(t[0]) + f_(t[1]); }
        float z[2];
#pragma unroll
        for (int k = 0; k < 2; ++k) { auto t = __builtin_amdgcn_permlane16_swap(u_(y[k]), u_(y[k + 2]), false, false); z[k] = f_(t[0]) + f_(t[1]); }
        const u32 hi = pk(z[0], z[1]), lo = pk(z[0] - blo(hi), z[1] - bhi(hi));
        *lp<u32>(L_B1 + v * 272 + (8 * n + 2 * g) * 2) = hi;
        *lp<u32>(L_B1 + (8 + v) * 272 + (8 * n + 2 * g) * 2) = lo;
        u32 c4[4], c2[2];
#pragma unroll
        for (int i = 0; i < 4; ++i) c4[i] = (pd[i].x + pd[i].y) + (pd[i].z + pd[i].w);
#pragma unroll
        for (int i = 0; i < 2; ++i) { const u32 p = c4[i] + dppu<X2>(c4[i]), q = c4[i + 2] + dppu<X2>(c4[i + 2]); c2[i] = (lane & 2) ? q : p; }
        const u32 p1 = c2[0] + dppu<X1>(c2[0]), q1 = c2[1] + dppu<X1>(c2[1]);
        u32 c1 = (lane & 1) ? q1 : p1;
        c1 += dppu<X8>(c1);
        c1 += dppu<0x124>(c1);
        if (c1 != ck) *lp<u32>(L_SYNC + 4 * S_BAD) = it;
      }
      bar();
      const u32 bad = *lp<u32>(L_SYNC + 4 * S_BAD);
#pragma unroll
      for (int s = 0; s < 4; ++s) bq[s] = lds128(L_B1 + n * 272 + g * 16 + s * 64);
      SB();
      if (__builtin_amdgcn_readfirstlane(bad) != it) break;
      if (lane == 15) tsv += 100;
    }
    {
      f32x4 d = {0.f, 0.f, 0.f, 0.f};
#pragma unroll
      for (int s = 0; s < 4; ++s) d = mfma16(a1[s], bq[s], d);
#pragma unroll
      for (int k = 0; k < 4; ++k) { float t = d[k]; t += dpp<X1>(t); t += dpp<X2>(t); t += dpp<X8>(t); d[k] = t; }
      if ((n & 11) == 0) ST1((v4u{pk(d[0], d[1]), tag, pk(d[2], d[3]), tag}), WS_H3B + ht * 4096 + q4 * 1024 + (n >> 2) * 512 + (4 * v + g) * 16);
    }
    STAMP(10);
    if (wave < 4) {
      v4u e;
      for (int spin = 0; spin < SPIN_CAP; ++spin) {
        e = LD1(WS_H3B + ht * 4096 + v * 1024 + lane * 16);
        asm volatile("" ::: "memory");
        if (__all(e.y == tag && e.w == tag)) break;
      }
      lds64w(L_B2 + ((lane >> 5) * 4 + v) * 272 + (lane & 31) * 8, e.x, e.z);
    }
    bar();
    STAMP(11);
    {
#pragma unroll
      for (int s = 0; s < 4; ++s) bq[s] = lds128(L_B2 + (n & 7) * 272 + g * 16 + s * 64);
      SB();
      f32x4 d = {0.f, 0.f, 0.f, 0.f};
#pragma unroll
      for (int s = 0; s < 4; ++s) d = mfma16(a2[s], bq[s], d);
#pragma unroll
      for (int k = 0; k < 4; ++k) { float t = d[k]; t += dpp<X1>(t); t += dpp<X2>(t); d[k] = t; }
      const bool k1 = (n & 1) != 0;
      if ((n & 10) == 0) ST1((v4u{u_(k1 ? d[2] : d[0]), tag, u_(k1 ? d[3] : d[1]), tag}),
                             WS_H4 + (((q4 * 8 + v) * 2 + (n >> 2)) * 32 + ht) * 128 + g * 32 + (n & 1) * 16);
    }
    STAMP(12);
  } else {
    bar();
    STAMP(8);
    v4u a1[4];
#pragma unroll
    for (int s = 0; s < 4; ++s) a1[s] = ldstr(L_W1 + ab + s * 8192, L_W1 + ab + s * 8192 + 1024);
    v4u e, st;
    for (int spin = 0; spin < SPIN_CAP; ++spin) {
      e = LD1(WS_BIN + ht * 9216 + v * 1024 + lane * 16);
      st = LD1(WS_BIN + ht * 9216 + 8192 + n * 16);
      asm volatile("" ::: "memory");
      if (__all(e.y == tag && e.w == tag && st.y == tag && st.w == tag)) break;
    }
    STAMP(9);
    lds64w(L_B1 + (2 * v + (lane >> 5)) * 272 + (lane & 31) * 8, e.x, e.z);
    float M = f_(st.x);
    M = fmaxf(M, dpp<X1>(M)); M = fmaxf(M, dpp<X2>(M)); M = fmaxf(M, dpp<X7>(M));
    const float ee = __builtin_amdgcn_exp2f(f_(st.x) - M);
    float S = ee * f_(st.z);
    S += dpp<X1>(S); S += dpp<X2>(S); S += dpp<X7>(S);
    const float wt = ee / S;
    bar();
    {
      v4u bq[4];
#pragma unroll
      for (int s = 0; s < 4; ++s) bq[s] = lds128(L_B1 + n * 272 + g * 16 + s * 64);
      SB();
      f32x4 d = {0.f, 0.f, 0.f, 0.f};
#pragma unroll
      for (int s = 0; s < 4; ++s) d = mfma16(a1[s], bq[s], d);
#pragma unroll
      for (int k = 0; k < 4; ++k) { float t = d[k] * wt; t += dpp<X1>(t); t += dpp<X2>(t); t += dpp<X7>(t); d[k] = t; }
      const bool k1 = (n & 1) != 0;
      if ((n & 6) == 0) ST1((v4u{u_(k1 ? d[2] : d[0]), tag, u_(k1 ? d[3] : d[1]), tag}),
                            WS_H4 + 262144 + (((q4 * 8 + v) * 2 + (n >> 3)) * 32 + ht) * 128 + g * 32 + (n & 1) * 16);
    }
    STAMP(12);
  }
  const u32 rho = x * 8 + l16;
  if (wave == 0 && l16 < 8) {
    const u32 part = isA ? 0u : 1u;
    v4u e[4];
    for (int spin = 0; spin < SPIN_CAP; ++spin) {
      bool ok = true;
#pragma unroll
      for (int i = 0; i < 4; ++i) e[i] = LD1(WS_H4 + part * 262144 + rho * 4096 + i * 1024 + lane * 16);
      asm volatile("" ::: "memory");
#pragma unroll
      for (int i = 0; i < 4; ++i) ok = ok && e[i].y == tag && e[i].w == tag;
      if (__all(ok)) break;
    }
    float s0 = (f_(e[0].x) + f_(e[1].x)) + (f_(e[2].x) + f_(e[3].x)), s1 = (f_(e[0].z) + f_(e[1].z)) + (f_(e[2].z) + f_(e[3].z));
    s0 += dpp<X8>(s0); s1 += dpp<X8>(s1);
    s0 = sum32(sum16(s0)); s1 = sum32(sum16(s1));
    if (lane < 8) *(GAS u32*)(out + (rho & 1) * 1024 + part * 512 + (rho >> 1) * 16 + 2 * lane) = pk(s0, s1);
    if (gid == 0 && lane == 0) __builtin_amdgcn_raw_buffer_store_b32(tag, rs, WS_EPOCH, 0, 16);
    STAMP(13);
  }
  {
    const __amdgpu_buffer_rsrc_t rs2 = __builtin_amdgcn_make_buffer_rsrc((void*)(ws + WS_STAMP), 0, 1 << 20, 0x00020000);
    if (lane < 16) __builtin_amdgcn_raw_buffer_store_b64(v2u{tsv, tag}, rs2, (gid * 8 + wave) * 128 + lane * 8, 0, 16);
    if (gid == 0 && wave == 0) {
      u32 st[4], sv[4];
      for (int spin = 0; spin < SPIN_CAP; ++spin) {
        bool ok = true;
#pragma unroll
        for (int i = 0; i < 4; ++i) {
          const v2u a = __builtin_amdgcn_raw_buffer_load_b64(rs2, ((lane + 64 * i) * 8 + 0) * 128, 0, 16);
          const v2u b = __builtin_amdgcn_raw_buffer_load_b64(rs2, ((lane + 64 * i) * 8 + XW) * 128 + XS * 8, 0, 16);
          st[i] = a.x; sv[i] = b.x; ok = ok && a.y == tag && b.y == tag;
        }
        asm volatile("" ::: "memory");
        if (__all(ok)) break;
      }
      const u32 base = __builtin_amdgcn_readfirstlane(st[0]) - 100000u;
      u32 mn = 0xffffffffu, mxs = 0, mx = 0, sum = 0, cnt = 0;
#pragma unroll
      for (int i = 0; i < 4; ++i) {
        const u32 gg = lane + 64 * i; const bool sel = ((((gg >> 3) & 1) == XT) && sv[i] != 0);
        mn = min(mn, st[i] - base); mxs = max(mxs, st[i] - base);
        if (sel) { mx = max(mx, sv[i] - base); sum += sv[i] - base; cnt += 1; }
      }
      for (int o = 32; o > 0; o >>= 1) { mn = min(mn, (u32)__shfl_xor(mn, o)); mxs = max(mxs, (u32)__shfl_xor(mxs, o)); mx = max(mx, (u32)__shfl_xor(mx, o)); sum += __shfl_xor(sum, o); cnt += __shfl_xor(cnt, o); }
      u32 X = XMODE == 0 ? 0u : XMODE == 1 ? mxs - mn : XMODE == 2 ? mx - mxs : sum / cnt - mxs + 200;
      X = __builtin_amdgcn_readfirstlane(X);
      const u64 t0 = __builtin_amdgcn_s_memrealtime();
      while (__builtin_amdgcn_s_memrealtime() - t0 < (u64)X * XK) { }
    }
  }
}

extern "C" void kernel_launch(void* const* d_in, const int* in_sizes, int n_in, void* d_out, int out_size, void* d_ws, size_t ws_size,
                              hipStream_t stream) {
  (void)hipFuncSetAttribute((const void*)fused_decode, hipFuncAttributeMaxDynamicSharedMemorySize, 163840);
  fused_decode<<<256, 512, 163840, stream>>>((const u16*)d_in[0], (const u16*)d_in[2], (const u16*)d_in[3], (unsigned char*)d_ws,
                                             (const u16*)d_in[1], (const u16*)d_in[7], (const u16*)d_in[8], (const u16*)d_in[6],
                                             (const u16*)d_in[9], (const u16*)d_in[4], (const u16*)d_in[5], (const u16*)d_in[10],
                                             (u16*)d_out);
}
```

```cpp
#define ZG 256
#define ZT 512
#define ZL 0
#define ZV 0
#define ZARGS 1
#define STAMPS
#define XMODE 0
#define XT 0
#define XW 0
#define XS 0
#define XK 0
#define YMODE 1
#define YK 10
#include <hip/hip_runtime.h>
#include <hip/hip_bf16.h>

typedef unsigned u32;
typedef unsigned short u16;
typedef unsigned long long u64;
typedef u32 v4u __attribute__((ext_vector_type(4)));
typedef u32 v2u __attribute__((ext_vector_type(2)));
typedef short s16x4 __attribute__((ext_vector_type(4)));
typedef short bf16x8 __attribute__((ext_vector_type(8)));
typedef __bf16 bf16x2 __attribute__((ext_vector_type(2)));
typedef float f32x4 __attribute__((ext_vector_type(4)));
typedef float f32x16 __attribute__((ext_vector_type(16)));
#define GAS __attribute__((address_space(1)))
#define LAS __attribute__((address_space(3)))
#define DI __device__ __forceinline__
#define SB() __builtin_amdgcn_sched_barrier(0)

#define WIN0 32
#define WINA0 16
#define NA0 2
#define WINA 44
#define WINB 32
#define ORDER 0
#define QW 8
#define CODE_BYTES 24576

constexpr u32 WS_EPOCH = 0;
constexpr u32 WS_H1 = 1835008;
constexpr u32 WS_QABS = 65536;
constexpr u32 WS_CHK = 131072;
constexpr u32 WS_QF0 = 139264;
constexpr u32 WS_QB = 1048576 + 65536;
constexpr u32 WS_ASTAT = 262144;
constexpr u32 WS_H3B = 524288;
constexpr u32 WS_BIN = 786432;
constexpr u32 WS_H4 = 1310720;
constexpr u32 WS_APART = 2097152;
constexpr u32 WS_BYTES = 6291456;
constexpr u32 WS_STAMP = 8388608;

constexpr u32 L_C = 0, L_K = 0, L_V = 65536, L_WQ = 0, L_UK = 32768, L_WQB = 98304;
constexpr u32 L_Q = 131072;
constexpr u32 L_X = L_Q + 16384, L_QL = L_Q + 18432, L_QBL = L_Q + 22528, L_PB = L_Q + 24576;
constexpr u32 L_P = L_Q, L_SCR = L_Q + 8192, L_LSUM = L_Q + 28672;
constexpr u32 L_MAXX = L_Q + 31744, L_SYNC = L_MAXX + 512;
constexpr u32 L_W1 = 0, L_W2 = 32768, L_WGT = 65536, L_CK = 66048, L_B1 = 67584, L_B2 = 73728;
enum { S_PUB = 0, S_WQ, S_UK, S_WQB, S_QBISS, S_QISS, S_BAD, S_QGO, S_QST, S_LAND, S_MAX, S_P, S_PV, S_LGO, S_P2DONE, S_QBRDY, S_VLAND, S_N };

constexpr float KSC = 0.08838834764831845f * 1.4426950408889634f;
constexpr int SPIN_CAP = 1 << 15;

#define STAMP(i) do { u64 t_; SB(); asm volatile("s_memrealtime %0\n\ts_waitcnt lgkmcnt(0)" : "=s"(t_) :: "memory"); SB(); if (lane == (i)) tsv = (u32)t_; } while (0)

DI void dma16(const void* sbase, u32 voff, u32 lds_dst) {
  u32 keep;
  asm volatile("s_mov_b32 %0, m0\n\ts_mov_b32 m0, %3\n\ts_nop 0\n\tglobal_load_lds_dwordx4 %1, %2 nt\n\ts_mov_b32 m0, %0"
               : "=&s"(keep) : "v"(voff), "s"(sbase), "s"(lds_dst) : "memory");
}
template <int W, u32 LDS> DI void dma4(const void* sb, u32 v0, u32 v1, u32 v2, u32 v3) {
  u32 keep;
  asm volatile("s_mov_b32 %0, m0\n\ts_mov_b32 m0, %6\n\ts_waitcnt vmcnt(%7)\n\t"
               "global_load_lds_dwordx4 %1, %5 nt\n\tglobal_load_lds_dwordx4 %2, %5 offset:1024 nt\n\ts_waitcnt vmcnt(%7)\n\t"
               "global_load_lds_dwordx4 %3, %5 offset:2048 nt\n\tglobal_load_lds_dwordx4 %4, %5 offset:3072 nt\n\ts_mov_b32 m0, %0"
               : "=&s"(keep) : "v"(v0), "v"(v1), "v"(v2), "v"(v3), "s"(sb), "i"(LDS), "i"(W) : "memory");
}
template <int I> struct IC { static constexpr int v = I; };
template <int B, int E, class F> DI void sfor(F&& f) { if constexpr (B < E) { f(IC<B>{}); sfor<B + 1, E>(f); } }
template <int N> DI void vmwait() { asm volatile("s_waitcnt vmcnt(%0)" :: "i"(N) : "memory"); }
DI u32 pk(float a, float b) { bf16x2 v = {(__bf16)a, (__bf16)b}; return __builtin_bit_cast(u32, v); }
DI float blo(u32 x) { return __builtin_bit_cast(float, x << 16); }
DI float bhi(u32 x) { return __builtin_bit_cast(float, x & 0xffff0000u); }
template <class T> DI LAS T* lp(u32 a) { return (LAS T*)(size_t)a; }
DI v4u lds128(u32 a) { return *lp<v4u>(a); }
DI void lds128w(u32 a, v4u v) { *lp<v4u>(a) = v; }
DI void lds64w(u32 a, u32 x, u32 y) { *lp<v2u>(a) = v2u{x, y}; }
DI v4u ldstr(u32 a0, u32 a1) {
  s16x4 lo = __builtin_amdgcn_ds_read_tr16_b64_v4i16(lp<s16x4>(a0));
  s16x4 hi = __builtin_amdgcn_ds_read_tr16_b64_v4i16(lp<s16x4>(a1));
  v2u l = __builtin_bit_cast(v2u, lo), h = __builtin_bit_cast(v2u, hi);
  return v4u{l.x, l.y, h.x, h.y};
}
DI f32x4 mfma16(v4u a, v4u b, f32x4 c) { return __builtin_amdgcn_mfma_f32_16x16x32_bf16(__builtin_bit_cast(bf16x8, a), __builtin_bit_cast(bf16x8, b), c, 0, 0, 0); }
DI f32x16 mfma32(v4u a, v4u b, f32x16 c) { return __builtin_amdgcn_mfma_f32_32x32x16_bf16(__builtin_bit_cast(bf16x8, a), __builtin_bit_cast(bf16x8, b), c, 0, 0, 0); }
template <int CTRL> DI u32 dppu(u32 x) { return (u32)__builtin_amdgcn_mov_dpp((int)x, CTRL, 0xf, 0xf, true); }
DI float f_(u32 x) { return __builtin_bit_cast(float, x); }
DI u32 u_(float x) { return __builtin_bit_cast(u32, x); }
template <int CTRL> DI float dpp(float x) { return f_(dppu<CTRL>(u_(x))); }
constexpr int X1 = 0xB1, X2 = 0x4E, X7 = 0x141, X8 = 0x128;
DI float sum32(float x) { auto t = __builtin_amdgcn_permlane32_swap(u_(x), u_(x), false, false); return f_(t[0]) + f_(t[1]); }
DI float max32(float x) { auto t = __builtin_amdgcn_permlane32_swap(u_(x), u_(x), false, false); return fmaxf(f_(t[0]), f_(t[1])); }
DI float sum16(float x) { auto t = __builtin_amdgcn_permlane16_swap(u_(x), u_(x), false, false); return f_(t[0]) + f_(t[1]); }
DI float max16(float x) { auto t = __builtin_amdgcn_permlane16_swap(u_(x), u_(x), false, false); return fmaxf(f_(t[0]), f_(t[1])); }
DI void sig(int word, int lane) {
  asm volatile("" ::: "memory");
  if (lane == 0) __hip_atomic_fetch_add(lp<u32>(L_SYNC + 4 * word), 1u, __ATOMIC_RELAXED, __HIP_MEMORY_SCOPE_WORKGROUP);
  asm volatile("" ::: "memory");
}
template <bool SLEEP> DI void waitw(int word, u32 n) {
  while ((u32)__builtin_amdgcn_readfirstlane(__hip_atomic_load(lp<u32>(L_SYNC + 4 * word), __ATOMIC_RELAXED, __HIP_MEMORY_SCOPE_WORKGROUP)) < n) {
    if (SLEEP) __builtin_amdgcn_s_sleep(2);
  }
  asm volatile("" ::: "memory");
}
DI void bar() { asm volatile("s_waitcnt lgkmcnt(0)\n\ts_barrier" ::: "memory"); }
#define LD1(off) __builtin_amdgcn_raw_buffer_load_b128(rs, (off), 0, 16)
#define ST1(v, off) __builtin_amdgcn_raw_buffer_store_b128((v), rs, (off), 0, 16)
#define TAGLOAD() __builtin_amdgcn_raw_buffer_load_b32(rs, WS_EPOCH, 0, 16)
DI v4u ldnt(const void* p, u32 off) { return __builtin_nontemporal_load((const GAS v4u*)((const GAS unsigned char*)p + off)); }
DI void stage_w(u32 base, const v4u (&park)[32], u32 g, u32 n) {
#pragma unroll
  for (int i = 0; i < 32; ++i) lds128w(base + g * 256 + i * 1024 + 16 * (n ^ ((g | (((i >> 1) & 1) << 2)) << 1)), park[i]);
}

__global__ void __launch_bounds__(512, 2)
fused_decode(const u16* __restrict__ xA, const u16* __restrict__ wqA, const u16* __restrict__ wuk, unsigned char* ws,
             const u16* __restrict__ ckv, const u16* __restrict__ kc, const u16* __restrict__ vc, const u16* __restrict__ xB,
             const u16* __restrict__ wqB, const u16* __restrict__ wuv, const u16* __restrict__ woA, const u16* __restrict__ woB,
             u16* __restrict__ out) {
  u64 pc0; asm volatile("s_getpc_b64 %0" : "=s"(pc0));
  const int tid = threadIdx.x, lane = tid & 63, wave = __builtin_amdgcn_readfirstlane(tid >> 6);
  u32 tsv = 0;
  STAMP(0);
  const u32 gid = blockIdx.x, x = gid & 7, r = gid >> 3;
  const bool isA = (r & 1) == 0;
  const u32 l16 = r >> 1;
  const u32 ht = 4 * x + (l16 >> 2), q4 = l16 & 3;
  const u32 g = lane >> 4, n = lane & 15, tq = n >> 2, tp = lane & 3;
  const __amdgpu_buffer_rsrc_t rs = __builtin_amdgcn_make_buffer_rsrc((void*)ws, 0, WS_BYTES, 0x00020000);
  u32 tag;

  if (wave == 4) {
    bar();
    tag = __builtin_amdgcn_readfirstlane(TAGLOAD()) + 1;
  } else if (wave == 5) {
    __builtin_amdgcn_s_setprio(1);
#define SIG_AT(ISSUED, W, NEED, WORD) if constexpr ((ISSUED) - (W) >= (NEED) && (ISSUED) - 4 - (W) < (NEED)) { vmwait<W>(); sig(WORD, lane); }
    const u32 rw = lane >> 3, sw = ((rw >> 1) & 1) << 1;
    const u32 ce = ((lane & 7) ^ sw) * 16, cd = ((lane & 7) ^ sw ^ 4) * 16 + 64512;
    if (isA) {
      dma16(xB, lane * 16, L_X);
      dma16(xB, lane * 16 + 1024, L_X + 1024);
      const u32 vb = (256 * (l16 >> 3) + rw) * 8192 + x * 1024 + (l16 & 7) * 128;
      sfor<0, 8>([&](auto I) {
        constexpr int G = I.v;
        dma4<WINA0, L_WQB + G * 4096>(wqB, vb + ce + G * 262144, vb + cd + G * 262144, vb + ce + G * 262144 + 129024, vb + cd + G * 262144 + 129024);
        if constexpr (G == 1) bar();
      });
      const unsigned char* src = (const unsigned char*)ckv + (size_t)(x * 16 + l16) * 131072;
      u32 cv[16];
#pragma unroll
      for (int i = 0; i < 16; ++i) cv[i] = (lane ^ (((i & 3) << 2) | (i >> 2))) * 16;
      sfor<0, 32>([&](auto I) {
        constexpr int G = I.v;
        if constexpr (G == 24) waitw<true>(S_P2DONE, 4);
        dma4<(G < NA0 ? WINA0 : WINA), L_C + G * 4096>(src + G * 4096, cv[(4 * G) & 15], cv[(4 * G + 1) & 15], cv[(4 * G + 2) & 15], cv[(4 * G + 3) & 15]);
        if constexpr (G + 1 == NA0) { vmwait<4 * NA0>(); sig(S_WQB, lane); }
      });
      STAMP(1);
      if constexpr (ORDER == 0) { vmwait<QW>(); sig(S_QGO, lane); vmwait<0>(); sig(S_LAND, lane); STAMP(2); waitw<false>(S_QISS, 4); }
      v4u park[32];
      const u32 vo = (128 * q4 + g) * 8192 + ht * 256 + n * 16;
      sfor<0, 8>([&](auto I) {
        constexpr int G = I.v;
        vmwait<WINA>(); SB();
        park[4 * G] = ldnt(wuv, vo + (4 * G) * 32768); park[4 * G + 1] = ldnt(wuv, vo + (4 * G + 1) * 32768);
        SB(); vmwait<WINA>(); SB();
        park[4 * G + 2] = ldnt(wuv, vo + (4 * G + 2) * 32768); park[4 * G + 3] = ldnt(wuv, vo + (4 * G + 3) * 32768);
        SB();
      });
      if constexpr (ORDER != 1) sig(S_LGO, lane);
      STAMP(3);
      if constexpr (ORDER != 0) { vmwait<32>(); sig(S_LAND, lane); STAMP(2); }
      if constexpr (ORDER == 1) { vmwait<QW>(); sig(S_QGO, lane); }
      waitw<true>(S_PV, 4);
      const u32 tv = TAGLOAD();
      stage_w(L_W1, park, g, n);
      tag = __builtin_amdgcn_readfirstlane(tv) + 1;
    } else {
      dma16(xA, lane * 16, L_X);
      dma16(xA, lane * 16 + 1024, L_X + 1024);
      const u32 va = (256 * (q4 >> 1) + rw) * 8192 + ht * 256 + (q4 & 1) * 128;
      sfor<0, 8>([&](auto I) {
        constexpr int G = I.v;
        dma4<63, L_WQ + G * 4096>(wqA, va + ce + G * 262144, va + cd + G * 262144, va + ce + G * 262144 + 129024, va + cd + G * 262144 + 129024);
      });
      STAMP(15);
      bar();
      const u32 uo = (128 * q4 + g) * 8192 + ht * 256;
      sfor<0, 8>([&](auto I) {
        constexpr int G = I.v;
        const u32 u0 = uo + G * 131072 + ((n ^ (g | (((2 * G) & 3) << 2))) * 16), u1 = uo + G * 131072 + ((n ^ (g | (((2 * G + 1) & 3) << 2))) * 16);
        dma4<WIN0, L_UK + G * 4096>(wuk, u0, u0 + 31744, u1 + 63488, u1 + 95232);
        SIG_AT(38 + 4 * G, WIN0, 34, S_WQ)
      });
      STAMP(14);
      vmwait<0>(); sig(S_UK, lane);
      tag = __builtin_amdgcn_readfirstlane(TAGLOAD()) + 1;
      waitw<false>(S_QBISS, 1);
      STAMP(13);
      const size_t base = (((size_t)(l16 >> 3) * 8 + x) * 8192 + 6144 + (l16 & 7) * 256) * 256;
      const unsigned char* ks = (const unsigned char*)kc + base;
      const unsigned char* vs = (const unsigned char*)vc + base;
      {
        u32 kv[4];
#pragma unroll
        for (int i = 0; i < 4; ++i) kv[i] = g * 256 + ((n ^ (4 * i + g)) * 16);
        sfor<0, 16>([&](auto I) { constexpr int G = I.v; dma4<WINB, L_K + G * 4096>(ks + G * 4096, kv[0], kv[1], kv[2], kv[3]); });
      }
      STAMP(1);
      {
        const u32 v0 = g * 256 + ((n ^ (g << 1)) * 16), v1 = g * 256 + ((n ^ ((4 + g) << 1)) * 16);
        sfor<0, 16>([&](auto I) {
          constexpr int G = I.v;
          dma4<WINB, L_V + G * 4096>(vs + G * 4096, v0, v1, v0, v1);
          SIG_AT(4 + 4 * G, WINB, 0, S_LAND)
          SIG_AT(4 + 4 * G, WINB, 16, S_VLAND)
          SIG_AT(4 + 4 * G, WINB, 32, S_VLAND)
          SIG_AT(4 + 4 * G, WINB, 48, S_VLAND)
        });
      }
      STAMP(2);
      waitw<false>(S_P, 4);
      {
        const u32 vo = (ht * 128 + g) * 1024 + q4 * 256, w0 = vo + ((n ^ (g << 1)) * 16), w1 = vo + ((n ^ ((g | 4) << 1)) * 16) + 6144;
        sfor<0, 8>([&](auto I) {
          constexpr int G = I.v;
          dma4<WINB, L_W1 + G * 4096>(woB, w0 + G * 16384, w0 + G * 16384 + 3072, w1 + G * 16384, w1 + G * 16384 + 3072);
          SIG_AT(68 + 4 * G, WINB, 48, S_VLAND)
          SIG_AT(68 + 4 * G, WINB, 64, S_VLAND)
        });
      }
      STAMP(3);
      vmwait<0>();
    }
    STAMP(4);
  } else if (wave == 6) {
    bar();
    const u32 tv = TAGLOAD();
    if (isA) {
      __builtin_amdgcn_s_setprio(1);
      v4u park[32];
      if constexpr (ORDER == 1) waitw<true>(S_QISS, 4); else waitw<true>(S_LGO, 1);
      const u32 vo = (ht * 128 + g) * 1024 + q4 * 256 + n * 16;
#pragma unroll
      for (int i = 0; i < 32; i += 2) {
        park[i] = ldnt(woA, vo + i * 4096); park[i + 1] = ldnt(woA, vo + (i + 1) * 4096);
        SB();
      }
      if constexpr (ORDER == 2) { vmwait<QW>(); sig(S_QGO, lane); }
      STAMP(3);
      waitw<true>(S_PV, 4);
      tag = __builtin_amdgcn_readfirstlane(tv) + 1;
      stage_w(L_W2, park, g, n);
      STAMP(4);
    } else {
      __builtin_amdgcn_s_setprio(2);
      tag = __builtin_amdgcn_readfirstlane(tv) + 1;
      waitw<false>(S_PUB, 4);
      STAMP(2);
      v4u e[8];
      for (int spin = 0; spin < SPIN_CAP; ++spin) {
        bool ok = true;
#pragma unroll
        for (int i = 0; i < 8; ++i) e[i] = LD1(WS_QB + x * 16384 + (l16 >> 3) * 8192 + i * 1024 + lane * 16);
        SB();
        if (spin == 0) sig(S_QBISS, lane);
        asm volatile("" ::: "memory");
#pragma unroll
        for (int i = 0; i < 8; ++i) ok = ok && e[i].y == tag && e[i].w == tag;
        if (__all(ok)) break;
      }
#pragma unroll
      for (int i = 0; i < 4; ++i) *lp<u32>(L_QBL + i * 256 + lane * 4) = pk(f_(e[i].x) + f_(e[4 + i].x), f_(e[i].z) + f_(e[4 + i].z));
      sig(S_QBRDY, lane);
      STAMP(3);
      __builtin_amdgcn_s_setprio(0);
    }
  } else if (wave == 7) {
    bar();
    const u32 tv = TAGLOAD();
    if (r < 2) {
      const GAS u32* c = (const GAS u32*)(pc0 & ~127ull);
      u32 s = 0;
#pragma unroll
      for (int i = 0; i < (CODE_BYTES / 128 + 127) / 128; ++i) {
        const u32 ln = (2 * i + r) * 64 + lane;
        if (ln < CODE_BYTES / 128) s += c[ln * 32];
      }
      asm volatile("" :: "v"(s));
    }
    if (lane < 6) { u32 w = __builtin_amdgcn_raw_buffer_load_b32(rs, lane * 1048576 + 1048576 - 64, 0, 16); asm volatile("" :: "v"(w)); }
    tag = __builtin_amdgcn_readfirstlane(tv) + 1;
  } else {
    if (wave == 0 && lane < S_N) *lp<u32>(L_SYNC + 4 * lane) = 0;
    bar();
    tag = __builtin_amdgcn_readfirstlane(TAGLOAD()) + 1;
    const u32 w = wave;
    const u32 a0 = (8 * g + tq) * 128 + 16 * ((2 * w + (tp >> 1)) ^ ((((tq >> 1) & 1) | ((g & 1) << 1)) << 1)) + 8 * (tp & 1);
    if (isA) {
      const u32 bs = x * 16 + l16, b = bs >> 6, hh = lane >> 5, hd = lane & 31;
      __builtin_amdgcn_s_setprio(2);
      {
        waitw<false>(S_WQB, 1);
        v4u xf[8], af[8];
#pragma unroll
        for (int s = 0; s < 8; ++s) { af[s] = ldstr(L_WQB + a0 + s * 4096, L_WQB + a0 + s * 4096 + 512); xf[s] = lds128(L_X + (lane & 1) * 1024 + (l16 >> 3) * 512 + g * 16 + s * 64); }
        sig(S_P2DONE, lane);
        SB();
        f32x4 c0 = {0.f, 0.f, 0.f, 0.f}, c1 = c0;
#pragma unroll
        for (int s = 0; s < 8; s += 2) { c0 = mfma16(af[s], xf[s], c0); c1 = mfma16(af[s + 1], xf[s + 1], c1); }
        c0 += c1;
        if (n < 4) ST1((v4u{u_((n & 2) ? c0[2] : c0[0]), tag, u_((n & 2) ? c0[3] : c0[1]), tag}),
                       WS_QB + x * 16384 + (n & 1) * 8192 + l16 * 512 + (8 * w + 2 * g + (n >> 1)) * 16);
        STAMP(13);
      }
      waitw<true>(S_QGO, 1);
      STAMP(14);
      v4u qr[8], z0, z1;
      for (int spin = 0; spin < SPIN_CAP; ++spin) {
#pragma unroll
        for (int i = 0; i < 8; ++i) qr[i] = LD1(WS_QABS + b * 32768 + (8 * w + i) * 1024 + lane * 16);
        const u32 co = WS_CHK + (8 * w + 4 * ((lane >> 1) & 1) + 2 * (lane & 1)) * 128 + (lane >> 2) * 8 + b * 4;
        const u32 ck0 = __builtin_amdgcn_raw_buffer_load_b32(rs, co, 0, 16), ck1 = __builtin_amdgcn_raw_buffer_load_b32(rs, co + 128, 0, 16);
        z0 = LD1(WS_QF0 + b * 2048 + hd * 64 + hh * 32);
        z1 = LD1(WS_QF0 + b * 2048 + hd * 64 + hh * 32 + 16);
        SB();
        if (spin == 0) sig(S_QISS, lane);
        asm volatile("" ::: "memory");
        u32 c8[8];
#pragma unroll
        for (int i = 0; i < 8; ++i) c8[i] = (qr[i].x + qr[i].y) + (qr[i].z + qr[i].w);
        u32 c4[4], c2[2];
#pragma unroll
        for (int i = 0; i < 4; ++i) { const u32 p = c8[i] + dppu<X2>(c8[i]), q = c8[i + 4] + dppu<X2>(c8[i + 4]); c4[i] = (lane & 2) ? q : p; }
#pragma unroll
        for (int i = 0; i < 2; ++i) { const u32 p = c4[i] + dppu<X1>(c4[i]), q = c4[i + 2] + dppu<X1>(c4[i + 2]); c2[i] = (lane & 1) ? q : p; }
        if (__all(c2[0] + tag == ck0 && c2[1] + tag == ck1 && z0.y == tag && z0.w == tag && z1.y == tag && z1.w == tag)) break;
        if (lane == 15) tsv += 1;
      }
      STAMP(1);
      if (lane >= 2) {
#pragma unroll
        for (int i = 0; i < 8; ++i)
          lds128w(L_Q - 1024 + (lane >> 1) * 1024 + ((((lane & 1) * 32 + 8 * w + i) ^ (lane & 7)) * 16), qr[i]);
      }
      sig(S_QST, lane);
      STAMP(2);
      waitw<false>(S_QST, 4);
      waitw<false>(S_LAND, 1);
      STAMP(3);
      f32x16 s;
#pragma unroll
      for (int i = 0; i < 16; ++i) s[i] = 0.f;
      {
        const u32 fr = ((hd & 3) << 2) | ((hd >> 2) & 3);
        const u32 ca = L_C + (32 * w + hd) * 1024 + 16 * (hh ^ fr), qa = L_Q - 1024 + ((lane ^ hh) * 16);
        v4u af[32], bf[32];
        bf[0] = v4u{z0.x, z0.z, z1.x, z1.z};
        constexpr int PD = 6;
#define S_RD(k) do { af[k] = lds128((ca ^ (32 * ((k) & 7))) + 256 * ((k) >> 3)); if ((k) > 0) bf[k] = lds128((qa ^ (32 * ((k) & 3))) + (k) * 1024); } while (0)
#pragma unroll
        for (int k = 0; k < PD; ++k) S_RD(k);
        SB();
#pragma unroll
        for (int k = 0; k < 32; ++k) {
          if (k + PD < 32) S_RD(k + PD);
          s = mfma32(af[k], bf[k], s);
          SB();
        }
      }
      float m = s[0];
#pragma unroll
      for (int i = 1; i < 16; ++i) m = fmaxf(m, s[i]);
      m = max32(m);
      if (hh == 0) *lp<float>(L_MAXX + hd * 16 + w * 4) = m;
      sig(S_MAX, lane);
      STAMP(4);
      waitw<false>(S_MAX, 4);
      const v4u mm = lds128(L_MAXX + hd * 16);
      const float mk = fmaxf(fmaxf(f_(mm.x), f_(mm.y)), fmaxf(f_(mm.z), f_(mm.w))) * KSC;
      float ls = 0.f;
#pragma unroll
      for (int i = 0; i < 16; ++i) { s[i] = __builtin_amdgcn_exp2f(fmaf(s[i], KSC, -mk)); ls += s[i]; }
      lds128w(L_P + (2 * w) * 1024 + lane * 16, v4u{pk(s[0], s[1]), pk(s[2], s[3]), pk(s[4], s[5]), pk(s[6], s[7])});
      lds128w(L_P + (2 * w + 1) * 1024 + lane * 16, v4u{pk(s[8], s[9]), pk(s[10], s[11]), pk(s[12], s[13]), pk(s[14], s[15])});
      ls = sum32(ls);
      if (hh == 0) *lp<float>(L_LSUM + hd * 16 + w * 4) = ls;
      sig(S_P, lane);
      const u32 blk = (lane >> 4) & 1;
      u32 ta[2];
#pragma unroll
      for (int t = 0; t < 2; ++t)
        ta[t] = L_C + (4 * hh + 8 * t + tq) * 1024 + 256 * w + 16 * ((2 * blk + (tp >> 1)) ^ ((tq << 2) | (hh + 2 * t))) + 8 * (tp & 1);
      v4u cf[32];
      constexpr int PV_PD = 5;
#define PV_RD(kk) cf[kk] = ldstr((ta[0] ^ (64 * ((kk) >> 3))) + ((kk) & 7) * 16384, (ta[1] ^ (64 * ((kk) >> 3))) + ((kk) & 7) * 16384)
#pragma unroll
      for (int k = 0; k < PV_PD; ++k) PV_RD(k);
      SB();
      waitw<false>(S_P, 4);
      STAMP(5);
      v4u pf[8];
#pragma unroll
      for (int k = 0; k < 8; ++k) pf[k] = lds128(L_P + k * 1024 + lane * 16);
      const v4u lv = lds128(L_LSUM + hd * 16);
      SB();
      const u32 scw = L_SCR + w * 4096 + hd * 64 + 8 * hh, scx = 16 * ((hd >> 1) & 3);
      const u32 scr = L_SCR + w * 4096 + (lane >> 2) * 64 + 16 * (tp ^ ((lane >> 3) & 3));
      const u32 sto = WS_APART + (((lane >> 2) * 4 + w) * 128 + bs) * 256 + 16 * tp;
      f32x16 o[4];
      v4u ov[2];
      u32 cs = tag;
#define EPI_W(c) _Pragma("unroll") for (int gg = 0; gg < 4; ++gg) { \
        const u32 p0 = pk(o[c][4 * gg], o[c][4 * gg + 1]), p1 = pk(o[c][4 * gg + 2], o[c][4 * gg + 3]); \
        lds64w(scw + ((c) & 1) * 2048 + ((16 * gg) ^ scx), p0, p1); cs += p0 + p1; }
#define EPI_R(c) do { ov[0] = lds128(scr + ((c) & 1) * 2048); ov[1] = lds128(scr + ((c) & 1) * 2048 + 1024); } while (0)
#define EPI_S(c) do { ST1(ov[0], sto + 64 * (c)); ST1(ov[1], sto + 64 * (c) + 16 * 4 * 128 * 256); } while (0)
#pragma unroll
      for (int kk = 0; kk < 32; ++kk) {
        const int c = kk >> 3, k = kk & 7;
        if (kk + PV_PD < 32) PV_RD(kk + PV_PD);
        if (k == 0) {
          f32x16 z;
#pragma unroll
          for (int i = 0; i < 16; ++i) z[i] = 0.f;
          o[c] = mfma32(cf[kk], pf[k], z);
        } else o[c] = mfma32(cf[kk], pf[k], o[c]);
        if (c > 0 && k == 1) { EPI_W(c - 1) EPI_R(c - 1); }
        if (c > 0 && k == 5) { EPI_S(c - 1); }
        SB();
      }
      sig(S_PV, lane);
      EPI_W(3) EPI_R(3);
      const float lsum = (f_(lv.x) + f_(lv.y)) + (f_(lv.z) + f_(lv.w));
      { auto t = __builtin_amdgcn_permlane32_swap(cs, cs, false, false); cs = t[0] + t[1] - tag + u_(mk) + u_(lsum); }
      EPI_S(3);
      if (hh == 0) ST1((v4u{u_(mk), u_(lsum), cs, tag}), WS_ASTAT + ((hd * 4 + w) * 128 + bs) * 16);
      STAMP(7);
    } else {
      __builtin_amdgcn_s_setprio(2);
      {
        waitw<false>(S_WQ, 1);
        STAMP(10);
        v4u xf[8], af[8];
#pragma unroll
        for (int s = 0; s < 8; ++s) { af[s] = ldstr(L_WQ + a0 + s * 4096, L_WQ + a0 + s * 4096 + 512); xf[s] = lds128(L_X + (lane & 1) * 1024 + (q4 >> 1) * 512 + g * 16 + s * 64); }
        SB();
        f32x4 c0 = {0.f, 0.f, 0.f, 0.f}, c1 = c0;
#pragma unroll
        for (int s = 0; s < 8; s += 2) { c0 = mfma16(af[s], xf[s], c0); c1 = mfma16(af[s + 1], xf[s + 1], c1); }
        c0 += c1;
        if (n < 4) ST1((v4u{u_((n & 2) ? c0[2] : c0[0]), tag, u_((n & 2) ? c0[3] : c0[1]), tag}),
                       WS_H1 + ht * 4096 + q4 * 1024 + (n & 1) * 512 + (8 * w + 2 * g + (n >> 1)) * 16);
        STAMP(11);
        waitw<false>(S_UK, 1);
        v4u uk[8];
#pragma unroll
        for (int t = 0; t < 2; ++t)
#pragma unroll
          for (int s = 0; s < 4; ++s) uk[t * 4 + s] = lds128(L_UK + (32 * w + 8 * (n >> 2) + 4 * t + (n & 3)) * 256 + 16 * ((4 * s + g) ^ n));
        v4u e[4];
        for (int spin = 0; spin < SPIN_CAP; ++spin) {
          bool ok = true;
#pragma unroll
          for (int i = 0; i < 4; ++i) e[i] = LD1(WS_H1 + ht * 4096 + i * 1024 + lane * 16);
          asm volatile("" ::: "memory");
#pragma unroll
          for (int i = 0; i < 4; ++i) ok = ok && e[i].y == tag && e[i].w == tag;
          if (__all(ok)) break;
        }
        STAMP(14);
        const u32 ql = L_QL + w * 512;
#pragma unroll
        for (int d = 0; d < 2; ++d)
          *lp<u32>(ql + (lane >> 5) * 256 + d * 128 + (lane & 31) * 4) = pk(f_(e[d].x) + f_(e[2 + d].x), f_(e[d].z) + f_(e[2 + d].z));
        v4u qb[4];
#pragma unroll
        for (int s = 0; s < 4; ++s) qb[s] = lds128(ql + (lane & 1) * 256 + g * 16 + s * 64);
        SB();
        f32x4 u0 = {0.f, 0.f, 0.f, 0.f}, u1 = u0;
#pragma unroll
        for (int s = 0; s < 4; ++s) { u0 = mfma16(uk[s], qb[s], u0); u1 = mfma16(uk[4 + s], qb[s], u1); }
        const v4u pv = {pk(u0[0], u0[1]), pk(u0[2], u0[3]), pk(u1[0], u1[1]), pk(u1[2], u1[3])};
        if (n < 2) ST1(pv, WS_QABS + n * 32768 + ht * 1024 + q4 * 256 + w * 64 + g * 16);
        if (q4 == 0 && w == 0 && n < 2 && g < 2) {
          ST1((v4u{pv.x, tag, pv.y, tag}), WS_QF0 + n * 2048 + ht * 64 + g * 32);
          ST1((v4u{pv.z, tag, pv.w, tag}), WS_QF0 + n * 2048 + ht * 64 + g * 32 + 16);
        }
        u32 cs = (pv.x + pv.y) + (pv.z + pv.w);
        { auto t = __builtin_amdgcn_permlane16_swap(cs, cs, false, false); cs = t[0] + t[1]; }
        { auto t = __builtin_amdgcn_permlane32_swap(cs, cs, false, false); cs = t[0] + t[1]; }
        cs += tag;
        const u32 cs1 = dppu<X1>(cs);
        if (lane == 0) __builtin_amdgcn_raw_buffer_store_b64(v2u{cs, cs1}, rs, WS_CHK + ht * 128 + (4 * q4 + w) * 8, 0, 16);
        sig(S_PUB, lane);
        STAMP(15);
      }
      waitw<true>(S_QBRDY, 1);
      v4u qf[4];
#pragma unroll
      for (int s = 0; s < 4; ++s) qf[s] = lds128(L_QBL + (lane & 3) * 256 + g * 16 + s * 64);
      waitw<true>(S_LAND, 1);
      STAMP(3);
      f32x4 sc[4];
      {
        v4u kf[16];
        const u32 ka = L_K + (64 * w + n) * 256 + 16 * (g ^ n);
#pragma unroll
        for (int i = 0; i < 16; ++i) kf[i] = lds128((ka ^ (64 * (i & 3))) + (i >> 2) * 4096);
        SB();
#pragma unroll
        for (int t = 0; t < 4; ++t) {
          sc[t] = f32x4{0.f, 0.f, 0.f, 0.f};
#pragma unroll
          for (int s = 0; s < 4; ++s) sc[t] = mfma16(kf[4 * t + s], qf[s], sc[t]);
        }
      }
      float m = sc[0][0];
#pragma unroll
      for (int i = 1; i < 16; ++i) m = fmaxf(m, sc[i >> 2][i & 3]);
      m = max32(max16(m));
      if (lane < 4) *lp<float>(L_MAXX + lane * 16 + w * 4) = m;
      sig(S_MAX, lane);
      waitw<false>(S_MAX, 4);
      const v4u mm = lds128(L_MAXX + (lane & 3) * 16);
      const float mk = fmaxf(fmaxf(f_(mm.x), f_(mm.y)), fmaxf(f_(mm.z), f_(mm.w))) * KSC;
      float ls = 0.f;
#pragma unroll
      for (int i = 0; i < 16; ++i) { const float p = __builtin_amdgcn_exp2f(fmaf(sc[i >> 2][i & 3], KSC, -mk)); sc[i >> 2][i & 3] = p; ls += p; }
      ls = sum32(sum16(ls));
      if (n < 4) {
#pragma unroll
        for (int u = 0; u < 2; ++u)
          lds128w(L_PB + (2 * w + u) * 256 + g * 64 + n * 16,
                  v4u{pk(sc[2 * u][0], sc[2 * u][1]), pk(sc[2 * u][2], sc[2 * u][3]), pk(sc[2 * u + 1][0], sc[2 * u + 1][1]), pk(sc[2 * u + 1][2], sc[2 * u + 1][3])});
      }
      if (lane < 4) *lp<float>(L_LSUM + lane * 16 + w * 4) = ls;
      sig(S_P, lane);
      waitw<false>(S_P, 4);
      STAMP(5);
      v4u pf[8];
#pragma unroll
      for (int k = 0; k < 8; ++k) pf[k] = lds128(L_PB + k * 256 + g * 64 + (lane & 3) * 16);
      if (w == 0) {
        const v4u lv = lds128(L_LSUM + (lane & 3) * 16);
        const float lsum = (f_(lv.x) + f_(lv.y)) + (f_(lv.z) + f_(lv.w));
        if (lane < 4) ST1((v4u{u_(mk), tag, u_(lsum), tag}), WS_BIN + (4 * x + lane) * 9216 + 8192 + l16 * 16);
      }
      f32x4 o0 = {0.f, 0.f, 0.f, 0.f}, o1 = o0;
      const u32 va = L_V + (4 * g + tq) * 256 + 16 * ((4 * w + (tp >> 1)) ^ ((4 * (g & 1) + tq) << 1)) + 8 * (tp & 1);
#pragma unroll
      for (int k2 = 0; k2 < 4; ++k2) {
        waitw<false>(S_VLAND, k2 + 1);
        v4u vf[4];
#pragma unroll
        for (int i = 0; i < 4; ++i) { const u32 a = (va ^ (32 * (i & 1))) + (2 * k2 + (i >> 1)) * 8192; vf[i] = ldstr(a, a + 4096); }
        SB();
        o0 = mfma16(vf[0], pf[2 * k2], o0); o1 = mfma16(vf[1], pf[2 * k2], o1);
        o0 = mfma16(vf[2], pf[2 * k2 + 1], o0); o1 = mfma16(vf[3], pf[2 * k2 + 1], o1);
      }
      STAMP(6);
      const f32x4 o = (n & 4) ? o1 : o0;
      if (n < 8) ST1((v4u{pk(o[0], o[1]), tag, pk(o[2], o[3]), tag}), WS_BIN + (4 * x + (n & 3)) * 9216 + l16 * 512 + (4 * (2 * w + (n >> 2)) + g) * 16);
      STAMP(7);
    }
  }

  const u32 v = wave;
  const u32 ab = (8 * g + tq) * 256 + 16 * ((2 * v + (tp >> 1)) ^ ((tq | ((g & 1) << 2)) << 1)) + 8 * (tp & 1);
  if (isA) {
    v4u a1[4], a2[4], bq[4];
    if (wave == 7) waitw<true>(S_PV, 4);
    for (u32 it = 1; it < 64; ++it) {
      v4u st0, st1;
      if (wave == 7) {
        for (int spin = 0; spin < SPIN_CAP; ++spin) {
          st0 = LD1(WS_ASTAT + (ht * 4 + q4) * 2048 + lane * 16);
          st1 = LD1(WS_ASTAT + (ht * 4 + q4) * 2048 + 1024 + lane * 16);
          asm volatile("" ::: "memory");
          if (__all(st0.w == tag && st1.w == tag)) break;
        }
        STAMP(2);
      }
      bar();
      STAMP(8);
      v4u pd[4];
#pragma unroll
      for (int i = 0; i < 4; ++i) pd[i] = LD1(WS_APART + (ht * 4 + q4) * 32768 + v * 4096 + i * 1024 + lane * 16);
      SB();
      if (wave == 7) {
#pragma unroll
        for (int i = 0; i < 2; ++i) {
          const v4u st = i ? st1 : st0;
          float M = f_(st.x);
          M = fmaxf(M, dpp<X1>(M)); M = fmaxf(M, dpp<X2>(M)); M = fmaxf(M, dpp<X7>(M)); M = fmaxf(M, dpp<X8>(M)); M = max16(M); M = max32(M);
          const float e = __builtin_amdgcn_exp2f(f_(st.x) - M);
          float S = e * f_(st.y);
          S += dpp<X1>(S); S += dpp<X2>(S); S += dpp<X7>(S); S += dpp<X8>(S); S = sum16(S); S = sum32(S);
          *lp<float>(L_WGT + i * 256 + lane * 4) = e / S;
          *lp<u32>(L_CK + i * 256 + lane * 4) = st.z - tag - st.x - st.y;
        }
      }
#pragma unroll
      for (int s = 0; s < 4; ++s) { a1[s] = ldstr(L_W1 + ab + s * 8192, L_W1 + ab + s * 8192 + 1024); a2[s] = ldstr(L_W2 + ab + s * 8192, L_W2 + ab + s * 8192 + 1024); }
      bar();
      float acc[8], wt[4];
#pragma unroll
      for (int i = 0; i < 4; ++i) wt[i] = *lp<float>(L_WGT + (v * 16 + 4 * i + g) * 4);
      const u32 ck = *lp<u32>(L_CK + (v * 16 + 4 * (lane & 3) + g) * 4);
#pragma unroll
      for (int k = 0; k < 4; ++k) { acc[2 * k] = wt[0] * blo(pd[0][k]); acc[2 * k + 1] = wt[0] * bhi(pd[0][k]); }
#pragma unroll
      for (int i = 1; i < 4; ++i)
#pragma unroll
        for (int k = 0; k < 4; ++k) { acc[2 * k] = fmaf(wt[i], blo(pd[i][k]), acc[2 * k]); acc[2 * k + 1] = fmaf(wt[i], bhi(pd[i][k]), acc[2 * k + 1]); }
      STAMP(9);
      {
        float y[4];
#pragma unroll
        for (int k = 0; k < 4; ++k) { auto t = __builtin_amdgcn_permlane32_swap(u_(acc[k]), u_(acc[k + 4]), false, false); y[k] = f_(t[0]) + f_(t[1]); }
        float z[2];
#pragma unroll
        for (int k = 0; k < 2; ++k) { auto t = __builtin_amdgcn_permlane16_swap(u_(y[k]), u_(y[k + 2]), false, false); z[k] = f_(t[0]) + f_(t[1]); }
        const u32 hi = pk(z[0], z[1]), lo = pk(z[0] - blo(hi), z[1] - bhi(hi));
        *lp<u32>(L_B1 + v * 272 + (8 * n + 2 * g) * 2) = hi;
        *lp<u32>(L_B1 + (8 + v) * 272 + (8 * n + 2 * g) * 2) = lo;
        u32 c4[4], c2[2];
#pragma unroll
        for (int i = 0; i < 4; ++i) c4[i] = (pd[i].x + pd[i].y) + (pd[i].z + pd[i].w);
#pragma unroll
        for (int i = 0; i < 2; ++i) { const u32 p = c4[i] + dppu<X2>(c4[i]), q = c4[i + 2] + dppu<X2>(c4[i + 2]); c2[i] = (lane & 2) ? q : p; }
        const u32 p1 = c2[0] + dppu<X1>(c2[0]), q1 = c2[1] + dppu<X1>(c2[1]);
        u32 c1 = (lane & 1) ? q1 : p1;
        c1 += dppu<X8>(c1);
        c1 += dppu<0x124>(c1);
        if (c1 != ck) *lp<u32>(L_SYNC + 4 * S_BAD) = it;
      }
      bar();
      const u32 bad = *lp<u32>(L_SYNC + 4 * S_BAD);
#pragma unroll
      for (int s = 0; s < 4; ++s) bq[s] = lds128(L_B1 + n * 272 + g * 16 + s * 64);
      SB();
      if (__builtin_amdgcn_readfirstlane(bad) != it) break;
      if (lane == 15) tsv += 100;
    }
    {
      f32x4 d = {0.f, 0.f, 0.f, 0.f};
#pragma unroll
      for (int s = 0; s < 4; ++s) d = mfma16(a1[s], bq[s], d);
#pragma unroll
      for (int k = 0; k < 4; ++k) { float t = d[k]; t += dpp<X1>(t); t += dpp<X2>(t); t += dpp<X8>(t); d[k] = t; }
      if ((n & 11) == 0) ST1((v4u{pk(d[0], d[1]), tag, pk(d[2], d[3]), tag}), WS_H3B + ht * 4096 + q4 * 1024 + (n >> 2) * 512 + (4 * v + g) * 16);
    }
    STAMP(10);
    if (wave < 4) {
      v4u e;
      for (int spin = 0; spin < SPIN_CAP; ++spin) {
        e = LD1(WS_H3B + ht * 4096 + v * 1024 + lane * 16);
        asm volatile("" ::: "memory");
        if (__all(e.y == tag && e.w == tag)) break;
      }
      lds64w(L_B2 + ((lane >> 5) * 4 + v) * 272 + (lane & 31) * 8, e.x, e.z);
    }
    bar();
    STAMP(11);
    {
#pragma unroll
      for (int s = 0; s < 4; ++s) bq[s] = lds128(L_B2 + (n & 7) * 272 + g * 16 + s * 64);
      SB();
      f32x4 d = {0.f, 0.f, 0.f, 0.f};
#pragma unroll
      for (int s = 0; s < 4; ++s) d = mfma16(a2[s], bq[s], d);
#pragma unroll
      for (int k = 0; k < 4; ++k) { float t = d[k]; t += dpp<X1>(t); t += dpp<X2>(t); d[k] = t; }
      const bool k1 = (n & 1) != 0;
      if ((n & 10) == 0) ST1((v4u{u_(k1 ? d[2] : d[0]), tag, u_(k1 ? d[3] : d[1]), tag}),
                             WS_H4 + (((q4 * 8 + v) * 2 + (n >> 2)) * 32 + ht) * 128 + g * 32 + (n & 1) * 16);
    }
    STAMP(12);
  } else {
    bar();
    STAMP(8);
    v4u a1[4];
#pragma unroll
    for (int s = 0; s < 4; ++s) a1[s] = ldstr(L_W1 + ab + s * 8192, L_W1 + ab + s * 8192 + 1024);
    v4u e, st;
    for (int spin = 0; spin < SPIN_CAP; ++spin) {
      e = LD1(WS_BIN + ht * 9216 + v * 1024 + lane * 16);
      st = LD1(WS_BIN + ht * 9216 + 8192 + n * 16);
      asm volatile("" ::: "memory");
      if (__all(e.y == tag && e.w == tag && st.y == tag && st.w == tag)) break;
    }
    STAMP(9);
    lds64w(L_B1 + (2 * v + (lane >> 5)) * 272 + (lane & 31) * 8, e.x, e.z);
    float M = f_(st.x);
    M = fmaxf(M, dpp<X1>(M)); M = fmaxf(M, dpp<X2>(M)); M = fmaxf(M, dpp<X7>(M));
    const float ee = __builtin_amdgcn_exp2f(f_(st.x) - M);
    float S = ee * f_(st.z);
    S += dpp<X1>(S); S += dpp<X2>(S); S += dpp<X7>(S);
    const float wt = ee / S;
    bar();
    {
      v4u bq[4];
#pragma unroll
      for (int s = 0; s < 4; ++s) bq[s] = lds128(L_B1 + n * 272 + g * 16 + s * 64);
      SB();
      f32x4 d = {0.f, 0.f, 0.f, 0.f};
#pragma unroll
      for (int s = 0; s < 4; ++s) d = mfma16(a1[s], bq[s], d);
#pragma unroll
      for (int k = 0; k < 4; ++k) { float t = d[k] * wt; t += dpp<X1>(t); t += dpp<X2>(t); t += dpp<X7>(t); d[k] = t; }
      const bool k1 = (n & 1) != 0;
      if ((n & 6) == 0) ST1((v4u{u_(k1 ? d[2] : d[0]), tag, u_(k1 ? d[3] : d[1]), tag}),
                            WS_H4 + 262144 + (((q4 * 8 + v) * 2 + (n >> 3)) * 32 + ht) * 128 + g * 32 + (n & 1) * 16);
    }
    STAMP(12);
  }
  const u32 rho = x * 8 + l16;
  if (wave == 0 && l16 < 8) {
    const u32 part = isA ? 0u : 1u;
    v4u e[4];
    for (int spin = 0; spin < SPIN_CAP; ++spin) {
      bool ok = true;
#pragma unroll
      for (int i = 0; i < 4; ++i) e[i] = LD1(WS_H4 + part * 262144 + rho * 4096 + i * 1024 + lane * 16);
      asm volatile("" ::: "memory");
#pragma unroll
      for (int i = 0; i < 4; ++i) ok = ok && e[i].y == tag && e[i].w == tag;
      if (__all(ok)) break;
    }
    float s0 = (f_(e[0].x) + f_(e[1].x)) + (f_(e[2].x) + f_(e[3].x)), s1 = (f_(e[0].z) + f_(e[1].z)) + (f_(e[2].z) + f_(e[3].z));
    s0 += dpp<X8>(s0); s1 += dpp<X8>(s1);
    s0 = sum32(sum16(s0)); s1 = sum32(sum16(s1));
    if (lane < 8) *(GAS u32*)(out + (rho & 1) * 1024 + part * 512 + (rho >> 1) * 16 + 2 * lane) = pk(s0, s1);
    if (gid == 0 && lane == 0) __builtin_amdgcn_raw_buffer_store_b32(tag, rs, WS_EPOCH, 0, 16);
    STAMP(13);
  }
  {
    const __amdgpu_buffer_rsrc_t rs2 = __builtin_amdgcn_make_buffer_rsrc((void*)(ws + WS_STAMP), 0, 1 << 20, 0x00020000);
    if (lane < 16) __builtin_amdgcn_raw_buffer_store_b64(v2u{tsv, tag}, rs2, (gid * 8 + wave) * 128 + lane * 8, 0, 16);
    if (gid == 0 && wave == 0) {
      u32 st[4], sv[4];
      for (int spin = 0; spin < SPIN_CAP; ++spin) {
        bool ok = true;
#pragma unroll
        for (int i = 0; i < 4; ++i) {
          const v2u a = __builtin_amdgcn_raw_buffer_load_b64(rs2, ((lane + 64 * i) * 8 + 0) * 128, 0, 16);
          const v2u b = __builtin_amdgcn_raw_buffer_load_b64(rs2, ((lane + 64 * i) * 8 + XW) * 128 + XS * 8, 0, 16);
          st[i] = a.x; sv[i] = b.x; ok = ok && a.y == tag && b.y == tag;
        }
        asm volatile("" ::: "memory");
        if (__all(ok)) break;
      }
      const u32 base = __builtin_amdgcn_readfirstlane(st[0]) - 100000u;
      u32 mn = 0xffffffffu, mxs = 0, mx = 0, sum = 0, cnt = 0;
#pragma unroll
      for (int i = 0; i < 4; ++i) {
        const u32 gg = lane + 64 * i; const bool sel = ((((gg >> 3) & 1) == XT) && sv[i] != 0);
        mn = min(mn, st[i] - base); mxs = max(mxs, st[i] - base);
        if (sel) { mx = max(mx, sv[i] - base); sum += sv[i] - base; cnt += 1; }
      }
      for (int o = 32; o > 0; o >>= 1) { mn = min(mn, (u32)__shfl_xor(mn, o)); mxs = max(mxs, (u32)__shfl_xor(mxs, o)); mx = max(mx, (u32)__shfl_xor(mx, o)); sum += __shfl_xor(sum, o); cnt += __shfl_xor(cnt, o); }
      if (YMODE == 2 || YMODE == 3) { const u32 tk = __builtin_amdgcn_raw_buffer_load_b32(rs2, 900000, 0, 16); mx = (YMODE == 2 ? mn : mxs) + base - tk; }
      if (YMODE == 1 && lane == 0) __builtin_amdgcn_raw_buffer_store_b32((u32)__builtin_amdgcn_s_memrealtime(), rs2, 900000, 0, 16);
      u32 X = XMODE == 9 ? mx : XMODE == 0 ? 0u : XMODE == 1 ? mxs - mn : XMODE == 2 ? mx - mxs : sum / cnt - mxs + 200;
      X = __builtin_amdgcn_readfirstlane(X);
      const u64 t0 = __builtin_amdgcn_s_memrealtime();
      while (__builtin_amdgcn_s_memrealtime() - t0 < (u64)X * XK) { }
    }
  }
}

__global__ void __launch_bounds__(ZT) tiny(unsigned char* ws
  , const void* a1, const void* a2, const void* a3, const void* a4, const void* a5, const void* a6, const void* a7, const void* a8, const void* a9, const void* a10, const void* a11, const void* a12
) {
  const u64 t0 = __builtin_amdgcn_s_memrealtime();
  if (blockIdx.x != 0 || threadIdx.x >= 64) return;
  GAS u32* p = (GAS u32*)(ws + WS_STAMP + 900000);
  const u32 te = __hip_atomic_load(p, __ATOMIC_RELAXED, __HIP_MEMORY_SCOPE_AGENT); const u32 X = (u32)t0 - te;
  const u64 t1 = __builtin_amdgcn_s_memrealtime(); while (__builtin_amdgcn_s_memrealtime() - t1 < (u64)(X > 5000u ? 0u : X) * YK) { }
}
extern "C" void kernel_launch(void* const* d_in, const int* in_sizes, int n_in, void* d_out, int out_size, void* d_ws, size_t ws_size,
                              hipStream_t stream) {
  (void)hipFuncSetAttribute((const void*)fused_decode, hipFuncAttributeMaxDynamicSharedMemorySize, 163840);
  fused_decode<<<256, 512, 163840, stream>>>((const u16*)d_in[0], (const u16*)d_in[2], (const u16*)d_in[3], (unsigned char*)d_ws,
                                             (const u16*)d_in[1], (const u16*)d_in[7], (const u16*)d_in[8], (const u16*)d_in[6],
                                             (const u16*)d_in[9], (const u16*)d_in[4], (const u16*)d_in[5], (const u16*)d_in[10],
                                             (u16*)d_out);
  (void)hipFuncSetAttribute((const void*)tiny, hipFuncAttributeMaxDynamicSharedMemorySize, 163840);
  tiny<<<ZG, ZT, ZL, stream>>>((unsigned char*)d_ws
   , d_in[0], d_in[1], d_in[2], d_in[3], d_in[4], d_in[5], d_in[6], d_in[7], d_in[8], d_in[9], d_in[10], d_out
  );
}
```

```cpp
#include <hip/hip_runtime.h>
#include <hip/hip_bf16.h>

typedef unsigned u32;
typedef unsigned short u16;
typedef unsigned long long u64;
typedef u32 v4u __attribute__((ext_vector_type(4)));
typedef u32 v2u __attribute__((ext_vector_type(2)));
typedef short s16x4 __attribute__((ext_vector_type(4)));
typedef short bf16x8 __attribute__((ext_vector_type(8)));
typedef __bf16 bf16x2 __attribute__((ext_vector_type(2)));
typedef float f32x4 __attribute__((ext_vector_type(4)));
typedef float f32x16 __attribute__((ext_vector_type(16)));
#define GAS __attribute__((address_space(1)))
#define LAS __attribute__((address_space(3)))
#define DI __device__ __forceinline__
#define SB() __builtin_amdgcn_sched_barrier(0)

#define WIN0 32
#define WINA0 24
#define NA0 2
#define WINA 44
#define WINB0 8
#define NB0 5
#define WINB 44
#define THOLD 60
#define QW 8
#define CODE_BYTES 22528

#define TYPEBIT 5
constexpr u32 WS_EPOCH = 0;
constexpr u32 WS_QABS = 65536;
constexpr u32 WS_CHK = 131072;
constexpr u32 WS_ASTAT = 262144;
constexpr u32 WS_H3B = 524288;
constexpr u32 WS_BIN = 786432;
constexpr u32 WS_QB = 1048576 + 65536;
constexpr u32 WS_H4 = 1310720;
constexpr u32 WS_H1 = 1835008;
constexpr u32 WS_APART = 2097152;
constexpr u32 WS_BYTES = 6291456;
constexpr u32 WS_STAMP = 8388608;

constexpr u32 L_C = 0, L_K = 0, L_V = 65536, L_UK = 32768, L_WQ = 98304;
constexpr u32 L_Q = 131072;
constexpr u32 L_X = L_Q + 16384, L_QL = L_Q + 18432, L_QBL = L_Q + 22528, L_PB = L_Q + 24576;
constexpr u32 L_P = L_Q, L_SCR = L_Q + 8192, L_LSUM = L_Q + 28672;
constexpr u32 L_MAXX = L_Q + 31744, L_SYNC = L_MAXX + 512;
constexpr u32 L_W1 = 0, L_W2 = 32768, L_WGT = 65536, L_CK = 66048, L_B1 = 67584;
constexpr u32 L_BB = L_Q, L_B2 = L_Q + 8192;
enum { S_WQ = 0, S_UK, S_PUB, S_QBISS, S_QBRDY, S_XRD, S_QGO, S_QISS, S_QST, S_LAND, S_MAX, S_BAD, S_P, S_PV, S_VLAND, S_W1, S_W2, S_TB, S_TA, S_N };

constexpr float KSC = 0.08838834764831845f * 1.4426950408889634f;
constexpr int SPIN_CAP = 1 << 15;

#define STAMP(i) do { } while (0)

template <u32 LDS> DI void dma2(const void* sb, u32 v) {
  u32 keep;
  asm volatile("s_mov_b32 %0, m0\n\ts_mov_b32 m0, %3\n\ts_nop 0\n\tglobal_load_lds_dwordx4 %1, %2 nt\n\t"
               "global_load_lds_dwordx4 %1, %2 offset:1024 nt\n\ts_mov_b32 m0, %0"
               : "=&s"(keep) : "v"(v), "s"(sb), "i"(LDS) : "memory");
}
DI void dma4s(const void* sb, u32 v, u32 lds) {
  u32 keep;
  asm volatile("s_mov_b32 %0, m0\n\ts_mov_b32 m0, %3\n\ts_nop 0\n\tglobal_load_lds_dwordx4 %1, %2 sc1\n\t"
               "global_load_lds_dwordx4 %1, %2 offset:1024 sc1\n\tglobal_load_lds_dwordx4 %1, %2 offset:2048 sc1\n\t"
               "global_load_lds_dwordx4 %1, %2 offset:3072 sc1\n\ts_mov_b32 m0, %0"
               : "=&s"(keep) : "v"(v), "s"(sb), "s"(lds) : "memory");
}
template <int W, u32 LDS> DI void dma4(const void* sb, u32 v0, u32 v1, u32 v2, u32 v3) {
  u32 keep;
  asm volatile("s_mov_b32 %0, m0\n\ts_mov_b32 m0, %6\n\ts_waitcnt vmcnt(%7)\n\t"
               "global_load_lds_dwordx4 %1, %5 nt\n\tglobal_load_lds_dwordx4 %2, %5 offset:1024 nt\n\ts_waitcnt vmcnt(%7)\n\t"
               "global_load_lds_dwordx4 %3, %5 offset:2048 nt\n\tglobal_load_lds_dwordx4 %4, %5 offset:3072 nt\n\ts_mov_b32 m0, %0"
               : "=&s"(keep) : "v"(v0), "v"(v1), "v"(v2), "v"(v3), "s"(sb), "i"(LDS), "i"(W) : "memory");
}
template <int I> struct IC { static constexpr int v = I; };
template <int B, int E, class F> DI void sfor(F&& f) { if constexpr (B < E) { f(IC<B>{}); sfor<B + 1, E>(f); } }
template <int N> DI void vmwait() { asm volatile("s_waitcnt vmcnt(%0)" :: "i"(N) : "memory"); }
DI u32 pk(float a, float b) { bf16x2 v = {(__bf16)a, (__bf16)b}; return __builtin_bit_cast(u32, v); }
DI float blo(u32 x) { return __builtin_bit_cast(float, x << 16); }
DI float bhi(u32 x) { return __builtin_bit_cast(float, x & 0xffff0000u); }
template <class T> DI LAS T* lp(u32 a) { return (LAS T*)(size_t)a; }
DI v4u lds128(u32 a) { return *lp<v4u>(a); }
DI void lds128w(u32 a, v4u v) { *lp<v4u>(a) = v; }
DI void lds64w(u32 a, u32 x, u32 y) { *lp<v2u>(a) = v2u{x, y}; }
DI v4u ldstr(u32 a0, u32 a1) {
  s16x4 lo = __builtin_amdgcn_ds_read_tr16_b64_v4i16(lp<s16x4>(a0));
  s16x4 hi = __builtin_amdgcn_ds_read_tr16_b64_v4i16(lp<s16x4>(a1));
  v2u l = __builtin_bit_cast(v2u, lo), h = __builtin_bit_cast(v2u, hi);
  return v4u{l.x, l.y, h.x, h.y};
}
DI f32x4 mfma16(v4u a, v4u b, f32x4 c) { return __builtin_amdgcn_mfma_f32_16x16x32_bf16(__builtin_bit_cast(bf16x8, a), __builtin_bit_cast(bf16x8, b), c, 0, 0, 0); }
DI f32x16 mfma32(v4u a, v4u b, f32x16 c) { return __builtin_amdgcn_mfma_f32_32x32x16_bf16(__builtin_bit_cast(bf16x8, a), __builtin_bit_cast(bf16x8, b), c, 0, 0, 0); }
template <int CTRL> DI u32 dppu(u32 x) { return (u32)__builtin_amdgcn_mov_dpp((int)x, CTRL, 0xf, 0xf, true); }
DI float f_(u32 x) { return __builtin_bit_cast(float, x); }
DI u32 u_(float x) { return __builtin_bit_cast(u32, x); }
template <int CTRL> DI float dpp(float x) { return f_(dppu<CTRL>(u_(x))); }
constexpr int X1 = 0xB1, X2 = 0x4E, X7 = 0x141, X8 = 0x128;
DI float sum32(float x) { auto t = __builtin_amdgcn_permlane32_swap(u_(x), u_(x), false, false); return f_(t[0]) + f_(t[1]); }
DI float max32(float x) { auto t = __builtin_amdgcn_permlane32_swap(u_(x), u_(x), false, false); return fmaxf(f_(t[0]), f_(t[1])); }
DI float sum16(float x) { auto t = __builtin_amdgcn_permlane16_swap(u_(x), u_(x), false, false); return f_(t[0]) + f_(t[1]); }
DI float max16(float x) { auto t = __builtin_amdgcn_permlane16_swap(u_(x), u_(x), false, false); return fmaxf(f_(t[0]), f_(t[1])); }
DI void sig(int word, int lane) {
  asm volatile("" ::: "memory");
  if (lane == 0) __hip_atomic_fetch_add(lp<u32>(L_SYNC + 4 * word), 1u, __ATOMIC_RELAXED, __HIP_MEMORY_SCOPE_WORKGROUP);
  asm volatile("" ::: "memory");
}
template <bool SLEEP> DI void waitw(int word, u32 n) {
  while ((u32)__builtin_amdgcn_readfirstlane(__hip_atomic_load(lp<u32>(L_SYNC + 4 * word), __ATOMIC_RELAXED, __HIP_MEMORY_SCOPE_WORKGROUP)) < n) {
    if (SLEEP) __builtin_amdgcn_s_sleep(2);
  }
  asm volatile("" ::: "memory");
}
DI void bar() { asm volatile("s_waitcnt lgkmcnt(0)\n\ts_barrier" ::: "memory"); }
#define LD1(off) __builtin_amdgcn_raw_buffer_load_b128(rs, (off), 0, 16)
#define ST1(v, off) __builtin_amdgcn_raw_buffer_store_b128((v), rs, (off), 0, 16)
#define TAGLOAD() __builtin_amdgcn_raw_buffer_load_b32(rs, WS_EPOCH, 0, 16)
DI v4u ldnt(const void* p, u32 off) { return __builtin_nontemporal_load((const GAS v4u*)((const GAS unsigned char*)p + off)); }
DI void stage_w(u32 base, const v4u (&park)[32], u32 g, u32 n) {
#pragma unroll
  for (int i = 0; i < 32; ++i) lds128w(base + g * 256 + i * 1024 + 16 * (n ^ ((g | (((i >> 1) & 1) << 2)) << 1)), park[i]);
}

__global__ void __launch_bounds__(512, 2)
fused_decode(const u16* __restrict__ xA, const u16* __restrict__ wqA, const u16* __restrict__ xB, const u16* __restrict__ wqB,
             unsigned char* ws, const u16* __restrict__ ckv, const u16* __restrict__ wuk, const u16* __restrict__ kc,
             const u16* __restrict__ vc, const u16* __restrict__ wuv, const u16* __restrict__ woA, const u16* __restrict__ woB,
             u16* __restrict__ out) {
  typedef u32 s8u __attribute__((ext_vector_type(8)));
  s8u ka;
  asm volatile("s_load_dwordx8 %0, %1, 0x0" : "=s"(ka) : "s"(__builtin_amdgcn_kernarg_segment_ptr()));
  u64 pc0; asm volatile("s_getpc_b64 %0" : "=s"(pc0));
  const int tid = threadIdx.x, lane = tid & 63, wave = __builtin_amdgcn_readfirstlane(tid >> 6);
  STAMP(0);
  const u32 gid = blockIdx.x, x = gid & 7, r = gid >> 3;
  const bool isA = (r & 4) == 0;
  const u32 l16 = (r & 3) | ((r >> 3) << 2);
  const u32 ht = 4 * x + (l16 >> 2), q4 = l16 & 3;
  const u32 g = lane >> 4, n = lane & 15, tq = n >> 2, tp = lane & 3;
  u32 warm = 0;
  if (wave == 0) {
    const u32 rw = lane >> 3, sw = ((rw >> 1) & 1) << 1;
    const u32 vo = (isA ? (l16 >> 3) * 2097152 + x * 1024 + (l16 & 7) * 128 : (q4 >> 1) * 2097152 + ht * 256 + (q4 & 1) * 128) + rw * 8192;
    const u32 ce = vo + ((lane & 7) ^ sw) * 16, cd = vo + ((lane & 7) ^ sw ^ 4) * 16 + 64512;
    asm volatile("s_waitcnt lgkmcnt(0)" : "+s"(ka));
    STAMP(12);
    const u64 xp = isA ? ((u64)ka[5] << 32 | ka[4]) : ((u64)ka[1] << 32 | ka[0]), wp = isA ? ((u64)ka[7] << 32 | ka[6]) : ((u64)ka[3] << 32 | ka[2]);
    const void* wq = (const void*)wp;
    dma2<L_X>((const void*)xp, lane * 16);
    sfor<0, 8>([&](auto I) {
      constexpr int G = I.v;
      dma4<63, L_WQ + G * 4096>(wq, ce + G * 262144, cd + G * 262144, ce + G * 262144 + 129024, cd + G * 262144 + 129024);
      if constexpr (G == 0 && THOLD > 0) {
        if (isA) {
          const u64 t0 = __builtin_amdgcn_s_memrealtime();
          while (__builtin_amdgcn_s_memrealtime() - t0 < THOLD) __builtin_amdgcn_s_sleep(1);
        }
      }
    });
    STAMP(15);
  } else if (wave == 1) {
    const u32 ln = (r & 3) * 64 + lane;
    warm = ((const GAS u32*)(pc0 & ~127ull))[(ln < CODE_BYTES / 128 ? ln : 0u) * 32];
    SB();
  } else if (wave == 2) {
    if (lane < S_N) *lp<u32>(L_SYNC + 4 * lane) = 0;
  }
  bar();
  asm volatile("" :: "s"(ka));
  const __amdgpu_buffer_rsrc_t rs = __builtin_amdgcn_make_buffer_rsrc((void*)ws, 0, WS_BYTES, 0x00020000);
  u32 tag;
#define SIG_AT(ISSUED, W, NEED, WORD) if constexpr ((ISSUED) - (W) >= (NEED) && (ISSUED) - 4 - (W) < (NEED)) { vmwait<W>(); sig(WORD, lane); }
  const u32 w = wave & 3;
  const u32 a0 = L_WQ + (8 * g + tq) * 128 + 16 * ((2 * w + (tp >> 1)) ^ ((((tq >> 1) & 1) | ((g & 1) << 1)) << 1)) + 8 * (tp & 1);

  if (isA) {
    if (wave == 0) {
      __builtin_amdgcn_s_setprio(1);
      const unsigned char* src = (const unsigned char*)ckv + (size_t)(x * 16 + l16) * 131072;
      u32 cv[16];
#pragma unroll
      for (int i = 0; i < 16; ++i) cv[i] = (lane ^ (((i & 3) << 2) | (i >> 2))) * 16;
      sfor<0, 32>([&](auto I) {
        constexpr int G = I.v;
        if constexpr (G == 24) waitw<true>(S_XRD, 4);
        dma4<(G < NA0 ? WINA0 : WINA), L_C + G * 4096>(src + G * 4096, cv[(4 * G) & 15], cv[(4 * G + 1) & 15], cv[(4 * G + 2) & 15], cv[(4 * G + 3) & 15]);
        if constexpr (G + 1 == NA0) { vmwait<4 * NA0>(); sig(S_WQ, lane); }
      });
      STAMP(1);
      vmwait<QW>(); sig(S_QGO, lane);
      vmwait<0>(); sig(S_LAND, lane);
      STAMP(2);
      waitw<false>(S_QISS, 4);
      v4u park[32];
      const u32 vo = (128 * q4 + g) * 8192 + ht * 256 + n * 16;
#pragma unroll
      for (int i = 0; i < 32; ++i) park[i] = ldnt(wuv, vo + i * 32768);
      SB();
      const u32 tv = TAGLOAD();
      STAMP(3);
      waitw<true>(S_PV, 4);
      stage_w(L_W1, park, g, n);
      tag = __builtin_amdgcn_readfirstlane(tv) + 1;
      STAMP(4);
    } else if (wave < 4) {
      if (wave == 1 && lane < 6) { u32 t = __builtin_amdgcn_raw_buffer_load_b32(rs, lane * 1048576 + 1048576 - 64, 0, 16); asm volatile("" :: "v"(t), "v"(warm)); }
      tag = __builtin_amdgcn_readfirstlane(TAGLOAD()) + 1;
    } else {
      tag = __builtin_amdgcn_readfirstlane(TAGLOAD()) + 1;
      const u32 bs = x * 16 + l16, b = bs >> 6, hh = lane >> 5, hd = lane & 31;
      __builtin_amdgcn_s_setprio(2);
      {
        waitw<false>(S_WQ, 1);
        v4u xf[8], af[8];
#pragma unroll
        for (int s = 0; s < 8; ++s) { af[s] = ldstr(a0 + s * 4096, a0 + s * 4096 + 512); xf[s] = lds128(L_X + (lane & 1) * 1024 + (l16 >> 3) * 512 + g * 16 + s * 64); }
        sig(S_XRD, lane);
        SB();
        f32x4 c0 = {0.f, 0.f, 0.f, 0.f}, c1 = c0;
#pragma unroll
        for (int s = 0; s < 8; s += 2) { c0 = mfma16(af[s], xf[s], c0); c1 = mfma16(af[s + 1], xf[s + 1], c1); }
        c0 += c1;
        if (n < 4) ST1((v4u{u_((n & 2) ? c0[2] : c0[0]), tag, u_((n & 2) ? c0[3] : c0[1]), tag}),
                       WS_QB + x * 16384 + (n & 1) * 8192 + l16 * 512 + (8 * w + 2 * g + (n >> 1)) * 16);
        STAMP(13);
      }
      waitw<true>(S_QGO, 1);
      STAMP(14);
      f32x16 s;
      float m;
      for (u32 it = 1; it < 256; ++it) {
        const unsigned char* qs = ws + WS_QABS + b * 32768 + w * 8192;
        if (w == 0) { dma4s(qs + 1024, lane * 16, L_Q); dma4s(qs + 4096, lane * 16, L_Q + 3072); }
        else { dma4s(qs, lane * 16, L_Q - 1024 + w * 8192); dma4s(qs + 4096, lane * 16, L_Q + 3072 + w * 8192); }
        const v4u q0 = LD1(WS_QABS + b * 32768 + lane * 16);
        const v4u k0 = LD1(WS_CHK + b * 2048 + hd * 64 + hh * 32), k1 = LD1(WS_CHK + b * 2048 + hd * 64 + hh * 32 + 16);
        SB();
        if (it == 1) sig(S_QISS, lane);
        vmwait<0>();
        STAMP(1);
        sig(S_QST, lane);
        u32 cs = (q0.x + q0.y) + (q0.z + q0.w) - (((k0.x + k0.y) + (k0.z + k0.w)) + ((k1.x + k1.y) + (k1.z + k1.w))) + 8 * tag;
        waitw<false>(S_QST, 4 * it);
        waitw<false>(S_LAND, 1);
        STAMP(3);
#pragma unroll
        for (int i = 0; i < 16; ++i) s[i] = 0.f;
        {
          const u32 fr = ((hd & 3) << 2) | ((hd >> 2) & 3);
          const u32 ca = L_C + (32 * w + hd) * 1024 + 16 * (hh ^ fr), qa = L_Q - 1024 + lane * 16;
          v4u af[32], bf[32];
          bf[0] = q0;
          constexpr int PD = 6;
#define S_RD(k) do { af[k] = lds128((ca ^ (32 * ((k) & 7))) + 256 * ((k) >> 3)); if ((k) > 0) bf[k] = lds128(qa + (k) * 1024); } while (0)
#pragma unroll
          for (int k = 0; k < PD; ++k) S_RD(k);
          SB();
#pragma unroll
          for (int k = 0; k < 32; ++k) {
            if (k + PD < 32) S_RD(k + PD);
            s = mfma32(af[k], bf[k], s);
            if (k > 0) { cs = cs + bf[k].x + bf[k].y; cs = cs + bf[k].z + bf[k].w; }
            SB();
          }
        }
        { auto t = __builtin_amdgcn_permlane32_swap(cs, cs, false, false); cs = t[0] + t[1]; }
        if (cs != 0) *lp<u32>(L_SYNC + 4 * S_BAD) = it;
        m = s[0];
#pragma unroll
        for (int i = 1; i < 16; ++i) m = fmaxf(m, s[i]);
        m = max32(m);
        if (hh == 0) *lp<float>(L_MAXX + hd * 16 + w * 4) = m;
        sig(S_MAX, lane);
        STAMP(4);
        waitw<false>(S_MAX, 4 * it);
        if ((u32)__builtin_amdgcn_readfirstlane(*lp<u32>(L_SYNC + 4 * S_BAD)) != it) break;
      }
      const v4u mm = lds128(L_MAXX + hd * 16);
      const float mk = fmaxf(fmaxf(f_(mm.x), f_(mm.y)), fmaxf(f_(mm.z), f_(mm.w))) * KSC;
      float ls = 0.f;
#pragma unroll
      for (int i = 0; i < 16; ++i) { s[i] = __builtin_amdgcn_exp2f(fmaf(s[i], KSC, -mk)); ls += s[i]; }
      lds128w(L_P + (2 * w) * 1024 + lane * 16, v4u{pk(s[0], s[1]), pk(s[2], s[3]), pk(s[4], s[5]), pk(s[6], s[7])});
      lds128w(L_P + (2 * w + 1) * 1024 + lane * 16, v4u{pk(s[8], s[9]), pk(s[10], s[11]), pk(s[12], s[13]), pk(s[14], s[15])});
      ls = sum32(ls);
      if (hh == 0) *lp<float>(L_LSUM + hd * 16 + w * 4) = ls;
      sig(S_P, lane);
      const u32 blk = (lane >> 4) & 1;
      u32 ta[2];
#pragma unroll
      for (int t = 0; t < 2; ++t)
        ta[t] = L_C + (4 * hh + 8 * t + tq) * 1024 + 256 * w + 16 * ((2 * blk + (tp >> 1)) ^ ((tq << 2) | (hh + 2 * t))) + 8 * (tp & 1);
      v4u cf[32];
      constexpr int PV_PD = 5;
#define PV_RD(kk) cf[kk] = ldstr((ta[0] ^ (64 * ((kk) >> 3))) + ((kk) & 7) * 16384, (ta[1] ^ (64 * ((kk) >> 3))) + ((kk) & 7) * 16384)
#pragma unroll
      for (int k = 0; k < PV_PD; ++k) PV_RD(k);
      SB();
      waitw<false>(S_P, 4);
      STAMP(5);
      v4u pf[8];
#pragma unroll
      for (int k = 0; k < 8; ++k) pf[k] = lds128(L_P + k * 1024 + lane * 16);
      const v4u lv = lds128(L_LSUM + hd * 16);
      SB();
      const u32 scw = L_SCR + w * 4096 + hd * 64 + 8 * hh, scx = 16 * ((hd >> 1) & 3);
      const u32 scr = L_SCR + w * 4096 + (lane >> 2) * 64 + 16 * (tp ^ ((lane >> 3) & 3));
      const u32 sto = WS_APART + (((lane >> 2) * 4 + w) * 128 + bs) * 256 + 16 * tp;
      f32x16 o[4];
      v4u ov[2];
      u32 cs = tag;
#define EPI_W(c) _Pragma("unroll") for (int gg = 0; gg < 4; ++gg) { \
        const u32 p0 = pk(o[c][4 * gg], o[c][4 * gg + 1]), p1 = pk(o[c][4 * gg + 2], o[c][4 * gg + 3]); \
        lds64w(scw + ((c) & 1) * 2048 + ((16 * gg) ^ scx), p0, p1); cs += p0 + p1; asm volatile("" : "+v"(cs)); }
#define EPI_R(c) do { ov[0] = lds128(scr + ((c) & 1) * 2048); ov[1] = lds128(scr + ((c) & 1) * 2048 + 1024); } while (0)
#define EPI_S(c) do { ST1(ov[0], sto + 64 * (c)); ST1(ov[1], sto + 64 * (c) + 16 * 4 * 128 * 256); } while (0)
#pragma unroll
      for (int kk = 0; kk < 32; ++kk) {
        const int c = kk >> 3, k = kk & 7;
        if (kk + PV_PD < 32) PV_RD(kk + PV_PD);
        if (k == 0) {
          f32x16 z;
#pragma unroll
          for (int i = 0; i < 16; ++i) z[i] = 0.f;
          o[c] = mfma32(cf[kk], pf[k], z);
        } else o[c] = mfma32(cf[kk], pf[k], o[c]);
        if (c > 0 && k == 1) { EPI_W(c - 1) EPI_R(c - 1); }
        if (c > 0 && k == 5) { EPI_S(c - 1); }
        SB();
      }
      sig(S_PV, lane);
      EPI_W(3) EPI_R(3);
      const float lsum = (f_(lv.x) + f_(lv.y)) + (f_(lv.z) + f_(lv.w));
      { auto t = __builtin_amdgcn_permlane32_swap(cs, cs, false, false); cs = t[0] + t[1] - tag + u_(mk) + u_(lsum); }
      EPI_S(3);
      if (hh == 0) ST1((v4u{u_(mk), u_(lsum), cs, tag}), WS_ASTAT + ((hd * 4 + w) * 128 + bs) * 16);
      STAMP(7);
    }
    const u32 v = wave;
    const u32 ab = (8 * g + tq) * 256 + 16 * ((2 * v + (tp >> 1)) ^ ((tq | ((g & 1) << 2)) << 1)) + 8 * (tp & 1);
    v4u a1[4], bq[4];
    if (wave == 3) waitw<true>(S_PV, 4);
    for (u32 it = 257; it < 320; ++it) {
      v4u st0, st1;
      if (wave == 3) {
        for (int spin = 0; spin < SPIN_CAP; ++spin) {
          st0 = LD1(WS_ASTAT + (ht * 4 + q4) * 2048 + lane * 16);
          st1 = LD1(WS_ASTAT + (ht * 4 + q4) * 2048 + 1024 + lane * 16);
          asm volatile("" ::: "memory");
          if (__all(st0.w == tag && st1.w == tag)) break;
        }
        STAMP(2);
      }
      bar();
      STAMP(8);
      v4u pd[4];
#pragma unroll
      for (int i = 0; i < 4; ++i) pd[i] = LD1(WS_APART + (ht * 4 + q4) * 32768 + v * 4096 + i * 1024 + lane * 16);
      SB();
      if (wave == 3) {
#pragma unroll
        for (int i = 0; i < 2; ++i) {
          const v4u st = i ? st1 : st0;
          float M = f_(st.x);
          M = fmaxf(M, dpp<X1>(M)); M = fmaxf(M, dpp<X2>(M)); M = fmaxf(M, dpp<X7>(M)); M = fmaxf(M, dpp<X8>(M)); M = max16(M); M = max32(M);
          const float e = __builtin_amdgcn_exp2f(f_(st.x) - M);
          float S = e * f_(st.y);
          S += dpp<X1>(S); S += dpp<X2>(S); S += dpp<X7>(S); S += dpp<X8>(S); S = sum16(S); S = sum32(S);
          *lp<float>(L_WGT + i * 256 + lane * 4) = e / S;
          *lp<u32>(L_CK + i * 256 + lane * 4) = st.z - tag - st.x - st.y;
        }
      }
#pragma unroll
      for (int s = 0; s < 4; ++s) a1[s] = ldstr(L_W1 + ab + s * 8192, L_W1 + ab + s * 8192 + 1024);
      bar();
      float acc[8], wt[4];
#pragma unroll
      for (int i = 0; i < 4; ++i) wt[i] = *lp<float>(L_WGT + (v * 16 + 4 * i + g) * 4);
      const u32 ck = *lp<u32>(L_CK + (v * 16 + 4 * (lane & 3) + g) * 4);
#pragma unroll
      for (int k = 0; k < 4; ++k) { acc[2 * k] = wt[0] * blo(pd[0][k]); acc[2 * k + 1] = wt[0] * bhi(pd[0][k]); }
#pragma unroll
      for (int i = 1; i < 4; ++i)
#pragma unroll
        for (int k = 0; k < 4; ++k) { acc[2 * k] = fmaf(wt[i], blo(pd[i][k]), acc[2 * k]); acc[2 * k + 1] = fmaf(wt[i], bhi(pd[i][k]), acc[2 * k + 1]); }
      STAMP(9);
      {
        float y[4];
#pragma unroll
        for (int k = 0; k < 4; ++k) { auto t = __builtin_amdgcn_permlane32_swap(u_(acc[k]), u_(acc[k + 4]), false, false); y[k] = f_(t[0]) + f_(t[1]); }
        float z[2];
#pragma unroll
        for (int k = 0; k < 2; ++k) { auto t = __builtin_amdgcn_permlane16_swap(u_(y[k]), u_(y[k + 2]), false, false); z[k] = f_(t[0]) + f_(t[1]); }
        const u32 hi = pk(z[0], z[1]), lo = pk(z[0] - blo(hi), z[1] - bhi(hi));
        *lp<u32>(L_B1 + v * 272 + (8 * n + 2 * g) * 2) = hi;
        *lp<u32>(L_B1 + (8 + v) * 272 + (8 * n + 2 * g) * 2) = lo;
        u32 c4[4], c2[2];
#pragma unroll
        for (int i = 0; i < 4; ++i) c4[i] = (pd[i].x + pd[i].y) + (pd[i].z + pd[i].w);
#pragma unroll
        for (int i = 0; i < 2; ++i) { const u32 p = c4[i] + dppu<X2>(c4[i]), q = c4[i + 2] + dppu<X2>(c4[i + 2]); c2[i] = (lane & 2) ? q : p; }
        const u32 p1 = c2[0] + dppu<X1>(c2[0]), q1 = c2[1] + dppu<X1>(c2[1]);
        u32 c1 = (lane & 1) ? q1 : p1;
        c1 += dppu<X8>(c1);
        c1 += dppu<0x124>(c1);
        if (c1 != ck) *lp<u32>(L_SYNC + 4 * S_BAD) = it;
      }
      bar();
      const u32 bad = *lp<u32>(L_SYNC + 4 * S_BAD);
#pragma unroll
      for (int s = 0; s < 4; ++s) bq[s] = lds128(L_B1 + n * 272 + g * 16 + s * 64);
      SB();
      if (__builtin_amdgcn_readfirstlane(bad) != it) break;
    }
    {
      f32x4 d = {0.f, 0.f, 0.f, 0.f};
#pragma unroll
      for (int s = 0; s < 4; ++s) d = mfma16(a1[s], bq[s], d);
#pragma unroll
      for (int k = 0; k < 4; ++k) { float t = d[k]; t += dpp<X1>(t); t += dpp<X2>(t); t += dpp<X8>(t); d[k] = t; }
      if ((n & 11) == 0) ST1((v4u{pk(d[0], d[1]), tag, pk(d[2], d[3]), tag}), WS_H3B + ht * 4096 + q4 * 1024 + (n >> 2) * 512 + (4 * v + g) * 16);
    }
    STAMP(10);
    if (wave == 0) {
      const u32 rho = x * 8 + (l16 >> 1), part = l16 & 1;
      v4u e[4];
      for (int spin = 0; spin < SPIN_CAP; ++spin) {
        bool ok = true;
#pragma unroll
        for (int i = 0; i < 4; ++i) e[i] = LD1(WS_H4 + part * 262144 + rho * 4096 + i * 1024 + lane * 16);
        asm volatile("" ::: "memory");
#pragma unroll
        for (int i = 0; i < 4; ++i) ok = ok && e[i].y == tag && e[i].w == tag;
        if (__all(ok)) break;
      }
      float s0 = (f_(e[0].x) + f_(e[1].x)) + (f_(e[2].x) + f_(e[3].x)), s1 = (f_(e[0].z) + f_(e[1].z)) + (f_(e[2].z) + f_(e[3].z));
      s0 += dpp<X8>(s0); s1 += dpp<X8>(s1);
      s0 = sum32(sum16(s0)); s1 = sum32(sum16(s1));
      if (lane < 8) *(GAS u32*)(out + (rho & 1) * 1024 + part * 512 + (rho >> 1) * 16 + 2 * lane) = pk(s0, s1);
      if (gid == 0 && lane == 0) __builtin_amdgcn_raw_buffer_store_b32(tag, rs, WS_EPOCH, 0, 16);
      STAMP(13);
    }
  } else {
    const u32 ab = (8 * g + tq) * 256 + 16 * ((4 * w + (tp >> 1)) ^ ((tq | ((g & 1) << 2)) << 1)) + 8 * (tp & 1);
    if (wave < 4) {
      if (wave == 0) {
        __builtin_amdgcn_s_setprio(1);
        const u32 uo = (128 * q4 + g) * 8192 + ht * 256;
        sfor<0, 8>([&](auto I) {
          constexpr int G = I.v;
          const u32 u0 = uo + G * 131072 + ((n ^ (g | (((2 * G) & 3) << 2))) * 16), u1 = uo + G * 131072 + ((n ^ (g | (((2 * G + 1) & 3) << 2))) * 16);
          dma4<WIN0, L_UK + G * 4096>(wuk, u0, u0 + 31744, u1 + 63488, u1 + 95232);
          SIG_AT(38 + 4 * G, WIN0, 34, S_WQ)
        });
        STAMP(14);
        vmwait<0>(); sig(S_UK, lane);
        tag = __builtin_amdgcn_readfirstlane(TAGLOAD()) + 1;
        waitw<false>(S_QBISS, 1);
        STAMP(13);
        const size_t base = (((size_t)(l16 >> 3) * 8 + x) * 8192 + 6144 + (l16 & 7) * 256) * 256;
        const unsigned char* ks = (const unsigned char*)kc + base;
        const unsigned char* vs = (const unsigned char*)vc + base;
        {
          u32 kv[4];
#pragma unroll
          for (int i = 0; i < 4; ++i) kv[i] = g * 256 + ((n ^ (4 * i + g)) * 16);
          sfor<0, 16>([&](auto I) { constexpr int G = I.v; dma4<(G < NB0 ? WINB0 : WINB), L_K + G * 4096>(ks + G * 4096, kv[0], kv[1], kv[2], kv[3]); });
        }
        STAMP(1);
        {
          const u32 v0 = g * 256 + ((n ^ (g << 1)) * 16), v1 = g * 256 + ((n ^ ((4 + g) << 1)) * 16);
          sfor<0, 16>([&](auto I) {
            constexpr int G = I.v;
            dma4<WINB, L_V + G * 4096>(vs + G * 4096, v0, v1, v0, v1);
            SIG_AT(4 + 4 * G, WINB, 0, S_LAND)
            SIG_AT(4 + 4 * G, WINB, 16, S_VLAND)
            SIG_AT(4 + 4 * G, WINB, 32, S_VLAND)
            SIG_AT(4 + 4 * G, WINB, 48, S_VLAND)
          });
        }
        STAMP(2);
        waitw<false>(S_P, 4);
        {
          const u32 vo = (ht * 128 + g) * 1024 + q4 * 256, w0 = vo + ((n ^ (g << 1)) * 16), w1 = vo + ((n ^ ((g | 4) << 1)) * 16) + 6144;
          sfor<0, 16>([&](auto I) {
            constexpr int G = I.v, H = G & 7;
            dma4<WINB, L_W1 + G * 4096>(G < 8 ? woB : woA, w0 + H * 16384, w0 + H * 16384 + 3072, w1 + H * 16384, w1 + H * 16384 + 3072);
            SIG_AT(68 + 4 * G, WINB, 32, S_VLAND)
            SIG_AT(68 + 4 * G, WINB, 48, S_VLAND)
            SIG_AT(68 + 4 * G, WINB, 64, S_VLAND)
          });
        }
        STAMP(3);
        vmwait<32>(); sig(S_W1, lane);
        vmwait<0>(); sig(S_W2, lane);
        STAMP(4);
      } else if (wave == 1) {
        __builtin_amdgcn_s_setprio(2);
        if (lane < 6) { u32 t = __builtin_amdgcn_raw_buffer_load_b32(rs, lane * 1048576 + 1048576 - 64, 0, 16); asm volatile("" :: "v"(t), "v"(warm)); }
        tag = __builtin_amdgcn_readfirstlane(TAGLOAD()) + 1;
        waitw<false>(S_PUB, 4);
        STAMP(2);
        v4u e[8];
        for (int spin = 0; spin < SPIN_CAP; ++spin) {
          bool ok = true;
#pragma unroll
          for (int i = 0; i < 8; ++i) e[i] = LD1(WS_QB + x * 16384 + (l16 >> 3) * 8192 + i * 1024 + lane * 16);
          SB();
          if (spin == 0) sig(S_QBISS, lane);
          asm volatile("" ::: "memory");
#pragma unroll
          for (int i = 0; i < 8; ++i) ok = ok && e[i].y == tag && e[i].w == tag;
          if (__all(ok)) break;
        }
#pragma unroll
        for (int i = 0; i < 4; ++i) *lp<u32>(L_QBL + i * 256 + lane * 4) = pk(f_(e[i].x) + f_(e[4 + i].x), f_(e[i].z) + f_(e[4 + i].z));
        sig(S_QBRDY, lane);
        STAMP(3);
      } else {
        tag = __builtin_amdgcn_readfirstlane(TAGLOAD()) + 1;
      }
      waitw<true>(S_W2, 1);
      v4u a2[8];
#pragma unroll
      for (int s = 0; s < 8; ++s) { const u32 a = L_W2 + (ab ^ (32 * (s >> 2))) + (s & 3) * 8192; a2[s] = ldstr(a, a + 1024); }
      v4u e;
      for (int spin = 0; spin < SPIN_CAP; ++spin) {
        e = LD1(WS_H3B + ht * 4096 + w * 1024 + lane * 16);
        asm volatile("" ::: "memory");
        if (__all(e.y == tag && e.w == tag)) break;
      }
      lds64w(L_B2 + ((lane >> 5) * 4 + w) * 272 + (lane & 31) * 8, e.x, e.z);
      sig(S_TA, lane);
      waitw<false>(S_TA, 4);
      STAMP(11);
      v4u bq[4];
#pragma unroll
      for (int s = 0; s < 4; ++s) bq[s] = lds128(L_B2 + (n & 7) * 272 + g * 16 + s * 64);
      SB();
      f32x4 d0 = {0.f, 0.f, 0.f, 0.f}, d1 = d0;
#pragma unroll
      for (int s = 0; s < 4; ++s) { d0 = mfma16(a2[s], bq[s], d0); d1 = mfma16(a2[4 + s], bq[s], d1); }
      f32x4 d = (n & 8) ? d1 : d0;
#pragma unroll
      for (int k = 0; k < 4; ++k) { float t = d[k]; t += dpp<X1>(t); t += dpp<X2>(t); d[k] = t; }
      const bool k1 = (n & 1) != 0;
      if ((n & 2) == 0) ST1((v4u{u_(k1 ? d[2] : d[0]), tag, u_(k1 ? d[3] : d[1]), tag}),
                            WS_H4 + (((q4 * 8 + 2 * w + (n >> 3)) * 2 + ((n >> 2) & 1)) * 32 + ht) * 128 + g * 32 + (n & 1) * 16);
      STAMP(12);
    } else {
      tag = __builtin_amdgcn_readfirstlane(TAGLOAD()) + 1;
      __builtin_amdgcn_s_setprio(2);
      {
        waitw<false>(S_WQ, 1);
        STAMP(10);
        v4u xf[8], af[8];
#pragma unroll
        for (int s = 0; s < 8; ++s) { af[s] = ldstr(a0 + s * 4096, a0 + s * 4096 + 512); xf[s] = lds128(L_X + (lane & 1) * 1024 + (q4 >> 1) * 512 + g * 16 + s * 64); }
        SB();
        f32x4 c0 = {0.f, 0.f, 0.f, 0.f}, c1 = c0;
#pragma unroll
        for (int s = 0; s < 8; s += 2) { c0 = mfma16(af[s], xf[s], c0); c1 = mfma16(af[s + 1], xf[s + 1], c1); }
        c0 += c1;
        if (n < 4) ST1((v4u{u_((n & 2) ? c0[2] : c0[0]), tag, u_((n & 2) ? c0[3] : c0[1]), tag}),
                       WS_H1 + ht * 4096 + q4 * 1024 + (n & 1) * 512 + (8 * w + 2 * g + (n >> 1)) * 16);
        STAMP(11);
        waitw<false>(S_UK, 1);
        v4u uk[8];
#pragma unroll
        for (int t = 0; t < 2; ++t)
#pragma unroll
          for (int s = 0; s < 4; ++s) uk[t * 4 + s] = lds128(L_UK + (32 * w + 8 * (n >> 2) + 4 * t + (n & 3)) * 256 + 16 * ((4 * s + g) ^ n));
        v4u e[4];
        for (int spin = 0; spin < SPIN_CAP; ++spin) {
          bool ok = true;
#pragma unroll
          for (int i = 0; i < 4; ++i) e[i] = LD1(WS_H1 + ht * 4096 + i * 1024 + lane * 16);
          asm volatile("" ::: "memory");
#pragma unroll
          for (int i = 0; i < 4; ++i) ok = ok && e[i].y == tag && e[i].w == tag;
          if (__all(ok)) break;
        }
        STAMP(14);
        const u32 ql = L_QL + w * 512;
#pragma unroll
        for (int d = 0; d < 2; ++d)
          *lp<u32>(ql + (lane >> 5) * 256 + d * 128 + (lane & 31) * 4) = pk(f_(e[d].x) + f_(e[2 + d].x), f_(e[d].z) + f_(e[2 + d].z));
        v4u qb[4];
#pragma unroll
        for (int s = 0; s < 4; ++s) qb[s] = lds128(ql + (lane & 1) * 256 + g * 16 + s * 64);
        SB();
        f32x4 u0 = {0.f, 0.f, 0.f, 0.f}, u1 = u0;
#pragma unroll
        for (int s = 0; s < 4; ++s) { u0 = mfma16(uk[s], qb[s], u0); u1 = mfma16(uk[4 + s], qb[s], u1); }
        const v4u pv = {pk(u0[0], u0[1]), pk(u0[2], u0[3]), pk(u1[0], u1[1]), pk(u1[2], u1[3])};
        if (n < 2) ST1(pv, WS_QABS + n * 32768 + (8 * q4 + 2 * w) * 1024 + g * 512 + ht * 16);
        u32 cs = (pv.x + pv.y) + (pv.z + pv.w);
        { auto t = __builtin_amdgcn_permlane16_swap(cs, cs, false, false); cs = t[0] + t[1]; }
        { auto t = __builtin_amdgcn_permlane32_swap(cs, cs, false, false); cs = t[0] + t[1]; }
        if (lane < 2) __builtin_amdgcn_raw_buffer_store_b32(cs + tag, rs, WS_CHK + lane * 2048 + ht * 64 + (4 * q4 + w) * 4, 0, 16);
        sig(S_PUB, lane);
        STAMP(15);
      }
      waitw<true>(S_QBRDY, 1);
      v4u qf[4];
#pragma unroll
      for (int s = 0; s < 4; ++s) qf[s] = lds128(L_QBL + (lane & 3) * 256 + g * 16 + s * 64);
      waitw<true>(S_LAND, 1);
      STAMP(3);
      f32x4 sc[4];
      {
        v4u kf[16];
        const u32 ka = L_K + (64 * w + n) * 256 + 16 * (g ^ n);
#pragma unroll
        for (int i = 0; i < 16; ++i) kf[i] = lds128((ka ^ (64 * (i & 3))) + (i >> 2) * 4096);
        SB();
#pragma unroll
        for (int t = 0; t < 4; ++t) {
          sc[t] = f32x4{0.f, 0.f, 0.f, 0.f};
#pragma unroll
          for (int s = 0; s < 4; ++s) sc[t] = mfma16(kf[4 * t + s], qf[s], sc[t]);
        }
      }
      float m = sc[0][0];
#pragma unroll
      for (int i = 1; i < 16; ++i) m = fmaxf(m, sc[i >> 2][i & 3]);
      m = max32(max16(m));
      if (lane < 4) *lp<float>(L_MAXX + lane * 16 + w * 4) = m;
      sig(S_MAX, lane);
      waitw<false>(S_MAX, 4);
      const v4u mm = lds128(L_MAXX + (lane & 3) * 16);
      const float mk = fmaxf(fmaxf(f_(mm.x), f_(mm.y)), fmaxf(f_(mm.z), f_(mm.w))) * KSC;
      float ls = 0.f;
#pragma unroll
      for (int i = 0; i < 16; ++i) { const float p = __builtin_amdgcn_exp2f(fmaf(sc[i >> 2][i & 3], KSC, -mk)); sc[i >> 2][i & 3] = p; ls += p; }
      ls = sum32(sum16(ls));
      if (n < 4) {
#pragma unroll
        for (int u = 0; u < 2; ++u)
          lds128w(L_PB + (2 * w + u) * 256 + g * 64 + n * 16,
                  v4u{pk(sc[2 * u][0], sc[2 * u][1]), pk(sc[2 * u][2], sc[2 * u][3]), pk(sc[2 * u + 1][0], sc[2 * u + 1][1]), pk(sc[2 * u + 1][2], sc[2 * u + 1][3])});
      }
      if (lane < 4) *lp<float>(L_LSUM + lane * 16 + w * 4) = ls;
      sig(S_P, lane);
      waitw<false>(S_P, 4);
      STAMP(5);
      v4u pf[8];
#pragma unroll
      for (int k = 0; k < 8; ++k) pf[k] = lds128(L_PB + k * 256 + g * 64 + (lane & 3) * 16);
      if (w == 0) {
        const v4u lv = lds128(L_LSUM + (lane & 3) * 16);
        const float lsum = (f_(lv.x) + f_(lv.y)) + (f_(lv.z) + f_(lv.w));
        if (lane < 4) ST1((v4u{u_(mk), tag, u_(lsum), tag}), WS_BIN + (4 * x + lane) * 9216 + 8192 + l16 * 16);
      }
      f32x4 o0 = {0.f, 0.f, 0.f, 0.f}, o1 = o0;
      const u32 va = L_V + (4 * g + tq) * 256 + 16 * ((4 * w + (tp >> 1)) ^ ((4 * (g & 1) + tq) << 1)) + 8 * (tp & 1);
#pragma unroll
      for (int k2 = 0; k2 < 4; ++k2) {
        waitw<false>(S_VLAND, k2 + 1);
        v4u vf[4];
#pragma unroll
        for (int i = 0; i < 4; ++i) { const u32 a = (va ^ (32 * (i & 1))) + (2 * k2 + (i >> 1)) * 8192; vf[i] = ldstr(a, a + 4096); }
        SB();
        o0 = mfma16(vf[0], pf[2 * k2], o0); o1 = mfma16(vf[1], pf[2 * k2], o1);
        o0 = mfma16(vf[2], pf[2 * k2 + 1], o0); o1 = mfma16(vf[3], pf[2 * k2 + 1], o1);
      }
      STAMP(6);
      {
        const f32x4 o = (n & 4) ? o1 : o0;
        if (n < 8) ST1((v4u{pk(o[0], o[1]), tag, pk(o[2], o[3]), tag}), WS_BIN + (4 * x + (n & 3)) * 9216 + l16 * 512 + (4 * (2 * w + (n >> 2)) + g) * 16);
      }
      STAMP(7);
      waitw<false>(S_W1, 1);
      STAMP(8);
      v4u a1[8];
#pragma unroll
      for (int s = 0; s < 8; ++s) { const u32 a = L_W1 + (ab ^ (32 * (s >> 2))) + (s & 3) * 8192; a1[s] = ldstr(a, a + 1024); }
      v4u e0, e1, st;
      for (int spin = 0; spin < SPIN_CAP; ++spin) {
        e0 = LD1(WS_BIN + ht * 9216 + w * 2048 + lane * 16);
        e1 = LD1(WS_BIN + ht * 9216 + w * 2048 + 1024 + lane * 16);
        st = LD1(WS_BIN + ht * 9216 + 8192 + n * 16);
        asm volatile("" ::: "memory");
        if (__all(e0.y == tag && e0.w == tag && e1.y == tag && e1.w == tag && st.y == tag && st.w == tag)) break;
      }
      STAMP(9);
      lds64w(L_BB + (4 * w + (lane >> 5)) * 272 + (lane & 31) * 8, e0.x, e0.z);
      lds64w(L_BB + (4 * w + 2 + (lane >> 5)) * 272 + (lane & 31) * 8, e1.x, e1.z);
      sig(S_TB, lane);
      float M = f_(st.x);
      M = fmaxf(M, dpp<X1>(M)); M = fmaxf(M, dpp<X2>(M)); M = fmaxf(M, dpp<X7>(M));
      const float ee = __builtin_amdgcn_exp2f(f_(st.x) - M);
      float S = ee * f_(st.z);
      S += dpp<X1>(S); S += dpp<X2>(S); S += dpp<X7>(S);
      const float wt = ee / S;
      waitw<false>(S_TB, 4);
      v4u bq[4];
#pragma unroll
      for (int s = 0; s < 4; ++s) bq[s] = lds128(L_BB + n * 272 + g * 16 + s * 64);
      SB();
      f32x4 d0 = {0.f, 0.f, 0.f, 0.f}, d1 = d0;
#pragma unroll
      for (int s = 0; s < 4; ++s) { d0 = mfma16(a1[s], bq[s], d0); d1 = mfma16(a1[4 + s], bq[s], d1); }
#pragma unroll
      for (int k = 0; k < 4; ++k) {
        float t = d0[k] * wt, u = d1[k] * wt;
        t += dpp<X1>(t); u += dpp<X1>(u); t += dpp<X2>(t); u += dpp<X2>(u); t += dpp<X7>(t); u += dpp<X7>(u);
        d0[k] = (n & 2) ? u : t;
      }
      const bool k1 = (n & 1) != 0;
      if ((n & 4) == 0) ST1((v4u{u_(k1 ? d0[2] : d0[0]), tag, u_(k1 ? d0[3] : d0[1]), tag}),
                            WS_H4 + 262144 + (((q4 * 8 + 2 * w + ((n >> 1) & 1)) * 2 + (n >> 3)) * 32 + ht) * 128 + g * 32 + (n & 1) * 16);
      STAMP(12);
    }
  }
}

extern "C" void kernel_launch(void* const* d_in, const int* in_sizes, int n_in, void* d_out, int out_size, void* d_ws, size_t ws_size,
                              hipStream_t stream) {
  (void)hipFuncSetAttribute((const void*)fused_decode, hipFuncAttributeMaxDynamicSharedMemorySize, 163840);
  fused_decode<<<256, 512, 163840, stream>>>((const u16*)d_in[0], (const u16*)d_in[2], (const u16*)d_in[6], (const u16*)d_in[9],
                                             (unsigned char*)d_ws, (const u16*)d_in[1], (const u16*)d_in[3], (const u16*)d_in[7],
                                             (const u16*)d_in[8], (const u16*)d_in[4], (const u16*)d_in[5], (const u16*)d_in[10],
                                             (u16*)d_out);
}
```

```cpp
#include <hip/hip_runtime.h>
#include <hip/hip_bf16.h>

typedef unsigned u32;
typedef unsigned short u16;
typedef unsigned long long u64;
typedef u32 v4u __attribute__((ext_vector_type(4)));
typedef u32 v2u __attribute__((ext_vector_type(2)));
typedef short s16x4 __attribute__((ext_vector_type(4)));
typedef short bf16x8 __attribute__((ext_vector_type(8)));
typedef __bf16 bf16x2 __attribute__((ext_vector_type(2)));
typedef float f32x4 __attribute__((ext_vector_type(4)));
typedef float f32x16 __attribute__((ext_vector_type(16)));
#define GAS __attribute__((address_space(1)))
#define LAS __attribute__((address_space(3)))
#define DI __device__ __forceinline__
#define SB() __builtin_amdgcn_sched_barrier(0)

#define WIN0 32
#define WINA0 24
#define NA0 2
#define WINA 32
#define WINB0 8
#define NB0 0
#define WINB 24
#define THOLD 40
#define BGATE 0
#define WINU 32
#define WUV_AT 8
#define PDELAY 70
#define ABL 0
#define FDELAY 110
#define KDELAY 80
#define XFIRST 0
#define HPOLL 6
#define QW 8
#define CODE_BYTES 23552

#define TYPEBIT 5
constexpr u32 WS_EPOCH = 0;
constexpr u32 WS_HINT = 16384;
constexpr u32 WS_QABS = 65536;
constexpr u32 WS_ASTAT = 262144;
constexpr u32 WS_H3B = 524288;
constexpr u32 WS_BIN = 786432;
constexpr u32 WS_QB = 1048576 + 65536;
constexpr u32 WS_H4 = 1310720;
constexpr u32 WS_H1 = 1835008;
constexpr u32 WS_APART = 2097152;
constexpr u32 WS_BYTES = 6291456;
constexpr u32 WS_STAMP = 8388608;

constexpr u32 L_C = 0, L_K = 0, L_V = 65536, L_UK = 32768, L_WQ = 98304;
constexpr u32 L_Q = 131072;
constexpr u32 L_X = L_Q + 16384, L_QL = L_Q + 18432, L_QX = L_Q + 20480, L_QBL = L_Q + 22528, L_PB = L_Q + 24576;
constexpr u32 L_P = L_Q, L_SCR = L_Q + 8192, L_LSUM = L_Q + 28672, L_EXP = L_Q + 29696, L_GOT = L_Q + 29824;
constexpr u32 L_MAXX = L_Q + 31744, L_SYNC = L_MAXX + 512, L_PTR = L_SYNC + 256;
constexpr u32 L_W1 = 0, L_W2 = 32768, L_WGT = 65536, L_CK = 66048, L_BAD = 66560, L_B1 = 67584;
constexpr u32 L_BB = L_Q, L_B2 = L_Q + 8192;
enum { S_WQ = 0, S_UK, S_PUB, S_QBISS, S_QBRDY, S_XRD, S_CISS, S_HINT, S_QGO, S_QISS, S_QST, S_LAND, S_MAX, S_BAD, S_P, S_PV, S_KRD, S_W1, S_W2, S_TB, S_TA, S_N };

constexpr float KSC = 0.08838834764831845f * 1.4426950408889634f;
constexpr int SPIN_CAP = 1 << 15;
constexpr u32 QTRIES = 64;

#define STAMP(i) do { } while (0)

template <u32 LDS> DI void dma2(const void* sb, u32 v) {
  u32 keep;
  asm volatile("s_mov_b32 %0, m0\n\ts_mov_b32 m0, %3\n\ts_nop 0\n\tglobal_load_lds_dwordx4 %1, %2\n\t"
               "global_load_lds_dwordx4 %1, %2 offset:1024\n\ts_mov_b32 m0, %0"
               : "=&s"(keep) : "v"(v), "s"(sb), "i"(LDS) : "memory");
}
DI void dma4s(const void* sb, u32 v0, u32 v1, u32 v2, u32 v3, u32 lds) {
  u32 keep;
  asm volatile("s_mov_b32 %0, m0\n\ts_mov_b32 m0, %6\n\ts_nop 0\n\tglobal_load_lds_dwordx4 %1, %5 sc1\n\t"
               "global_load_lds_dwordx4 %2, %5 offset:1024 sc1\n\tglobal_load_lds_dwordx4 %3, %5 offset:2048 sc1\n\t"
               "global_load_lds_dwordx4 %4, %5 offset:3072 sc1\n\ts_mov_b32 m0, %0"
               : "=&s"(keep) : "v"(v0), "v"(v1), "v"(v2), "v"(v3), "s"(sb), "s"(lds) : "memory");
}
template <int W, u32 LDS> DI void dma4(const void* sb, u32 v0, u32 v1, u32 v2, u32 v3) {
  u32 keep;
  asm volatile("s_mov_b32 %0, m0\n\ts_mov_b32 m0, %6\n\ts_waitcnt vmcnt(%7)\n\t"
               "global_load_lds_dwordx4 %1, %5 nt\n\tglobal_load_lds_dwordx4 %2, %5 offset:1024 nt\n\ts_waitcnt vmcnt(%7)\n\t"
               "global_load_lds_dwordx4 %3, %5 offset:2048 nt\n\tglobal_load_lds_dwordx4 %4, %5 offset:3072 nt\n\ts_mov_b32 m0, %0"
               : "=&s"(keep) : "v"(v0), "v"(v1), "v"(v2), "v"(v3), "s"(sb), "i"(LDS), "i"(W) : "memory");
}
template <int W> DI void dma4r(const void* sb, u32 v0, u32 v1, u32 v2, u32 v3, u32 lds) {
  u32 keep;
  asm volatile("s_mov_b32 %0, m0\n\ts_mov_b32 m0, %6\n\ts_waitcnt vmcnt(%7)\n\t"
               "global_load_lds_dwordx4 %1, %5 nt\n\tglobal_load_lds_dwordx4 %2, %5 offset:1024 nt\n\ts_waitcnt vmcnt(%7)\n\t"
               "global_load_lds_dwordx4 %3, %5 offset:2048 nt\n\tglobal_load_lds_dwordx4 %4, %5 offset:3072 nt\n\ts_mov_b32 m0, %0"
               : "=&s"(keep) : "v"(v0), "v"(v1), "v"(v2), "v"(v3), "s"(sb), "s"(lds), "i"(W) : "memory");
}
typedef const unsigned char* cptr;
DI cptr mkptr(u32 lo, u32 hi) { return (cptr)((u64)hi << 32 | lo); }
template <int I> struct IC { static constexpr int v = I; };
template <int B, int E, class F> DI void sfor(F&& f) { if constexpr (B < E) { f(IC<B>{}); sfor<B + 1, E>(f); } }
template <int N> DI void vmwait() { asm volatile("s_waitcnt vmcnt(%0)" :: "i"(N) : "memory"); }
DI u32 pk(float a, float b) { bf16x2 v = {(__bf16)a, (__bf16)b}; return __builtin_bit_cast(u32, v); }
DI float blo(u32 x) { return __builtin_bit_cast(float, x << 16); }
DI float bhi(u32 x) { return __builtin_bit_cast(float, x & 0xffff0000u); }
template <class T> DI LAS T* lp(u32 a) { return (LAS T*)(size_t)a; }
DI v4u lds128(u32 a) { return *lp<v4u>(a); }
DI void lds128w(u32 a, v4u v) { *lp<v4u>(a) = v; }
DI void lds64w(u32 a, u32 x, u32 y) { *lp<v2u>(a) = v2u{x, y}; }
DI v4u ldstr(u32 a0, u32 a1) {
  s16x4 lo = __builtin_amdgcn_ds_read_tr16_b64_v4i16(lp<s16x4>(a0));
  s16x4 hi = __builtin_amdgcn_ds_read_tr16_b64_v4i16(lp<s16x4>(a1));
  v2u l = __builtin_bit_cast(v2u, lo), h = __builtin_bit_cast(v2u, hi);
  return v4u{l.x, l.y, h.x, h.y};
}
DI f32x4 mfma16(v4u a, v4u b, f32x4 c) { return __builtin_amdgcn_mfma_f32_16x16x32_bf16(__builtin_bit_cast(bf16x8, a), __builtin_bit_cast(bf16x8, b), c, 0, 0, 0); }
DI f32x16 mfma32(v4u a, v4u b, f32x16 c) { return __builtin_amdgcn_mfma_f32_32x32x16_bf16(__builtin_bit_cast(bf16x8, a), __builtin_bit_cast(bf16x8, b), c, 0, 0, 0); }
template <int CTRL> DI u32 dppu(u32 x) { return (u32)__builtin_amdgcn_mov_dpp((int)x, CTRL, 0xf, 0xf, true); }
DI float f_(u32 x) { return __builtin_bit_cast(float, x); }
DI u32 u_(float x) { return __builtin_bit_cast(u32, x); }
template <int CTRL> DI float dpp(float x) { return f_(dppu<CTRL>(u_(x))); }
constexpr int X1 = 0xB1, X2 = 0x4E, X7 = 0x141, X8 = 0x128;
DI float sum32(float x) { auto t = __builtin_amdgcn_permlane32_swap(u_(x), u_(x), false, false); return f_(t[0]) + f_(t[1]); }
DI float max32(float x) { auto t = __builtin_amdgcn_permlane32_swap(u_(x), u_(x), false, false); return fmaxf(f_(t[0]), f_(t[1])); }
DI float sum16(float x) { auto t = __builtin_amdgcn_permlane16_swap(u_(x), u_(x), false, false); return f_(t[0]) + f_(t[1]); }
DI float max16(float x) { auto t = __builtin_amdgcn_permlane16_swap(u_(x), u_(x), false, false); return fmaxf(f_(t[0]), f_(t[1])); }
DI void sig(int word, int lane) {
  asm volatile("" ::: "memory");
  if (lane == 0) __hip_atomic_fetch_add(lp<u32>(L_SYNC + 4 * word), 1u, __ATOMIC_RELAXED, __HIP_MEMORY_SCOPE_WORKGROUP);
  asm volatile("" ::: "memory");
}
template <bool SLEEP> DI void waitw(int word, u32 n) {
  while ((u32)__builtin_amdgcn_readfirstlane(__hip_atomic_load(lp<u32>(L_SYNC + 4 * word), __ATOMIC_RELAXED, __HIP_MEMORY_SCOPE_WORKGROUP)) < n) {
    if (SLEEP) __builtin_amdgcn_s_sleep(2);
  }
  asm volatile("" ::: "memory");
}
#define STUB(n) asm volatile("s_branch .Lsk" #n "\n.Lstub" #n ":\n\ts_setpc_b64 vcc\n.Lsk" #n ":")
#define TOUCH(n) asm volatile("s_call_b64 vcc, .Lstub" #n ::: "vcc")
#define BAR0(id) asm volatile("s_waitcnt lgkmcnt(0)\n\ts_barrier ; start " #id ::: "memory")
DI bool qbad(int lane) { return __any(*lp<u32>(L_EXP + (lane & 31) * 4) != *lp<u32>(L_GOT + (lane & 31) * 4)); }
DI void bar() { asm volatile("s_waitcnt lgkmcnt(0)\n\ts_barrier" ::: "memory"); }
#define LD1(off) __builtin_amdgcn_raw_buffer_load_b128(rs, (off), 0, 16)
#define ST1(v, off) __builtin_amdgcn_raw_buffer_store_b128((v), rs, (off), 0, 16)
#define TAGLOAD() __builtin_amdgcn_raw_buffer_load_b32(rs, WS_EPOCH, 0, 16)
DI v4u ldnt(const void* p, u32 off) { return __builtin_nontemporal_load((const GAS v4u*)((const GAS unsigned char*)p + off)); }
DI void stage_w(u32 base, const v4u (&park)[32], u32 g, u32 n) {
#pragma unroll
  for (int i = 0; i < 32; ++i) lds128w(base + g * 256 + i * 1024 + 16 * (n ^ ((g | (((i >> 1) & 1) << 2)) << 1)), park[i]);
}

__global__ void __launch_bounds__(512, 2)
fused_decode(const u16* __restrict__ xA, const u16* __restrict__ wqA, const u16* __restrict__ xB, const u16* __restrict__ wqB,
             unsigned char* ws_, const u16* __restrict__ ckv, const u16* __restrict__ wuk, const u16* __restrict__ kc,
             const u16* __restrict__ vc, const u16* __restrict__ wuv, const u16* __restrict__ woA, const u16* __restrict__ woB,
             u16* __restrict__ out) {
  typedef u32 s8u __attribute__((ext_vector_type(8)));
  typedef u32 s16u __attribute__((ext_vector_type(16)));
  const int tid = threadIdx.x, lane = tid & 63, wave = __builtin_amdgcn_readfirstlane(tid >> 6);
  s8u ka;
  s16u kb;
  if (wave == 0) asm volatile("s_load_dwordx8 %0, %2, 0x0\n\ts_load_dwordx16 %1, %2, 0x20" : "=&s"(ka), "=&s"(kb) : "s"(__builtin_amdgcn_kernarg_segment_ptr()));
  u64 pc0; asm volatile("s_getpc_b64 %0" : "=s"(pc0));
  const u32 gid = blockIdx.x, x = gid & 7, r = gid >> 3;
  const bool isA = (r & 4) == 0;
  const u32 l16 = (r & 3) | ((r >> 3) << 2);
  const u32 ht = 4 * x + (l16 >> 2), q4 = l16 & 3;
  const u32 g = lane >> 4, n = lane & 15, tq = n >> 2, tp = lane & 3;
  u32 warm = 0;
  unsigned char* ws;
  if (wave == 0) {
    const u32 rw = lane >> 3, sw = ((rw >> 1) & 1) << 1;
    const u32 vo = (isA ? (l16 >> 3) * 2097152 + x * 1024 + (l16 & 7) * 128 : (q4 >> 1) * 2097152 + ht * 256 + (q4 & 1) * 128) + rw * 8192;
    const u32 ce = vo + ((lane & 7) ^ sw) * 16, cd = vo + ((lane & 7) ^ sw ^ 4) * 16 + 64512;
    asm volatile("s_waitcnt lgkmcnt(0)" : "+s"(ka), "+s"(kb));
    STAMP(12);
    cptr wq = isA ? mkptr(ka[6], ka[7]) : mkptr(ka[2], ka[3]);
    const u32 G1 = isA && THOLD > 0 ? 1u : 8u;
    u64 t0; asm volatile("s_memrealtime %0" : "=s"(t0));
    if (XFIRST) dma2<L_X>(isA ? mkptr(ka[4], ka[5]) : mkptr(ka[0], ka[1]), lane * 16);
#pragma unroll 1
    for (u32 G = 0; G < G1; ++G) dma4r<63>(wq + G * 262144, ce, cd, ce + 129024, cd + 129024, L_WQ + G * 4096);
    if (!XFIRST) dma2<L_X>(isA ? mkptr(ka[4], ka[5]) : mkptr(ka[0], ka[1]), lane * 16);
    if (lane < 2) *lp<u32>(L_PTR + 4 * lane) = lane ? kb[1] : kb[0];
    BAR0(0);
    asm volatile("" : "+s"(t0));
    if (G1 < 8) {
      while (__builtin_amdgcn_s_memrealtime() - t0 < THOLD) __builtin_amdgcn_s_sleep(1);
#pragma unroll 1
      for (u32 G = 1; G < 8; ++G) dma4r<WINA>(wq + G * 262144, ce, cd, ce + 129024, cd + 129024, L_WQ + G * 4096);
    }
    STAMP(15);
  } else if (wave == 1) {
    const u32 ln = (r & 3) * 64 + lane;
    warm = ((const GAS u32*)(pc0 & ~127ull))[(ln < CODE_BYTES / 128 ? ln : 0u) * 32];
    SB();
  } else if (wave == 2) {
    if (lane < S_N) *lp<u32>(L_SYNC + 4 * lane) = 0;
  } else if (wave == 3) {
    if (isA) TOUCH(1); else TOUCH(2);
  }
  __amdgpu_buffer_rsrc_t rs;
#define SETWS(lo, hi) do { ws = (unsigned char*)mkptr(lo, hi); rs = __builtin_amdgcn_make_buffer_rsrc((void*)ws, 0, WS_BYTES, 0x00020000); } while (0)
#define START(id) do { BAR0(id); const v2u p_ = *lp<v2u>(L_PTR); SETWS(__builtin_amdgcn_readfirstlane(p_.x), __builtin_amdgcn_readfirstlane(p_.y)); } while (0)
  u32 tag;
#define SIG_AT(ISSUED, W, NEED, WORD) if constexpr ((ISSUED) - (W) >= (NEED) && (ISSUED) - 4 - (W) < (NEED)) { vmwait<W>(); sig(WORD, lane); }
  const u32 w = wave & 3;
  const u32 a0 = L_WQ + (8 * g + tq) * 128 + 16 * ((2 * w + (tp >> 1)) ^ ((((tq >> 1) & 1) | ((g & 1) << 1)) << 1)) + 8 * (tp & 1);
  const u32 ab = (8 * g + tq) * 256 + 16 * ((4 * w + (tp >> 1)) ^ ((tq | ((g & 1) << 2)) << 1)) + 8 * (tp & 1);

  if (isA) {
    if (wave == 0) {
      STUB(1);
      SETWS(kb[0], kb[1]);
      __builtin_amdgcn_s_setprio(1);
      cptr src = mkptr(kb[2], kb[3]) + (size_t)(x * 16 + l16) * 131072;
      u32 cv[16];
#pragma unroll
      for (int i = 0; i < 16; ++i) cv[i] = (lane ^ (((i & 3) << 2) | (i >> 2))) * 16;
      cptr qs = ws + WS_QABS + (x >> 2) * 32768;
#define Q_ISSUE() _Pragma("unroll 1") for (u32 H2 = 0; H2 < 2; ++H2) { _Pragma("unroll") for (int G = 0; G < 4; ++G) \
          dma4s(qs + H2 * 16384 + G * 4096, cv[4 * G], cv[4 * G + 1], cv[4 * G + 2], cv[4 * G + 3], L_Q + H2 * 16384 + G * 4096); }
#pragma unroll 1
      for (u32 H = 0; H < 8; ++H) {
        if (H == HPOLL) sig(S_CISS, lane);
        if (H == 6) waitw<true>(S_XRD, 6);
#pragma unroll
        for (int G = 0; G < 4; ++G) {
          dma4r<WINA>(src + H * 16384 + G * 4096, cv[4 * G], cv[4 * G + 1], cv[4 * G + 2], cv[4 * G + 3], L_C + H * 16384 + G * 4096);
          if (G == NA0 - 1 && H == 0) { vmwait<4 * NA0>(); sig(S_WQ, lane); }
        }
      }
      sig(S_CISS, lane);
      STAMP(1);
      waitw<false>(S_HINT, 1);
      STAMP(14);
      Q_ISSUE()
      vmwait<0>();
      STAMP(2);
      bar();
      v4u park[32];
      const u32 vo = (128 * q4 + g) * 8192 + ht * 256 + n * 16;
#pragma unroll
      for (int i = 0; i < 32; ++i) park[i] = ldnt(mkptr(kb[10], kb[11]), vo + i * 32768);
      const u32 tv = TAGLOAD();
      SB();
      STAMP(3);
      for (u32 it = 1; it <= QTRIES; ++it) {
        bar(); bar();
        if (!qbad(lane) || it == QTRIES) break;
        bar();
        Q_ISSUE()
        vmwait<0>();
        bar();
      }
      bar(); bar();
      stage_w(L_W1, park, g, n);
      tag = __builtin_amdgcn_readfirstlane(tv) + 1;
      STAMP(4);
    } else if (wave < 4) {
      START(1);
      if (wave == 1) {
        asm volatile("" :: "v"(warm));
        TOUCH(11); TOUCH(12); TOUCH(13); TOUCH(14); TOUCH(15); TOUCH(16); TOUCH(17); TOUCH(18); TOUCH(19);
        if (lane < 6) { u32 t = __builtin_amdgcn_raw_buffer_load_b32(rs, lane * 1048576 + 1048576 - 64, 0, 16); asm volatile("" :: "v"(t)); }
      }
      if (wave != 2) sig(S_XRD, lane);
      tag = __builtin_amdgcn_readfirstlane(TAGLOAD()) + 1;
      v4u h0, h1;
      if (wave == 2) {
        __builtin_amdgcn_s_setprio(2);
        waitw<true>(S_CISS, 1);
        for (int spin = 0; spin < 64; ++spin) {
          h0 = LD1(WS_HINT + lane * 128); h1 = LD1(WS_HINT + 8192 + lane * 128);
          asm volatile("" ::: "memory");
          if (__all(h0.z == tag && h1.z == tag)) break;
          if (spin == 0) waitw<false>(S_CISS, 2);
        }
        sig(S_HINT, lane);
        STAMP(1);
      }
      bar();
      for (u32 it = 1; it <= QTRIES; ++it) {
        bar();
        if (wave == 2) {
          u32 e0 = (x & 4 ? h0.y : h0.x) - tag, e1 = (x & 4 ? h1.y : h1.x) - tag;
          e0 += dppu<X1>(e0); e1 += dppu<X1>(e1); e0 += dppu<X2>(e0); e1 += dppu<X2>(e1);
          if (tp < 2) *lp<u32>(L_EXP + (lane >> 2) * 4 + tp * 64) = tp ? e1 : e0;
        }
        bar();
        if (!qbad(lane) || it == QTRIES) break;
        bar();
        if (wave == 2) { h0 = LD1(WS_HINT + lane * 128); h1 = LD1(WS_HINT + 8192 + lane * 128); }
        bar();
      }
      bar(); bar();
      if (wave == 1) *lp<u32>(L_BAD) = 0;
    } else {
      START(2);
      tag = __builtin_amdgcn_readfirstlane(TAGLOAD()) + 1;
      const u32 bs = x * 16 + l16, b = bs >> 6, hh = lane >> 5, hd = lane & 31;
      __builtin_amdgcn_s_setprio(2);
      {
        waitw<false>(S_WQ, 1);
        v4u xf[8], af[8];
#pragma unroll
        for (int s = 0; s < 8; ++s) { af[s] = ldstr(a0 + s * 4096, a0 + s * 4096 + 512); xf[s] = lds128(L_X + (lane & 1) * 1024 + (l16 >> 3) * 512 + g * 16 + s * 64); }
        sig(S_XRD, lane);
        SB();
        f32x4 c0 = {0.f, 0.f, 0.f, 0.f}, c1 = c0;
#pragma unroll
        for (int s = 0; s < 8; s += 2) { c0 = mfma16(af[s], xf[s], c0); c1 = mfma16(af[s + 1], xf[s + 1], c1); }
        c0 += c1;
        if (n < 4) ST1((v4u{u_((n & 2) ? c0[2] : c0[0]), tag, u_((n & 2) ? c0[3] : c0[1]), tag}),
                       WS_QB + x * 16384 + (n & 1) * 8192 + l16 * 512 + (8 * w + 2 * g + (n >> 1)) * 16);
        STAMP(13);
      }
      STUB(11);
      f32x16 s;
      v4u mm;
      for (u32 it = 1; it <= QTRIES; ++it) {
        u32 cs = 0;
        bar();
        STAMP(3);
#pragma unroll
        for (int i = 0; i < 16; ++i) s[i] = 0.f;
        {
          const u32 fr = ((hd & 3) << 2) | ((hd >> 2) & 3);
          const u32 qa = L_Q + hd * 1024 + 16 * (hh ^ fr), ca = qa - L_Q + L_C + w * 32768;
          v4u af[32], bf[32];
          constexpr int PD = 6;
#define S_RD(k) do { bf[k] = lds128((qa ^ (32 * ((k) & 7))) + 256 * ((k) >> 3)); af[k] = lds128((ca ^ (32 * ((k) & 7))) + 256 * ((k) >> 3)); } while (0)
#pragma unroll
          for (int k = 0; k < PD; ++k) S_RD(k);
          SB();
#pragma unroll
          for (int k = 0; k < 32; ++k) {
            if (k + PD < 32) S_RD(k + PD);
            s = mfma32(af[k], bf[k], s);
            cs = cs + bf[k].x + bf[k].y; cs = cs + bf[k].z + bf[k].w;
            if (k == 4) STUB(12);
            if (k == 26) { STUB(13); }
            if (k == 29) bar();
            SB();
          }
        }
        { auto t = __builtin_amdgcn_permlane32_swap(cs, cs, false, false); cs = t[0] + t[1]; }
        float m = s[0];
#pragma unroll
        for (int i = 1; i < 16; ++i) m = fmaxf(m, s[i]);
        m = max32(m);
        if (hh == 0) *lp<float>(L_MAXX + hd * 16 + w * 4) = m;
        if (w == 0 && hh == 0) *lp<u32>(L_GOT + hd * 4) = cs;
        STAMP(4);
        bar();
        mm = lds128(L_MAXX + hd * 16);
        if (!qbad(lane) || it == QTRIES) break;
        bar();
      }
      const float mk = fmaxf(fmaxf(f_(mm.x), f_(mm.y)), fmaxf(f_(mm.z), f_(mm.w))) * KSC;
      float ls = 0.f;
#pragma unroll
      for (int i = 0; i < 16; ++i) { s[i] = __builtin_amdgcn_exp2f(fmaf(s[i], KSC, -mk)); ls += s[i]; }
      lds128w(L_P + (2 * w) * 1024 + lane * 16, v4u{pk(s[0], s[1]), pk(s[2], s[3]), pk(s[4], s[5]), pk(s[6], s[7])});
      lds128w(L_P + (2 * w + 1) * 1024 + lane * 16, v4u{pk(s[8], s[9]), pk(s[10], s[11]), pk(s[12], s[13]), pk(s[14], s[15])});
      ls = sum32(ls);
      if (hh == 0) *lp<float>(L_LSUM + hd * 16 + w * 4) = ls;
      const u32 blk = (lane >> 4) & 1;
      u32 ta[2];
#pragma unroll
      for (int t = 0; t < 2; ++t)
        ta[t] = L_C + (4 * hh + 8 * t + tq) * 1024 + 256 * w + 16 * ((2 * blk + (tp >> 1)) ^ ((tq << 2) | (hh + 2 * t))) + 8 * (tp & 1);
      v4u cf[32];
      constexpr int PV_PD = 5;
#define PV_RD(kk) cf[kk] = ldstr((ta[0] ^ (64 * ((kk) >> 3))) + ((kk) & 7) * 16384, (ta[1] ^ (64 * ((kk) >> 3))) + ((kk) & 7) * 16384)
#pragma unroll
      for (int k = 0; k < PV_PD; ++k) PV_RD(k);
      SB();
      STUB(14);
      bar();
      STAMP(5);
      v4u pf[8];
#pragma unroll
      for (int k = 0; k < 8; ++k) pf[k] = lds128(L_P + k * 1024 + lane * 16);
      const v4u lv = lds128(L_LSUM + hd * 16);
      SB();
      const u32 scw = L_SCR + w * 4096 + hd * 128 + 8 * hh + 16 * (hd & 7);
      const u32 scr = L_SCR + w * 4096 + (lane >> 3) * 128 + 16 * ((lane & 7) ^ (lane >> 3));
      const u32 sto = WS_APART + (((lane >> 3) * 4 + w) * 128 + bs) * 256 + 16 * (lane & 7);
      f32x16 o[4];
      v4u ov[4];
      u32 cs = tag;
#define EPI_W(c, gg) do { \
        const u32 p0 = pk(o[c][4 * (gg)], o[c][4 * (gg) + 1]), p1 = pk(o[c][4 * (gg) + 2], o[c][4 * (gg) + 3]); \
        lds64w(scw ^ (64 * ((c) & 1) + 16 * (gg)), p0, p1); cs += p0 + p1; asm volatile("" : "+v"(cs)); } while (0)
#define EPI_R() _Pragma("unroll") for (int i = 0; i < 4; ++i) ov[i] = lds128(scr + i * 1024);
#define EPI_S(p, i) ST1(ov[i], sto + 128 * (p) + (i) * (8 * 4 * 128 * 256))
#pragma unroll
      for (int kk = 0; kk < 32; ++kk) {
        const int c = kk >> 3, k = kk & 7;
        if (kk + PV_PD < 32) PV_RD(kk + PV_PD);
        if (k == 0) {
          f32x16 z;
#pragma unroll
          for (int i = 0; i < 16; ++i) z[i] = 0.f;
          o[c] = mfma32(cf[kk], pf[k], z);
        } else o[c] = mfma32(cf[kk], pf[k], o[c]);
        if (c > 0 && k >= 1 && k <= 4) EPI_W(c > 0 ? c - 1 : 0, (k - 1) & 3);
        if (kk == 20) { EPI_R() }
        if (kk == 23) { EPI_S(0, 0); EPI_S(0, 1); }
        if (kk == 24) { EPI_S(0, 2); EPI_S(0, 3); }
        if (kk == 13) STUB(15);
        if (kk == 22) STUB(16);
        SB();
      }
      EPI_W(3, 0); EPI_W(3, 1); EPI_W(3, 2); EPI_W(3, 3); EPI_R()
      const float lsum = (f_(lv.x) + f_(lv.y)) + (f_(lv.z) + f_(lv.w));
      { auto t = __builtin_amdgcn_permlane32_swap(cs, cs, false, false); cs = t[0] + t[1] - tag + u_(mk) + u_(lsum); }
      EPI_S(1, 0); EPI_S(1, 1); EPI_S(1, 2); EPI_S(1, 3);
      if (hh == 0) ST1((v4u{u_(mk), u_(lsum), cs, tag}), WS_ASTAT + bs * 2048 + w * 512 + hd * 16);
      STAMP(7);
      bar();
    }
    const u32 v = wave;
    v4u a1[8], bq[4];
    STUB(17);
    const u64 tpv = __builtin_amdgcn_s_memrealtime();
    bar();
    if (wave >= 4) {
#pragma unroll
      for (int s = 0; s < 8; ++s) { const u32 a = L_W1 + (ab ^ (32 * (s >> 2))) + (s & 3) * 8192; a1[s] = ldstr(a, a + 1024); }
    }
    if (wave == 3) {
      while (__builtin_amdgcn_s_memrealtime() - tpv < PDELAY) { }
    }
    for (u32 it = 257; it < 320; ++it) {
      v4u st0, st1;
      if (wave == 3) {
        for (int spin = 0; spin < SPIN_CAP; ++spin) {
          st0 = LD1(WS_ASTAT + lane * 2048 + q4 * 512 + ht * 16);
          st1 = LD1(WS_ASTAT + 131072 + lane * 2048 + q4 * 512 + ht * 16);
          asm volatile("" ::: "memory");
          if (__all(st0.w == tag && st1.w == tag)) break;
        }
        STAMP(2);
      }
      bar();
      STAMP(8);
      v4u pd[4];
#pragma unroll
      for (int i = 0; i < 4; ++i) pd[i] = LD1(WS_APART + (ht * 4 + q4) * 32768 + v * 4096 + i * 1024 + lane * 16);
      SB();
      if (wave == 3) {
#pragma unroll
        for (int i = 0; i < 2; ++i) {
          const v4u st = i ? st1 : st0;
          float M = f_(st.x);
          M = fmaxf(M, dpp<X1>(M)); M = fmaxf(M, dpp<X2>(M)); M = fmaxf(M, dpp<X7>(M)); M = fmaxf(M, dpp<X8>(M)); M = max16(M); M = max32(M);
          const float e = __builtin_amdgcn_exp2f(f_(st.x) - M);
          float S = e * f_(st.y);
          S += dpp<X1>(S); S += dpp<X2>(S); S += dpp<X7>(S); S += dpp<X8>(S); S = sum16(S); S = sum32(S);
          *lp<float>(L_WGT + i * 256 + lane * 4) = e / S;
          *lp<u32>(L_CK + i * 256 + lane * 4) = st.z - tag - st.x - st.y;
        }
      }
      STUB(18);
      bar();
      float acc[8], wt[4];
#pragma unroll
      for (int i = 0; i < 4; ++i) wt[i] = *lp<float>(L_WGT + (v * 16 + 4 * i + g) * 4);
      const u32 ck = *lp<u32>(L_CK + (v * 16 + 4 * (lane & 3) + g) * 4);
#pragma unroll
      for (int k = 0; k < 4; ++k) { acc[2 * k] = wt[0] * blo(pd[0][k]); acc[2 * k + 1] = wt[0] * bhi(pd[0][k]); }
#pragma unroll
      for (int i = 1; i < 4; ++i)
#pragma unroll
        for (int k = 0; k < 4; ++k) { acc[2 * k] = fmaf(wt[i], blo(pd[i][k]), acc[2 * k]); acc[2 * k + 1] = fmaf(wt[i], bhi(pd[i][k]), acc[2 * k + 1]); }
      STAMP(9);
      {
        float y[4];
#pragma unroll
        for (int k = 0; k < 4; ++k) { auto t = __builtin_amdgcn_permlane32_swap(u_(acc[k]), u_(acc[k + 4]), false, false); y[k] = f_(t[0]) + f_(t[1]); }
        float z[2];
#pragma unroll
        for (int k = 0; k < 2; ++k) { auto t = __builtin_amdgcn_permlane16_swap(u_(y[k]), u_(y[k + 2]), false, false); z[k] = f_(t[0]) + f_(t[1]); }
        const u32 hi = pk(z[0], z[1]), lo = pk(z[0] - blo(hi), z[1] - bhi(hi));
        *lp<u32>(L_B1 + v * 272 + (8 * n + 2 * g) * 2) = hi;
        *lp<u32>(L_B1 + (8 + v) * 272 + (8 * n + 2 * g) * 2) = lo;
        u32 c4[4], c2[2];
#pragma unroll
        for (int i = 0; i < 4; ++i) c4[i] = (pd[i].x + pd[i].y) + (pd[i].z + pd[i].w);
#pragma unroll
        for (int i = 0; i < 2; ++i) { const u32 p = c4[i] + dppu<X2>(c4[i]), q = c4[i + 2] + dppu<X2>(c4[i + 2]); c2[i] = (lane & 2) ? q : p; }
        const u32 p1 = c2[0] + dppu<X1>(c2[0]), q1 = c2[1] + dppu<X1>(c2[1]);
        u32 c1 = (lane & 1) ? q1 : p1;
        c1 += dppu<X8>(c1);
        c1 += dppu<0x124>(c1);
        if (c1 != ck) *lp<u32>(L_BAD) = it;
      }
      STUB(19);
      bar();
      const u32 bad = *lp<u32>(L_BAD);
#pragma unroll
      for (int s = 0; s < 4; ++s) bq[s] = lds128(L_B1 + n * 272 + g * 16 + s * 64);
      SB();
      if (__builtin_amdgcn_readfirstlane(bad) != it) break;
    }
    if (wave >= 4) {
      f32x4 d0 = {0.f, 0.f, 0.f, 0.f}, d1 = d0;
#pragma unroll
      for (int s = 0; s < 4; ++s) { d0 = mfma16(a1[s], bq[s], d0); d1 = mfma16(a1[4 + s], bq[s], d1); }
#pragma unroll
      for (int k = 0; k < 4; ++k) {
        const float t = d0[k] + dpp<X1>(d0[k]), u = d1[k] + dpp<X1>(d1[k]);
        float z = (n & 1) ? u : t;
        z += dpp<X2>(z); z += dpp<X8>(z); d0[k] = z;
      }
      if ((n & 10) == 0) ST1((v4u{pk(d0[0], d0[1]), tag, pk(d0[2], d0[3]), tag}), WS_H3B + ht * 4096 + q4 * 1024 + (n >> 2) * 512 + (8 * w + 4 * (n & 1) + g) * 16);
    }
    STAMP(10);
    if (wave == 0) {
      const u32 rho = x * 8 + (l16 >> 1), part = l16 & 1;
      u16* outp = out; asm volatile("" : "+s"(outp));
      { const u64 t0 = __builtin_amdgcn_s_memrealtime(); while (__builtin_amdgcn_s_memrealtime() - t0 < (part ? 0 : FDELAY)) { } }
      v4u e[4];
      for (int spin = 0; spin < SPIN_CAP; ++spin) {
        bool ok = true;
#pragma unroll
        for (int i = 0; i < 4; ++i) e[i] = LD1(WS_H4 + part * 262144 + rho * 4096 + i * 1024 + lane * 16);
        asm volatile("" ::: "memory");
#pragma unroll
        for (int i = 0; i < 4; ++i) ok = ok && e[i].y == tag && e[i].w == tag;
        if (__all(ok)) break;
      }
      float s0 = (f_(e[0].x) + f_(e[1].x)) + (f_(e[2].x) + f_(e[3].x)), s1 = (f_(e[0].z) + f_(e[1].z)) + (f_(e[2].z) + f_(e[3].z));
      s0 += dpp<X8>(s0); s1 += dpp<X8>(s1);
      s0 = sum32(sum16(s0)); s1 = sum32(sum16(s1));
      if (lane < 8) *(GAS u32*)(outp + (rho & 1) * 1024 + part * 512 + (rho >> 1) * 16 + 2 * lane) = pk(s0, s1);
      if (gid == 0 && lane == 0) __builtin_amdgcn_raw_buffer_store_b32(tag, rs, WS_EPOCH, 0, 16);
      STAMP(13);
    }
  } else {
    if (wave < 4) {
      if (wave == 0) {
        STUB(2);
        SETWS(kb[0], kb[1]);
        __builtin_amdgcn_s_setprio(1);
        const u32 uo = (128 * q4 + g) * 8192 + ht * 256;
        {
          u32 uv[4];
#pragma unroll
          for (int i = 0; i < 4; ++i) uv[i] = uo + ((n ^ (g | (i << 2))) * 16) + (i & 1) * 63488 + (i >> 1) * 131072;
          cptr uk = mkptr(kb[4], kb[5]);
#pragma unroll 1
          for (u32 H = 0; H < 4; ++H) {
            dma4r<32>(uk + H * 262144, uv[0], uv[0] + 31744, uv[1], uv[1] + 31744, L_UK + H * 8192);
            dma4r<32>(uk + H * 262144, uv[2], uv[2] + 31744, uv[3], uv[3] + 31744, L_UK + H * 8192 + 4096);
          }
        }
        vmwait<32>(); sig(S_WQ, lane);
        STAMP(14);
        vmwait<0>(); sig(S_UK, lane);
        tag = __builtin_amdgcn_readfirstlane(TAGLOAD()) + 1;
        STUB(3);
        waitw<false>(S_QBISS, 1);
        { const u64 t1 = __builtin_amdgcn_s_memrealtime(); while (__builtin_amdgcn_s_memrealtime() - t1 < KDELAY) __builtin_amdgcn_s_sleep(1); }
        STAMP(13);
        const size_t base = (((size_t)(l16 >> 3) * 8 + x) * 8192 + 6144 + (l16 & 7) * 256) * 256;
        cptr ks = mkptr(kb[6], kb[7]) + base, vs = mkptr(kb[8], kb[9]) + base;
        static_assert(WINB % 4 == 0 && NB0 <= 3 + WINB / 4, "block signals");
        {
          u32 kv[4];
#pragma unroll
          for (int i = 0; i < 4; ++i) kv[i] = g * 256 + ((n ^ (4 * i + g)) * 16);
#pragma unroll 1
          for (u32 G = 0; G < NB0; ++G) dma4r<WINB0>(ks + G * 4096, kv[0], kv[1], kv[2], kv[3], L_K + G * 4096);
#pragma unroll 1
          for (u32 G = NB0; G < 16; ++G) {
            dma4r<WINB>(ks + G * 4096, kv[0], kv[1], kv[2], kv[3], L_K + G * 4096);
            if (((G + 1 - WINB / 4) & 3) == 0 && G >= 3 + WINB / 4) { vmwait<WINB>(); sig(S_LAND, lane); }
          }
        }
        STAMP(1);
        {
          const u32 v0 = g * 256 + ((n ^ (g << 1)) * 16), v1 = g * 256 + ((n ^ ((4 + g) << 1)) * 16);
#pragma unroll 1
          for (u32 G = 0; G < 16; ++G) {
            if (ABL != 3) dma4r<WINB>(vs + G * 4096, v0, v1, v0, v1, L_V + G * 4096);
            if (((G + 1 - WINB / 4) & 3) == 0 && G + 16 >= 3 + WINB / 4) { vmwait<WINB>(); sig(S_LAND, lane); }
          }
        }
        STAMP(2);
        waitw<false>(S_KRD, 4);
        {
          const u32 vo = (ht * 128 + g) * 1024 + q4 * 256, w0 = vo + ((n ^ (g << 1)) * 16), w1 = vo + ((n ^ ((g | 4) << 1)) * 16) + 6144;
#pragma unroll 1
          for (u32 G = 0; G < 16; ++G) {
            cptr wp = (G < 8 ? mkptr(kb[14], kb[15]) : mkptr(kb[12], kb[13])) + (G & 7) * 16384;
            if (ABL != 3) dma4r<WINB>(wp, w0, w0 + 3072, w1, w1 + 3072, L_W1 + G * 4096);
            if (((G + 1 - WINB / 4) & 3) == 0) { vmwait<WINB>(); sig(S_LAND, lane); }
          }
        }
        STAMP(3);
        vmwait<32>(); sig(S_W1, lane);
        vmwait<0>(); sig(S_W2, lane);
        STAMP(4);
      } else if (wave == 1) {
        START(3);
        __builtin_amdgcn_s_setprio(2);
        asm volatile("" :: "v"(warm));
        TOUCH(4); TOUCH(3); TOUCH(5); TOUCH(6); TOUCH(7); TOUCH(8); TOUCH(9); TOUCH(10);
        if (lane < 2) { u32 t = __builtin_amdgcn_raw_buffer_load_b32(rs, lane * 1048576 + 1048576 - 64, 0, 16); asm volatile("" :: "v"(t)); }
        tag = __builtin_amdgcn_readfirstlane(TAGLOAD()) + 1;
        waitw<false>(S_PUB, 4);
        {
          const v4u pv = lds128(L_QX + (lane & 31) * 16);
          if (lane < 32) ST1(pv, WS_QABS + (lane >> 4) * 32768 + ht * 1024 + q4 * 256 + n * 16);
          u32 cs = (pv.x + pv.y) + (pv.z + pv.w);
          cs += dppu<X1>(cs); cs += dppu<X2>(cs); cs += dppu<X7>(cs); cs += dppu<X8>(cs);
          auto t = __builtin_amdgcn_permlane16_swap(cs, cs, false, false);
          if (lane == 0) ST1((v4u{t[0] + tag, t[1] + tag, tag, tag}), WS_HINT + (x * 16 + l16) * 128);
        }
        STAMP(2);
        v4u e[8];
        for (int spin = 0; spin < SPIN_CAP; ++spin) {
          bool ok = true;
#pragma unroll
          for (int i = 0; i < 8; ++i) e[i] = LD1(WS_QB + x * 16384 + (l16 >> 3) * 8192 + i * 1024 + lane * 16);
          SB();
          if (!BGATE && spin == 0) sig(S_QBISS, lane);
          asm volatile("" ::: "memory");
#pragma unroll
          for (int i = 0; i < 8; ++i) ok = ok && e[i].y == tag && e[i].w == tag;
          if (__all(ok)) break;
        }
#pragma unroll
        for (int i = 0; i < 4; ++i) *lp<u32>(L_QBL + i * 256 + lane * 4) = pk(f_(e[i].x) + f_(e[4 + i].x), f_(e[i].z) + f_(e[4 + i].z));
        sig(S_QBRDY, lane);
        STAMP(3);
        if (BGATE) {
          for (int spin = 0; spin < 6; ++spin) {
            const u32 h0 = __builtin_amdgcn_raw_buffer_load_b32(rs, WS_HINT + 8 + lane * 128, 0, 16), h1 = __builtin_amdgcn_raw_buffer_load_b32(rs, WS_HINT + 8 + 8192 + lane * 128, 0, 16);
            asm volatile("" ::: "memory");
            if (__all(h0 == tag && h1 == tag)) break;
          }
          sig(S_QBISS, lane);
        }
      } else {
        START(4);
        tag = __builtin_amdgcn_readfirstlane(TAGLOAD()) + 1;
      }
      STUB(10);
      waitw<true>(S_W2, 1);
      v4u a2[8];
#pragma unroll
      for (int s = 0; s < 8; ++s) { const u32 a = L_W2 + (ab ^ (32 * (s >> 2))) + (s & 3) * 8192; a2[s] = ldstr(a, a + 1024); }
      v4u e;
      for (int spin = 0; spin < SPIN_CAP; ++spin) {
        e = LD1(WS_H3B + ht * 4096 + w * 1024 + lane * 16);
        asm volatile("" ::: "memory");
        if (__all(e.y == tag && e.w == tag)) break;
      }
      lds64w(L_B2 + ((lane >> 5) * 4 + w) * 272 + (lane & 31) * 8, e.x, e.z);
      sig(S_TA, lane);
      waitw<false>(S_TA, 4);
      STAMP(11);
      v4u bq[4];
#pragma unroll
      for (int s = 0; s < 4; ++s) bq[s] = lds128(L_B2 + (n & 7) * 272 + g * 16 + s * 64);
      SB();
      f32x4 d0 = {0.f, 0.f, 0.f, 0.f}, d1 = d0;
#pragma unroll
      for (int s = 0; s < 4; ++s) { d0 = mfma16(a2[s], bq[s], d0); d1 = mfma16(a2[4 + s], bq[s], d1); }
      f32x4 d = (n & 8) ? d1 : d0;
#pragma unroll
      for (int k = 0; k < 4; ++k) { float t = d[k]; t += dpp<X1>(t); t += dpp<X2>(t); d[k] = t; }
      const bool k1 = (n & 1) != 0;
      if ((n & 2) == 0) ST1((v4u{u_(k1 ? d[2] : d[0]), tag, u_(k1 ? d[3] : d[1]), tag}),
                            WS_H4 + (((q4 * 8 + 2 * w + (n >> 3)) * 2 + ((n >> 2) & 1)) * 32 + ht) * 128 + g * 32 + (n & 1) * 16);
      STAMP(12);
    } else {
      START(5);
      tag = __builtin_amdgcn_readfirstlane(TAGLOAD()) + 1;
      __builtin_amdgcn_s_setprio(2);
      {
        waitw<false>(S_WQ, 1);
        STAMP(10);
        v4u xf[8], af[8];
#pragma unroll
        for (int s = 0; s < 8; ++s) { af[s] = ldstr(a0 + s * 4096, a0 + s * 4096 + 512); xf[s] = lds128(L_X + (lane & 1) * 1024 + (q4 >> 1) * 512 + g * 16 + s * 64); }
        SB();
        f32x4 c0 = {0.f, 0.f, 0.f, 0.f}, c1 = c0;
#pragma unroll
        for (int s = 0; s < 8; s += 2) { c0 = mfma16(af[s], xf[s], c0); c1 = mfma16(af[s + 1], xf[s + 1], c1); }
        c0 += c1;
        if (n < 4) ST1((v4u{u_((n & 2) ? c0[2] : c0[0]), tag, u_((n & 2) ? c0[3] : c0[1]), tag}),
                       WS_H1 + ht * 4096 + q4 * 1024 + (n & 1) * 512 + (8 * w + 2 * g + (n >> 1)) * 16);
        STAMP(11);
        STUB(4);
        waitw<false>(S_UK, 1);
        v4u uk[8];
#pragma unroll
        for (int t = 0; t < 2; ++t)
#pragma unroll
          for (int s = 0; s < 4; ++s) uk[t * 4 + s] = lds128(L_UK + (32 * w + 8 * (n >> 2) + 4 * t + (n & 3)) * 256 + 16 * ((4 * s + g) ^ n));
        v4u e[4];
        for (int spin = 0; spin < SPIN_CAP; ++spin) {
          bool ok = true;
#pragma unroll
          for (int i = 0; i < 4; ++i) e[i] = LD1(WS_H1 + ht * 4096 + i * 1024 + lane * 16);
          asm volatile("" ::: "memory");
#pragma unroll
          for (int i = 0; i < 4; ++i) ok = ok && e[i].y == tag && e[i].w == tag;
          if (__all(ok)) break;
        }
        STAMP(14);
        const u32 ql = L_QL + w * 512;
#pragma unroll
        for (int d = 0; d < 2; ++d)
          *lp<u32>(ql + (lane >> 5) * 256 + d * 128 + (lane & 31) * 4) = pk(f_(e[d].x) + f_(e[2 + d].x), f_(e[d].z) + f_(e[2 + d].z));
        v4u qb[4];
#pragma unroll
        for (int s = 0; s < 4; ++s) qb[s] = lds128(ql + (lane & 1) * 256 + g * 16 + s * 64);
        SB();
        f32x4 u0 = {0.f, 0.f, 0.f, 0.f}, u1 = u0;
#pragma unroll
        for (int s = 0; s < 4; ++s) { u0 = mfma16(uk[s], qb[s], u0); u1 = mfma16(uk[4 + s], qb[s], u1); }
        const v4u pv = {pk(u0[0], u0[1]), pk(u0[2], u0[3]), pk(u1[0], u1[1]), pk(u1[2], u1[3])};
        if (n < 2) lds128w(L_QX + n * 256 + w * 64 + g * 16, pv);
        sig(S_PUB, lane);
        STAMP(15);
      }
      STUB(5);
      waitw<true>(S_QBRDY, 1);
      v4u qf[4];
#pragma unroll
      for (int s = 0; s < 4; ++s) qf[s] = lds128(L_QBL + (lane & 3) * 256 + g * 16 + s * 64);
      waitw<true>(S_LAND, w + 1);
      STAMP(3);
      f32x4 sc[4];
      {
        v4u kf[16];
        const u32 ka = L_K + (64 * w + n) * 256 + 16 * (g ^ n);
#pragma unroll
        for (int i = 0; i < 16; ++i) kf[i] = lds128((ka ^ (64 * (i & 3))) + (i >> 2) * 4096);
        sig(S_KRD, lane);
        SB();
#pragma unroll
        for (int t = 0; t < 4; ++t) {
          sc[t] = f32x4{0.f, 0.f, 0.f, 0.f};
#pragma unroll
          for (int s = 0; s < 4; ++s) sc[t] = mfma16(kf[4 * t + s], qf[s], sc[t]);
        }
      }
      float m = sc[0][0];
#pragma unroll
      for (int i = 1; i < 16; ++i) m = fmaxf(m, sc[i >> 2][i & 3]);
      m = max32(max16(m));
      if (lane < 4) *lp<float>(L_MAXX + lane * 16 + w * 4) = m;
      sig(S_MAX, lane);
      STUB(6);
      waitw<false>(S_MAX, 4);
      const v4u mm = lds128(L_MAXX + (lane & 3) * 16);
      const float mk = fmaxf(fmaxf(f_(mm.x), f_(mm.y)), fmaxf(f_(mm.z), f_(mm.w))) * KSC;
      float ls = 0.f;
#pragma unroll
      for (int i = 0; i < 16; ++i) { const float p = __builtin_amdgcn_exp2f(fmaf(sc[i >> 2][i & 3], KSC, -mk)); sc[i >> 2][i & 3] = p; ls += p; }
      ls = sum32(sum16(ls));
      if (n < 4) {
#pragma unroll
        for (int u = 0; u < 2; ++u)
          lds128w(L_PB + (2 * w + u) * 256 + g * 64 + n * 16,
                  v4u{pk(sc[2 * u][0], sc[2 * u][1]), pk(sc[2 * u][2], sc[2 * u][3]), pk(sc[2 * u + 1][0], sc[2 * u + 1][1]), pk(sc[2 * u + 1][2], sc[2 * u + 1][3])});
      }
      if (lane < 4) *lp<float>(L_LSUM + lane * 16 + w * 4) = ls;
      sig(S_P, lane);
      STUB(7);
      waitw<false>(S_P, 4);
      STAMP(5);
      v4u pf[8];
#pragma unroll
      for (int k = 0; k < 8; ++k) pf[k] = lds128(L_PB + k * 256 + g * 64 + (lane & 3) * 16);
      if (w == 0) {
        const v4u lv = lds128(L_LSUM + (lane & 3) * 16);
        const float lsum = (f_(lv.x) + f_(lv.y)) + (f_(lv.z) + f_(lv.w));
        if (lane < 4) ST1((v4u{u_(mk), tag, u_(lsum), tag}), WS_BIN + (4 * x + lane) * 9216 + 8192 + l16 * 16);
      }
      f32x4 o0 = {0.f, 0.f, 0.f, 0.f}, o1 = o0;
      const u32 va = L_V + (4 * g + tq) * 256 + 16 * ((4 * w + (tp >> 1)) ^ ((4 * (g & 1) + tq) << 1)) + 8 * (tp & 1);
#pragma unroll
      for (int k2 = 0; k2 < 4; ++k2) {
        waitw<false>(S_LAND, k2 + 5);
        v4u vf[4];
#pragma unroll
        for (int i = 0; i < 4; ++i) { const u32 a = (va ^ (32 * (i & 1))) + (2 * k2 + (i >> 1)) * 8192; vf[i] = ldstr(a, a + 4096); }
        SB();
        o0 = mfma16(vf[0], pf[2 * k2], o0); o1 = mfma16(vf[1], pf[2 * k2], o1);
        o0 = mfma16(vf[2], pf[2 * k2 + 1], o0); o1 = mfma16(vf[3], pf[2 * k2 + 1], o1);
      }
      STAMP(6);
      {
        const f32x4 o = (n & 4) ? o1 : o0;
        if (n < 8) ST1((v4u{pk(o[0], o[1]), tag, pk(o[2], o[3]), tag}), WS_BIN + (4 * x + (n & 3)) * 9216 + l16 * 512 + (4 * (2 * w + (n >> 2)) + g) * 16);
      }
      STAMP(7);
      STUB(8);
      waitw<false>(S_W1, 1);
      STAMP(8);
      v4u a1[8];
#pragma unroll
      for (int s = 0; s < 8; ++s) { const u32 a = L_W1 + (ab ^ (32 * (s >> 2))) + (s & 3) * 8192; a1[s] = ldstr(a, a + 1024); }
      v4u e0, e1, st;
      for (int spin = 0; spin < SPIN_CAP; ++spin) {
        e0 = LD1(WS_BIN + ht * 9216 + w * 2048 + lane * 16);
        e1 = LD1(WS_BIN + ht * 9216 + w * 2048 + 1024 + lane * 16);
        st = LD1(WS_BIN + ht * 9216 + 8192 + n * 16);
        asm volatile("" ::: "memory");
        if (__all(e0.y == tag && e0.w == tag && e1.y == tag && e1.w == tag && st.y == tag && st.w == tag)) break;
      }
      STAMP(9);
      lds64w(L_BB + (4 * w + (lane >> 5)) * 272 + (lane & 31) * 8, e0.x, e0.z);
      lds64w(L_BB + (4 * w + 2 + (lane >> 5)) * 272 + (lane & 31) * 8, e1.x, e1.z);
      sig(S_TB, lane);
      float M = f_(st.x);
      M = fmaxf(M, dpp<X1>(M)); M = fmaxf(M, dpp<X2>(M)); M = fmaxf(M, dpp<X7>(M));
      const float ee = __builtin_amdgcn_exp2f(f_(st.x) - M);
      float S = ee * f_(st.z);
      S += dpp<X1>(S); S += dpp<X2>(S); S += dpp<X7>(S);
      const float wt = ee / S;
      STUB(9);
      waitw<false>(S_TB, 4);
      v4u bq[4];
#pragma unroll
      for (int s = 0; s < 4; ++s) bq[s] = lds128(L_BB + n * 272 + g * 16 + s * 64);
      SB();
      f32x4 d0 = {0.f, 0.f, 0.f, 0.f}, d1 = d0;
#pragma unroll
      for (int s = 0; s < 4; ++s) { d0 = mfma16(a1[s], bq[s], d0); d1 = mfma16(a1[4 + s], bq[s], d1); }
#pragma unroll
      for (int k = 0; k < 4; ++k) {
        float t = d0[k] * wt, u = d1[k] * wt;
        t += dpp<X1>(t); u += dpp<X1>(u); t += dpp<X2>(t); u += dpp<X2>(u); t += dpp<X7>(t); u += dpp<X7>(u);
        d0[k] = (n & 2) ? u : t;
      }
      const bool k1 = (n & 1) != 0;
      if ((n & 4) == 0) ST1((v4u{u_(k1 ? d0[2] : d0[0]), tag, u_(k1 ? d0[3] : d0[1]), tag}),
                            WS_H4 + 262144 + (((q4 * 8 + 2 * w + ((n >> 1) & 1)) * 2 + (n >> 3)) * 32 + ht) * 128 + g * 32 + (n & 1) * 16);
      STAMP(12);
    }
  }
}

extern "C" void kernel_launch(void* const* d_in, const int* in_sizes, int n_in, void* d_out, int out_size, void* d_ws, size_t ws_size,
                              hipStream_t stream) {
  (void)hipFuncSetAttribute((const void*)fused_decode, hipFuncAttributeMaxDynamicSharedMemorySize, 163840);
  fused_decode<<<256, 512, 163840, stream>>>((const u16*)d_in[0], (const u16*)d_in[2], (const u16*)d_in[6], (const u16*)d_in[9],
                                             (unsigned char*)d_ws, (const u16*)d_in[1], (const u16*)d_in[3], (const u16*)d_in[7],
                                             (const u16*)d_in[8], (const u16*)d_in[4], (const u16*)d_in[5], (const u16*)d_in[10],
                                             (u16*)d_out);
}
```

```cpp
#include <hip/hip_runtime.h>
#include <hip/hip_bf16.h>

typedef unsigned u32;
typedef unsigned short u16;
typedef unsigned long long u64;
typedef u32 v4u __attribute__((ext_vector_type(4)));
typedef u32 v2u __attribute__((ext_vector_type(2)));
typedef short s16x4 __attribute__((ext_vector_type(4)));
typedef short bf16x8 __attribute__((ext_vector_type(8)));
typedef __bf16 bf16x2 __attribute__((ext_vector_type(2)));
typedef float f32x4 __attribute__((ext_vector_type(4)));
typedef float f32x16 __attribute__((ext_vector_type(16)));
#define GAS __attribute__((address_space(1)))
#define LAS __attribute__((address_space(3)))
#define DI __device__ __forceinline__
#define SB() __builtin_amdgcn_sched_barrier(0)

#define WIN0 32
#define WINA0 24
#define NA0 2
#define WINA 32
#define WINB0 8
#define NB0 0
#define WINB 24
#define THOLD 70
#define BGATE 0
#define WINU 32
#define WUV_AT 8
#define PDELAY 70
#define ABL 0
#define FDELAY 110
#define KSTART 560
#define XFIRST 0
#define HPOLL 6
#define QBDELAY 70
#define H1DELAY 0
#define WINUK 63
#define XPOL ""
#define NC0 0
#define OPTQ 1
#define GATEL 5
#define PAGES 1
#define QW 8
#define CODE_BYTES 23552

#define TYPEBIT 5
constexpr u32 WS_EPOCH = 0;
constexpr u32 WS_GATE = 4096;
constexpr u32 WS_HINT = 16384;
constexpr u32 WS_QABS = 65536;
constexpr u32 WS_ASTAT = 262144;
constexpr u32 WS_H3B = 524288;
constexpr u32 WS_BIN = 786432;
constexpr u32 WS_QB = 6291456 + 4096;
constexpr u32 WS_H4 = 1310720;
constexpr u32 WS_H1 = 1835008;
constexpr u32 WS_APART = 2097152;
constexpr u32 APART_BLK = 1048576 + 256;
constexpr u32 WS_BYTES = 6291456 + 4096 + 262144;
constexpr u32 WS_STAMP = 8388608;

constexpr u32 L_C = 0, L_K = 0, L_V = 65536, L_UK = 32768, L_WQ = 98304;
constexpr u32 L_Q = 131072;
constexpr u32 L_X = L_Q + 16384, L_QL = L_Q + 18432, L_QX = L_Q + 20480, L_QBL = L_Q + 22528, L_PB = L_Q + 24576;
constexpr u32 L_P = L_Q, L_SCR = L_Q + 8192, L_LSUM = L_Q + 28672, L_EXP = L_Q + 29696, L_GOT = L_Q + 29824;
constexpr u32 L_MAXX = L_Q + 31744, L_SYNC = L_MAXX + 512, L_PTR = L_SYNC + 256;
constexpr u32 L_W1 = 0, L_W2 = 32768, L_WGT = 65536, L_CK = 66048, L_BAD = 66560, L_B1 = 67584;
constexpr u32 L_BB = L_Q, L_B2 = L_Q + 8192;
enum { S_WQ = 0, S_UK, S_PUB, S_QBISS, S_H1, S_QBRDY, S_XRD, S_CISS, S_HINT, S_QGO, S_QISS, S_QST, S_LAND, S_MAX, S_BAD, S_P, S_PV, S_KRD, S_W1, S_W2, S_TB, S_TA, S_N };

constexpr float KSC = 0.08838834764831845f * 1.4426950408889634f;
constexpr int SPIN_CAP = 1 << 15;
constexpr u32 QTRIES = 64;

#define STAMP(i) do { } while (0)

template <u32 LDS> DI void dma1(const void* sb, u32 v) {
  u32 keep;
  asm volatile("s_mov_b32 %0, m0\n\ts_mov_b32 m0, %3\n\ts_nop 0\n\tglobal_load_lds_dwordx4 %1, %2" XPOL "\n\ts_mov_b32 m0, %0"
               : "=&s"(keep) : "v"(v), "s"(sb), "i"(LDS) : "memory");
}
DI void dma4s(const void* sb, u32 v0, u32 v1, u32 v2, u32 v3, u32 lds) {
  u32 keep;
  asm volatile("s_mov_b32 %0, m0\n\ts_mov_b32 m0, %6\n\ts_nop 0\n\tglobal_load_lds_dwordx4 %1, %5 sc1\n\t"
               "global_load_lds_dwordx4 %2, %5 offset:1024 sc1\n\tglobal_load_lds_dwordx4 %3, %5 offset:2048 sc1\n\t"
               "global_load_lds_dwordx4 %4, %5 offset:3072 sc1\n\ts_mov_b32 m0, %0"
               : "=&s"(keep) : "v"(v0), "v"(v1), "v"(v2), "v"(v3), "s"(sb), "s"(lds) : "memory");
}
template <int W, u32 LDS> DI void dma4(const void* sb, u32 v0, u32 v1, u32 v2, u32 v3) {
  u32 keep;
  asm volatile("s_mov_b32 %0, m0\n\ts_mov_b32 m0, %6\n\ts_waitcnt vmcnt(%7)\n\t"
               "global_load_lds_dwordx4 %1, %5 nt\n\tglobal_load_lds_dwordx4 %2, %5 offset:1024 nt\n\ts_waitcnt vmcnt(%7)\n\t"
               "global_load_lds_dwordx4 %3, %5 offset:2048 nt\n\tglobal_load_lds_dwordx4 %4, %5 offset:3072 nt\n\ts_mov_b32 m0, %0"
               : "=&s"(keep) : "v"(v0), "v"(v1), "v"(v2), "v"(v3), "s"(sb), "i"(LDS), "i"(W) : "memory");
}
template <int W> DI void dma4r(const void* sb, u32 v0, u32 v1, u32 v2, u32 v3, u32 lds) {
  u32 keep;
  asm volatile("s_mov_b32 %0, m0\n\ts_mov_b32 m0, %6\n\ts_waitcnt vmcnt(%7)\n\t"
               "global_load_lds_dwordx4 %1, %5 nt\n\tglobal_load_lds_dwordx4 %2, %5 offset:1024 nt\n\ts_waitcnt vmcnt(%7)\n\t"
               "global_load_lds_dwordx4 %3, %5 offset:2048 nt\n\tglobal_load_lds_dwordx4 %4, %5 offset:3072 nt\n\ts_mov_b32 m0, %0"
               : "=&s"(keep) : "v"(v0), "v"(v1), "v"(v2), "v"(v3), "s"(sb), "s"(lds), "i"(W) : "memory");
}
typedef const unsigned char* cptr;
DI cptr mkptr(u32 lo, u32 hi) { return (cptr)((u64)hi << 32 | lo); }
template <int I> struct IC { static constexpr int v = I; };
template <int B, int E, class F> DI void sfor(F&& f) { if constexpr (B < E) { f(IC<B>{}); sfor<B + 1, E>(f); } }
template <int N> DI void vmwait() { asm volatile("s_waitcnt vmcnt(%0)" :: "i"(N) : "memory"); }
DI u32 pk(float a, float b) { bf16x2 v = {(__bf16)a, (__bf16)b}; return __builtin_bit_cast(u32, v); }
DI float blo(u32 x) { return __builtin_bit_cast(float, x << 16); }
DI float bhi(u32 x) { return __builtin_bit_cast(float, x & 0xffff0000u); }
template <class T> DI LAS T* lp(u32 a) { return (LAS T*)(size_t)a; }
DI v4u lds128(u32 a) { return *lp<v4u>(a); }
DI void lds128w(u32 a, v4u v) { *lp<v4u>(a) = v; }
DI void lds64w(u32 a, u32 x, u32 y) { *lp<v2u>(a) = v2u{x, y}; }
DI v4u ldstr(u32 a0, u32 a1) {
  s16x4 lo = __builtin_amdgcn_ds_read_tr16_b64_v4i16(lp<s16x4>(a0));
  s16x4 hi = __builtin_amdgcn_ds_read_tr16_b64_v4i16(lp<s16x4>(a1));
  v2u l = __builtin_bit_cast(v2u, lo), h = __builtin_bit_cast(v2u, hi);
  return v4u{l.x, l.y, h.x, h.y};
}
DI f32x4 mfma16(v4u a, v4u b, f32x4 c) { return __builtin_amdgcn_mfma_f32_16x16x32_bf16(__builtin_bit_cast(bf16x8, a), __builtin_bit_cast(bf16x8, b), c, 0, 0, 0); }
DI f32x16 mfma32(v4u a, v4u b, f32x16 c) { return __builtin_amdgcn_mfma_f32_32x32x16_bf16(__builtin_bit_cast(bf16x8, a), __builtin_bit_cast(bf16x8, b), c, 0, 0, 0); }
template <int CTRL> DI u32 dppu(u32 x) { return (u32)__builtin_amdgcn_mov_dpp((int)x, CTRL, 0xf, 0xf, true); }
DI float f_(u32 x) { return __builtin_bit_cast(float, x); }
DI u32 u_(float x) { return __builtin_bit_cast(u32, x); }
template <int CTRL> DI float dpp(float x) { return f_(dppu<CTRL>(u_(x))); }
constexpr int X1 = 0xB1, X2 = 0x4E, X7 = 0x141, X8 = 0x128;
DI float sum32(float x) { auto t = __builtin_amdgcn_permlane32_swap(u_(x), u_(x), false, false); return f_(t[0]) + f_(t[1]); }
DI float max32(float x) { auto t = __builtin_amdgcn_permlane32_swap(u_(x), u_(x), false, false); return fmaxf(f_(t[0]), f_(t[1])); }
DI float sum16(float x) { auto t = __builtin_amdgcn_permlane16_swap(u_(x), u_(x), false, false); return f_(t[0]) + f_(t[1]); }
DI float max16(float x) { auto t = __builtin_amdgcn_permlane16_swap(u_(x), u_(x), false, false); return fmaxf(f_(t[0]), f_(t[1])); }
DI void sig(int word, int lane) {
  asm volatile("" ::: "memory");
  if (lane == 0) __hip_atomic_fetch_add(lp<u32>(L_SYNC + 4 * word), 1u, __ATOMIC_RELAXED, __HIP_MEMORY_SCOPE_WORKGROUP);
  asm volatile("" ::: "memory");
}
template <bool SLEEP> DI void waitw(int word, u32 n) {
#pragma unroll 1
  for (u32 spin = 0; spin < (1u << 20); ++spin) {
    if ((u32)__builtin_amdgcn_readfirstlane(__hip_atomic_load(lp<u32>(L_SYNC + 4 * word), __ATOMIC_RELAXED, __HIP_MEMORY_SCOPE_WORKGROUP)) >= n) break;
    if (SLEEP) __builtin_amdgcn_s_sleep(2);
  }
  asm volatile("" ::: "memory");
}
#define STUB(n) asm volatile("s_branch .Lsk" #n "\n.Lstub" #n ":\n\ts_setpc_b64 vcc\n.Lsk" #n ":")
#define TOUCH(n) asm volatile("s_call_b64 vcc, .Lstub" #n ::: "vcc")
#define BAR0(id) asm volatile("s_waitcnt lgkmcnt(0)\n\ts_barrier ; start " #id ::: "memory")
DI bool qbad(int lane) { return __any(*lp<u32>(L_EXP + (lane & 31) * 4) != *lp<u32>(L_GOT + (lane & 31) * 4)); }
DI void bar() { asm volatile("s_waitcnt lgkmcnt(0)\n\ts_barrier" ::: "memory"); }
#define LD1(off) __builtin_amdgcn_raw_buffer_load_b128(rs, (off), 0, 16)
#define ST1(v, off) __builtin_amdgcn_raw_buffer_store_b128((v), rs, (off), 0, 16)
#define TAGLOAD() __builtin_amdgcn_raw_buffer_load_b32(rs, WS_EPOCH, 0, 16)
DI v4u ldnt(const void* p, u32 off) { return __builtin_nontemporal_load((const GAS v4u*)((const GAS unsigned char*)p + off)); }
DI void stage_w(u32 base, const v4u (&park)[32], u32 g, u32 n) {
#pragma unroll
  for (int i = 0; i < 32; ++i) lds128w(base + g * 256 + i * 1024 + 16 * (n ^ ((g | (((i >> 1) & 1) << 2)) << 1)), park[i]);
}

__global__ void __launch_bounds__(512, 2)
fused_decode(const u16* __restrict__ xA, const u16* __restrict__ wqA, const u16* __restrict__ xB, const u16* __restrict__ wqB,
             unsigned char* ws_, const u16* __restrict__ ckv, const u16* __restrict__ wuk, const u16* __restrict__ kc,
             const u16* __restrict__ vc, const u16* __restrict__ wuv, const u16* __restrict__ woA, const u16* __restrict__ woB,
             u16* __restrict__ out) {
  typedef u32 s8u __attribute__((ext_vector_type(8)));
  typedef u32 s16u __attribute__((ext_vector_type(16)));
  const int tid = threadIdx.x, lane = tid & 63, wave = __builtin_amdgcn_readfirstlane(tid >> 6);
  s8u ka;
  s16u kb;
  if (wave == 0) asm volatile("s_load_dwordx8 %0, %2, 0x0\n\ts_load_dwordx16 %1, %2, 0x20" : "=&s"(ka), "=&s"(kb) : "s"(__builtin_amdgcn_kernarg_segment_ptr()));
  u64 pc0; asm volatile("s_getpc_b64 %0" : "=s"(pc0));
  const u32 gid = blockIdx.x, x = gid & 7, r = gid >> 3;
  const bool isA = (r & 4) == 0;
  const u32 l16 = (r & 3) | ((r >> 3) << 2);
  const u32 ht = 4 * x + (l16 >> 2), q4 = l16 & 3;
  const u32 g = lane >> 4, n = lane & 15, tq = n >> 2, tp = lane & 3;
  u32 warm = 0;
  u64 t0 = 0;
  unsigned char* ws;
  if (wave == 0) {
    const u32 vo = q4 * 1048576 + (((l16 >> 2) + (isA ? 0 : 1)) & 3) * 256 + x * 1024 + g * 8192;
    const u32 ce = vo + ((n ^ (g << 1)) * 16), cd = vo + ((n ^ ((g | 4) << 1)) * 16) + 63488;
    const u32 uo = q4 * 1048576 + ht * 256 + g * 8192;
    u32 uv[4];
#pragma unroll
    for (int i = 0; i < 4; ++i) uv[i] = uo + ((n ^ (g | (i << 2))) * 16) + (i & 1) * 63488 + (i >> 1) * 131072;
    asm volatile("s_waitcnt lgkmcnt(0)" : "+s"(ka), "+s"(kb));
    STAMP(12);
    asm volatile("s_memrealtime %0" : "=s"(t0));
    if (!isA) {
      cptr uk = mkptr(kb[4], kb[5]);
#pragma unroll 1
      for (u32 H = 0; H < 4; ++H) {
        dma4r<63>(uk + H * 262144, uv[0], uv[0] + 31744, uv[1], uv[1] + 31744, L_UK + H * 8192);
        dma4r<63>(uk + H * 262144, uv[2], uv[2] + 31744, uv[3], uv[3] + 31744, L_UK + H * 8192 + 4096);
      }
      if (lane < 2) *lp<u32>(L_PTR + 4 * lane) = lane ? kb[1] : kb[0];
      BAR0(6);
    }
    dma1<L_X>(isA ? mkptr(ka[0], ka[1]) : mkptr(ka[4], ka[5]), (g & 1) * 1024 + q4 * 256 + n * 16);
    cptr wq = isA ? mkptr(ka[2], ka[3]) : mkptr(ka[6], ka[7]);
#pragma unroll 1
    for (u32 G = 0; G < 8; ++G) dma4r<63>(wq + G * 131072, ce, ce + 31744, cd, cd + 31744, L_WQ + G * 4096);
    if (isA) {
      if (lane < 2) *lp<u32>(L_PTR + 4 * lane) = lane ? kb[1] : kb[0];
      BAR0(0);
    }
    asm volatile("s_waitcnt lgkmcnt(0)" : "+s"(t0));
    STAMP(15);
  } else if (wave == 1) {
    const u32 ln = (r & 3) * 64 + lane;
    warm = ((const GAS u32*)(pc0 & ~127ull))[(ln < CODE_BYTES / 128 ? ln : 0u) * 32];
    SB();
  } else if (wave == 2) {
    if (lane < S_N) *lp<u32>(L_SYNC + 4 * lane) = 0;
  } else if (wave == 3) {
    s16u kc3;
    if (PAGES) asm volatile("s_load_dwordx16 %0, %1, 0x20" : "=s"(kc3) : "s"(__builtin_amdgcn_kernarg_segment_ptr()));
    if (isA) TOUCH(1); else TOUCH(2);
    if (PAGES) {
      asm volatile("s_waitcnt lgkmcnt(0)" : "+s"(kc3));
      const size_t kvb = (((size_t)(l16 >> 3) * 8 + x) * 8192 + 6144 + (l16 & 7) * 256) * 256;
      cptr p0 = isA ? mkptr(kc3[2], kc3[3]) + (size_t)(x * 16 + l16) * 131072 : mkptr(kc3[6], kc3[7]) + kvb;
      cptr p1 = isA ? mkptr(kc3[10], kc3[11]) + q4 * 1048576 + ht * 256 : mkptr(kc3[8], kc3[9]) + kvb;
      cptr p2 = isA ? p1 + 1040384 : mkptr(kc3[12], kc3[13]) + ht * 131072 + q4 * 256;
      cptr p3 = isA ? p0 + 130944 : mkptr(kc3[14], kc3[15]) + ht * 131072 + q4 * 256;
      warm = __builtin_nontemporal_load((const GAS u32*)((lane & 2) ? ((lane & 1) ? p3 : p2) : ((lane & 1) ? p1 : p0)));
      SB();
    }
  }
  __amdgpu_buffer_rsrc_t rs;
#define SETWS(lo, hi) do { ws = (unsigned char*)mkptr(lo, hi); rs = __builtin_amdgcn_make_buffer_rsrc((void*)ws, 0, WS_BYTES, 0x00020000); } while (0)
#define START(id) do { BAR0(id); const v2u p_ = *lp<v2u>(L_PTR); SETWS(__builtin_amdgcn_readfirstlane(p_.x), __builtin_amdgcn_readfirstlane(p_.y)); } while (0)
  u32 tag;
#define SIG_AT(ISSUED, W, NEED, WORD) if constexpr ((ISSUED) - (W) >= (NEED) && (ISSUED) - 4 - (W) < (NEED)) { vmwait<W>(); sig(WORD, lane); }
  const u32 w = wave & 3;
  const u32 ab = (8 * g + tq) * 256 + 16 * ((4 * w + (tp >> 1)) ^ ((tq | ((g & 1) << 2)) << 1)) + 8 * (tp & 1);

#define PROJ(BEFORE_STORE, DST) do { \
    v4u xf[4], af[8]; \
    _Pragma("unroll") for (int s = 0; s < 4; ++s) xf[s] = lds128(L_X + (lane & 1) * 256 + g * 16 + s * 64); \
    _Pragma("unroll") for (int s = 0; s < 8; ++s) { const u32 a = L_WQ + (ab ^ (32 * (s & 1))) + (s >> 1) * 8192; af[s] = ldstr(a, a + 1024); } \
    SB(); \
    f32x4 c0 = {0.f, 0.f, 0.f, 0.f}, c1 = c0; \
    _Pragma("unroll") for (int s = 0; s < 4; ++s) { c0 = mfma16(af[2 * s], xf[s], c0); c1 = mfma16(af[2 * s + 1], xf[s], c1); } \
    const f32x4 c = (n & 4) ? c1 : c0; \
    BEFORE_STORE; \
    if (n < 8) ST1((v4u{u_((n & 2) ? c[2] : c[0]), tag, u_((n & 2) ? c[3] : c[1]), tag}), (DST) + (16 * w + 8 * (n >> 2) + 2 * g + ((n >> 1) & 1)) * 16); \
  } while (0)

  if (isA) {
    if (wave == 0) {
      STUB(1);
      SETWS(kb[0], kb[1]);
      __builtin_amdgcn_s_setprio(1);
      cptr src = mkptr(kb[2], kb[3]) + (size_t)(x * 16 + l16) * 131072;
      u32 cv[16];
#pragma unroll
      for (int i = 0; i < 16; ++i) cv[i] = (lane ^ (((i & 3) << 2) | (i >> 2))) * 16;
#pragma unroll
      for (int G = 0; G < NC0; ++G) dma4r<63>(src + G * 4096, cv[4 * G], cv[4 * G + 1], cv[4 * G + 2], cv[4 * G + 3], L_C + G * 4096);
      vmwait<4 * NC0>(); sig(S_WQ, lane);
      STAMP(6);
      waitw<false>(S_XRD, 6);
      STAMP(5);
#pragma unroll 1
      for (u32 H = 0; H < 8; ++H) {
        if (H == 6) sig(S_CISS, lane);
#pragma unroll
        for (int G = 0; G < 4; ++G) {
          if (G >= NC0 || H > 0) dma4r<WINA>(src + H * 16384 + G * 4096, cv[4 * G], cv[4 * G + 1], cv[4 * G + 2], cv[4 * G + 3], L_C + H * 16384 + G * 4096);
        }
      }
      sig(S_CISS, lane);
      STAMP(1);
      vmwait<0>();
      STAMP(2);
      bar();
      v4u park[32];
      const u32 vo = (128 * q4 + g) * 8192 + ht * 256 + n * 16;
#pragma unroll
      for (int i = 0; i < 32; ++i) park[i] = ldnt(mkptr(kb[10], kb[11]), vo + i * 32768);
      const u32 tv = TAGLOAD();
      SB();
      STAMP(3);
      for (u32 it = 1; it <= QTRIES; ++it) {
        bar(); bar();
        if (!qbad(lane) || it == QTRIES) break;
        bar(); bar();
      }
      bar(); bar();
      stage_w(L_W1, park, g, n);
      tag = __builtin_amdgcn_readfirstlane(tv) + 1;
      STAMP(4);
    } else if (wave < 4) {
      START(1);
      if (wave == 1) {
        asm volatile("" :: "v"(warm));
        TOUCH(11); TOUCH(12); TOUCH(13); TOUCH(14); TOUCH(15); TOUCH(16); TOUCH(17); TOUCH(18); TOUCH(19);
        if (lane < 6) { u32 t = __builtin_amdgcn_raw_buffer_load_b32(rs, lane < 2 ? lane * 1048576 + 262144 - 64 : WS_APART + (lane - 1) * APART_BLK - 128, 0, 16); asm volatile("" :: "v"(t)); }
      }
      if (wave != 2) sig(S_XRD, lane);
      asm volatile("" :: "v"(warm));
      tag = __builtin_amdgcn_readfirstlane(TAGLOAD()) + 1;
      v4u h0, h1;
      cptr qs = ws + WS_QABS + (x >> 2) * 32768;
#define Q_ISSUE() _Pragma("unroll 1") for (u32 G = 0; G < 8; ++G) { const u32 l0 = (lane ^ (G & 3)) * 16; \
          dma4s(qs + G * 4096, l0, l0 ^ 64, l0 ^ 128, l0 ^ 192, L_Q + G * 4096); }
#define HINT_LD(p, q) do { p = LD1(WS_HINT + lane * 128); q = LD1(WS_HINT + 8192 + lane * 128); asm volatile("" ::: "memory"); } while (0)
      if (wave == 2) {
        __builtin_amdgcn_s_setprio(2);
        waitw<true>(S_CISS, 1);
#pragma unroll 1
        for (int spin = 0; spin < 64; ++spin) {
          const u32 gt = __builtin_amdgcn_raw_buffer_load_b32(rs, WS_GATE + (lane & 7) * 128, 0, 16);
          asm volatile("" ::: "memory");
          const bool ok = __all(gt == tag);
          if (spin == 0) waitw<false>(S_CISS, 2);
          if (ok) break;
        }
        STAMP(3);
        if (OPTQ) {
          asm volatile("global_load_dwordx4 %0, %2, %4 sc1\n\tglobal_load_dwordx4 %1, %3, %4 sc1"
                       : "=&v"(h0), "=&v"(h1) : "v"(WS_HINT + lane * 128), "v"(WS_HINT + 8192 + lane * 128), "s"(ws) : "memory");
          Q_ISSUE()
          asm volatile("s_waitcnt vmcnt(32)" : "+v"(h0), "+v"(h1) :: "memory");
        } else HINT_LD(h0, h1);
        if (!OPTQ || !__all(h0.z == tag && h1.z == tag)) {
#pragma unroll 1
          for (int spin = 0; spin < 64; ++spin) {
            if (__all(h0.z == tag && h1.z == tag)) break;
            HINT_LD(h0, h1);
          }
          Q_ISSUE()
        }
        STAMP(1);
        vmwait<0>();
      }
      bar();
      for (u32 it = 1; it <= QTRIES; ++it) {
        bar();
        if (wave == 2) {
          u32 e0 = (x & 4 ? h0.y : h0.x) - tag, e1 = (x & 4 ? h1.y : h1.x) - tag;
          e0 += dppu<X1>(e0); e1 += dppu<X1>(e1); e0 += dppu<X2>(e0); e1 += dppu<X2>(e1);
          if (tp < 2) *lp<u32>(L_EXP + (lane >> 2) * 4 + tp * 64) = tp ? e1 : e0;
        }
        bar();
        if (!qbad(lane) || it == QTRIES) break;
        bar();
        if (wave == 2) { HINT_LD(h0, h1); Q_ISSUE() vmwait<0>(); }
        bar();
      }
      bar(); bar();
      if (wave == 1) *lp<u32>(L_BAD) = 0;
    } else {
      START(2);
      tag = __builtin_amdgcn_readfirstlane(TAGLOAD()) + 1;
      const u32 bs = x * 16 + l16, b = bs >> 6, hh = lane >> 5, hd = lane & 31;
      __builtin_amdgcn_s_setprio(2);
      waitw<false>(S_WQ, 1);
      PROJ((void)0, WS_H1 + ht * 8192 + q4 * 2048 + (n & 1) * 1024);
      sig(S_XRD, lane);
      STAMP(13);
      STUB(11);
      f32x16 s;
      v4u mm;
      for (u32 it = 1; it <= QTRIES; ++it) {
        u32 cs = 0;
        bar();
        STAMP(3);
#pragma unroll
        for (int i = 0; i < 16; ++i) s[i] = 0.f;
        {
          const u32 fr = ((hd & 3) << 2) | ((hd >> 2) & 3);
          const u32 qa = L_Q + hd * 1024 + 16 * (hh ^ fr), ca = qa - L_Q + L_C + w * 32768;
          v4u af[32], bf[32];
          constexpr int PD = 6;
#define S_RD(k) do { bf[k] = lds128((qa ^ (32 * ((k) & 7))) + 256 * ((k) >> 3)); af[k] = lds128((ca ^ (32 * ((k) & 7))) + 256 * ((k) >> 3)); } while (0)
#pragma unroll
          for (int k = 0; k < PD; ++k) S_RD(k);
          SB();
#pragma unroll
          for (int k = 0; k < 32; ++k) {
            if (k + PD < 32) S_RD(k + PD);
            s = mfma32(af[k], bf[k], s);
            cs = cs + bf[k].x + bf[k].y; cs = cs + bf[k].z + bf[k].w;
            if (k == 4) STUB(12);
            if (k == 26) { STUB(13); }
            if (k == 29) bar();
            SB();
          }
        }
        { auto t = __builtin_amdgcn_permlane32_swap(cs, cs, false, false); cs = t[0] + t[1]; }
        float m = s[0];
#pragma unroll
        for (int i = 1; i < 16; ++i) m = fmaxf(m, s[i]);
        m = max32(m);
        if (hh == 0) *lp<float>(L_MAXX + hd * 16 + w * 4) = m;
        if (w == 0 && hh == 0) *lp<u32>(L_GOT + hd * 4) = cs;
        STAMP(4);
        bar();
        mm = lds128(L_MAXX + hd * 16);
        if (!qbad(lane) || it == QTRIES) break;
        bar();
      }
      const float mk = fmaxf(fmaxf(f_(mm.x), f_(mm.y)), fmaxf(f_(mm.z), f_(mm.w))) * KSC;
      float ls = 0.f;
#pragma unroll
      for (int i = 0; i < 16; ++i) { s[i] = __builtin_amdgcn_exp2f(fmaf(s[i], KSC, -mk)); ls += s[i]; }
      lds128w(L_P + (2 * w) * 1024 + lane * 16, v4u{pk(s[0], s[1]), pk(s[2], s[3]), pk(s[4], s[5]), pk(s[6], s[7])});
      lds128w(L_P + (2 * w + 1) * 1024 + lane * 16, v4u{pk(s[8], s[9]), pk(s[10], s[11]), pk(s[12], s[13]), pk(s[14], s[15])});
      ls = sum32(ls);
      if (hh == 0) *lp<float>(L_LSUM + hd * 16 + w * 4) = ls;
      const u32 blk = (lane >> 4) & 1;
      u32 ta[2];
#pragma unroll
      for (int t = 0; t < 2; ++t)
        ta[t] = L_C + (4 * hh + 8 * t + tq) * 1024 + 256 * w + 16 * ((2 * blk + (tp >> 1)) ^ ((tq << 2) | (hh + 2 * t))) + 8 * (tp & 1);
      v4u cf[32];
      constexpr int PV_PD = 5;
#define PV_RD(kk) cf[kk] = ldstr((ta[0] ^ (64 * ((kk) >> 3))) + ((kk) & 7) * 16384, (ta[1] ^ (64 * ((kk) >> 3))) + ((kk) & 7) * 16384)
#pragma unroll
      for (int k = 0; k < PV_PD; ++k) PV_RD(k);
      SB();
      STUB(14);
      bar();
      STAMP(5);
      v4u pf[8];
#pragma unroll
      for (int k = 0; k < 8; ++k) pf[k] = lds128(L_P + k * 1024 + lane * 16);
      const v4u lv = lds128(L_LSUM + hd * 16);
      SB();
      const u32 scw = L_SCR + w * 4096 + hd * 128 + 8 * hh + 16 * (hd & 7);
      const u32 scr = L_SCR + w * 4096 + (lane >> 3) * 128 + 16 * ((lane & 7) ^ (lane >> 3));
      const u32 sto = WS_APART + (((lane >> 3) * 4 + w) * 128 + bs) * 256 + 16 * (lane & 7);
      f32x16 o[4];
      v4u ov[4];
      u32 cs = tag;
#define EPI_W(c, gg) do { \
        const u32 p0 = pk(o[c][4 * (gg)], o[c][4 * (gg) + 1]), p1 = pk(o[c][4 * (gg) + 2], o[c][4 * (gg) + 3]); \
        lds64w(scw ^ (64 * ((c) & 1) + 16 * (gg)), p0, p1); cs += p0 + p1; asm volatile("" : "+v"(cs)); } while (0)
#define EPI_R() _Pragma("unroll") for (int i = 0; i < 4; ++i) ov[i] = lds128(scr + i * 1024);
#define EPI_S(p, i) ST1(ov[i], sto + 128 * (p) + (i) * APART_BLK)
#pragma unroll
      for (int kk = 0; kk < 32; ++kk) {
        const int c = kk >> 3, k = kk & 7;
        if (kk + PV_PD < 32) PV_RD(kk + PV_PD);
        if (k == 0) {
          f32x16 z;
#pragma unroll
          for (int i = 0; i < 16; ++i) z[i] = 0.f;
          o[c] = mfma32(cf[kk], pf[k], z);
        } else o[c] = mfma32(cf[kk], pf[k], o[c]);
        if (c > 0 && k >= 1 && k <= 4) EPI_W(c > 0 ? c - 1 : 0, (k - 1) & 3);
        if (kk == 20) { EPI_R() }
        if (kk == 23) { EPI_S(0, 0); EPI_S(0, 1); }
        if (kk == 24) { EPI_S(0, 2); EPI_S(0, 3); }
        if (kk == 13) STUB(15);
        if (kk == 22) STUB(16);
        SB();
      }
      EPI_W(3, 0); EPI_W(3, 1); EPI_W(3, 2); EPI_W(3, 3); EPI_R()
      const float lsum = (f_(lv.x) + f_(lv.y)) + (f_(lv.z) + f_(lv.w));
      { auto t = __builtin_amdgcn_permlane32_swap(cs, cs, false, false); cs = t[0] + t[1] - tag + u_(mk) + u_(lsum); }
      EPI_S(1, 0); EPI_S(1, 1); EPI_S(1, 2); EPI_S(1, 3);
      if (hh == 0) ST1((v4u{u_(mk), u_(lsum), cs, tag}), WS_ASTAT + bs * 2048 + w * 512 + hd * 16);
      STAMP(7);
      bar();
    }
    const u32 v = wave;
    v4u a1[8], bq[4];
    STUB(17);
    const u64 tpv = __builtin_amdgcn_s_memrealtime();
    bar();
    if (wave >= 4) {
#pragma unroll
      for (int s = 0; s < 8; ++s) { const u32 a = L_W1 + (ab ^ (32 * (s >> 2))) + (s & 3) * 8192; a1[s] = ldstr(a, a + 1024); }
    }
    if (wave == 3) {
      while (__builtin_amdgcn_s_memrealtime() - tpv < PDELAY) { }
    }
    for (u32 it = 257; it < 320; ++it) {
      v4u st0, st1;
      if (wave == 3) {
        for (int spin = 0; spin < SPIN_CAP; ++spin) {
          st0 = LD1(WS_ASTAT + lane * 2048 + q4 * 512 + ht * 16);
          st1 = LD1(WS_ASTAT + 131072 + lane * 2048 + q4 * 512 + ht * 16);
          asm volatile("" ::: "memory");
          if (__all(st0.w == tag && st1.w == tag)) break;
        }
        STAMP(2);
      }
      bar();
      STAMP(8);
      v4u pd[4];
#pragma unroll
      for (int i = 0; i < 4; ++i) pd[i] = LD1(WS_APART + (ht >> 3) * APART_BLK + ((ht & 7) * 4 + q4) * 32768 + v * 4096 + i * 1024 + lane * 16);
      SB();
      if (wave == 3) {
#pragma unroll
        for (int i = 0; i < 2; ++i) {
          const v4u st = i ? st1 : st0;
          float M = f_(st.x);
          M = fmaxf(M, dpp<X1>(M)); M = fmaxf(M, dpp<X2>(M)); M = fmaxf(M, dpp<X7>(M)); M = fmaxf(M, dpp<X8>(M)); M = max16(M); M = max32(M);
          const float e = __builtin_amdgcn_exp2f(f_(st.x) - M);
          float S = e * f_(st.y);
          S += dpp<X1>(S); S += dpp<X2>(S); S += dpp<X7>(S); S += dpp<X8>(S); S = sum16(S); S = sum32(S);
          *lp<float>(L_WGT + i * 256 + lane * 4) = e / S;
          *lp<u32>(L_CK + i * 256 + lane * 4) = st.z - tag - st.x - st.y;
        }
      }
      STUB(18);
      bar();
      float acc[8], wt[4];
#pragma unroll
      for (int i = 0; i < 4; ++i) wt[i] = *lp<float>(L_WGT + (v * 16 + 4 * i + g) * 4);
      const u32 ck = *lp<u32>(L_CK + (v * 16 + 4 * (lane & 3) + g) * 4);
#pragma unroll
      for (int k = 0; k < 4; ++k) { acc[2 * k] = wt[0] * blo(pd[0][k]); acc[2 * k + 1] = wt[0] * bhi(pd[0][k]); }
#pragma unroll
      for (int i = 1; i < 4; ++i)
#pragma unroll
        for (int k = 0; k < 4; ++k) { acc[2 * k] = fmaf(wt[i], blo(pd[i][k]), acc[2 * k]); acc[2 * k + 1] = fmaf(wt[i], bhi(pd[i][k]), acc[2 * k + 1]); }
      STAMP(9);
      {
        float y[4];
#pragma unroll
        for (int k = 0; k < 4; ++k) { auto t = __builtin_amdgcn_permlane32_swap(u_(acc[k]), u_(acc[k + 4]), false, false); y[k] = f_(t[0]) + f_(t[1]); }
        float z[2];
#pragma unroll
        for (int k = 0; k < 2; ++k) { auto t = __builtin_amdgcn_permlane16_swap(u_(y[k]), u_(y[k + 2]), false, false); z[k] = f_(t[0]) + f_(t[1]); }
        const u32 hi = pk(z[0], z[1]), lo = pk(z[0] - blo(hi), z[1] - bhi(hi));
        *lp<u32>(L_B1 + v * 272 + (8 * n + 2 * g) * 2) = hi;
        *lp<u32>(L_B1 + (8 + v) * 272 + (8 * n + 2 * g) * 2) = lo;
        u32 c4[4], c2[2];
#pragma unroll
        for (int i = 0; i < 4; ++i) c4[i] = (pd[i].x + pd[i].y) + (pd[i].z + pd[i].w);
#pragma unroll
        for (int i = 0; i < 2; ++i) { const u32 p = c4[i] + dppu<X2>(c4[i]), q = c4[i + 2] + dppu<X2>(c4[i + 2]); c2[i] = (lane & 2) ? q : p; }
        const u32 p1 = c2[0] + dppu<X1>(c2[0]), q1 = c2[1] + dppu<X1>(c2[1]);
        u32 c1 = (lane & 1) ? q1 : p1;
        c1 += dppu<X8>(c1);
        c1 += dppu<0x124>(c1);
        if (c1 != ck) *lp<u32>(L_BAD) = it;
      }
      STUB(19);
      bar();
      const u32 bad = *lp<u32>(L_BAD);
#pragma unroll
      for (int s = 0; s < 4; ++s) bq[s] = lds128(L_B1 + n * 272 + g * 16 + s * 64);
      SB();
      if (__builtin_amdgcn_readfirstlane(bad) != it) break;
    }
    if (wave >= 4) {
      f32x4 d0 = {0.f, 0.f, 0.f, 0.f}, d1 = d0;
#pragma unroll
      for (int s = 0; s < 4; ++s) { d0 = mfma16(a1[s], bq[s], d0); d1 = mfma16(a1[4 + s], bq[s], d1); }
#pragma unroll
      for (int k = 0; k < 4; ++k) {
        const float t = d0[k] + dpp<X1>(d0[k]), u = d1[k] + dpp<X1>(d1[k]);
        float z = (n & 1) ? u : t;
        z += dpp<X2>(z); z += dpp<X8>(z); d0[k] = z;
      }
      if ((n & 10) == 0) ST1((v4u{pk(d0[0], d0[1]), tag, pk(d0[2], d0[3]), tag}), WS_H3B + ht * 4096 + q4 * 1024 + (n >> 2) * 512 + (8 * w + 4 * (n & 1) + g) * 16);
    }
    STAMP(10);
    if (wave == 0) {
      const u32 rho = x * 8 + (l16 >> 1), part = l16 & 1;
      u16* outp = out; asm volatile("" : "+s"(outp));
      { const u64 t0 = __builtin_amdgcn_s_memrealtime(); while (__builtin_amdgcn_s_memrealtime() - t0 < (part ? 0 : FDELAY)) { } }
      v4u e[4];
      for (int spin = 0; spin < SPIN_CAP; ++spin) {
        bool ok = true;
#pragma unroll
        for (int i = 0; i < 4; ++i) e[i] = LD1(WS_H4 + part * 262144 + rho * 4096 + i * 1024 + lane * 16);
        asm volatile("" ::: "memory");
#pragma unroll
        for (int i = 0; i < 4; ++i) ok = ok && e[i].y == tag && e[i].w == tag;
        if (__all(ok)) break;
      }
      float s0 = (f_(e[0].x) + f_(e[1].x)) + (f_(e[2].x) + f_(e[3].x)), s1 = (f_(e[0].z) + f_(e[1].z)) + (f_(e[2].z) + f_(e[3].z));
      s0 += dpp<X8>(s0); s1 += dpp<X8>(s1);
      s0 = sum32(sum16(s0)); s1 = sum32(sum16(s1));
      if (lane < 8) *(GAS u32*)(outp + (rho & 1) * 1024 + part * 512 + (rho >> 1) * 16 + 2 * lane) = pk(s0, s1);
      if (gid == 0 && lane == 0) __builtin_amdgcn_raw_buffer_store_b32(tag, rs, WS_EPOCH, 0, 16);
      STAMP(13);
    }
  } else {
    if (wave < 4) {
      if (wave == 0) {
        STUB(2);
        SETWS(kb[0], kb[1]);
        __builtin_amdgcn_s_setprio(1);
        vmwait<33>(); sig(S_UK, lane);
        STAMP(14);
        vmwait<0>(); sig(S_WQ, lane);
        STAMP(5);
        tag = __builtin_amdgcn_readfirstlane(TAGLOAD()) + 1;
        STUB(3);
        waitw<false>(S_QBISS, 1);
        while (__builtin_amdgcn_s_memrealtime() - t0 < KSTART) __builtin_amdgcn_s_sleep(1);
        STAMP(13);
        const size_t base = (((size_t)(l16 >> 3) * 8 + x) * 8192 + 6144 + (l16 & 7) * 256) * 256;
        cptr ks = mkptr(kb[6], kb[7]) + base, vs = mkptr(kb[8], kb[9]) + base;
        static_assert(WINB % 4 == 0 && NB0 <= 3 + WINB / 4, "block signals");
        {
          u32 kv[4];
#pragma unroll
          for (int i = 0; i < 4; ++i) kv[i] = g * 256 + ((n ^ (4 * i + g)) * 16);
#pragma unroll 1
          for (u32 G = 0; G < NB0; ++G) dma4r<WINB0>(ks + G * 4096, kv[0], kv[1], kv[2], kv[3], L_K + G * 4096);
#pragma unroll 1
          for (u32 G = NB0; G < 16; ++G) {
            dma4r<WINB>(ks + G * 4096, kv[0], kv[1], kv[2], kv[3], L_K + G * 4096);
            if (((G + 1 - WINB / 4) & 3) == 0 && G >= 3 + WINB / 4) { vmwait<WINB>(); sig(S_LAND, lane); }
          }
        }
        STAMP(1);
        {
          const u32 v0 = g * 256 + ((n ^ (g << 1)) * 16), v1 = g * 256 + ((n ^ ((4 + g) << 1)) * 16);
#pragma unroll 1
          for (u32 G = 0; G < 16; ++G) {
            if (ABL != 3) dma4r<WINB>(vs + G * 4096, v0, v1, v0, v1, L_V + G * 4096);
            if (((G + 1 - WINB / 4) & 3) == 0 && G + 16 >= 3 + WINB / 4) { vmwait<WINB>(); sig(S_LAND, lane); }
          }
        }
        STAMP(2);
        waitw<false>(S_KRD, 4);
        {
          const u32 vo = (ht * 128 + g) * 1024 + q4 * 256, w0 = vo + ((n ^ (g << 1)) * 16), w1 = vo + ((n ^ ((g | 4) << 1)) * 16) + 6144;
#pragma unroll 1
          for (u32 G = 0; G < 16; ++G) {
            cptr wp = (G < 8 ? mkptr(kb[14], kb[15]) : mkptr(kb[12], kb[13])) + (G & 7) * 16384;
            if (ABL != 3) dma4r<WINB>(wp, w0, w0 + 3072, w1, w1 + 3072, L_W1 + G * 4096);
            if (((G + 1 - WINB / 4) & 3) == 0) { vmwait<WINB>(); sig(S_LAND, lane); }
          }
        }
        STAMP(3);
        vmwait<32>(); sig(S_W1, lane);
        vmwait<0>(); sig(S_W2, lane);
        STAMP(4);
      } else if (wave == 1) {
        START(3);
        __builtin_amdgcn_s_setprio(2);
        asm volatile("" :: "v"(warm));
        TOUCH(3); TOUCH(5); TOUCH(6); TOUCH(7); TOUCH(8); TOUCH(9); TOUCH(10);
        if (lane < 6) { u32 t = __builtin_amdgcn_raw_buffer_load_b32(rs, lane < 2 ? lane * 1048576 + 262144 - 64 : WS_APART + (lane - 1) * APART_BLK - 128, 0, 16); asm volatile("" :: "v"(t)); }
        tag = __builtin_amdgcn_readfirstlane(TAGLOAD()) + 1;
        waitw<false>(S_PUB, 4);
        {
          const v4u pv = lds128(L_QX + (lane & 31) * 16);
          if (lane < 32) ST1(pv, WS_QABS + (lane >> 4) * 32768 + ht * 1024 + q4 * 256 + n * 16);
          u32 cs = (pv.x + pv.y) + (pv.z + pv.w);
          cs += dppu<X1>(cs); cs += dppu<X2>(cs); cs += dppu<X7>(cs); cs += dppu<X8>(cs);
          auto t = __builtin_amdgcn_permlane16_swap(cs, cs, false, false);
          if (lane == 0) ST1((v4u{t[0] + tag, t[1] + tag, tag, tag}), WS_HINT + (x * 16 + l16) * 128);
          if (l16 == GATEL && lane == 0) __builtin_amdgcn_raw_buffer_store_b32(tag, rs, WS_GATE + x * 128, 0, 16);
        }
        STAMP(2);
        v4u e[16];
        for (int spin = 0; spin < SPIN_CAP; ++spin) {
          bool ok = true;
#pragma unroll
          for (int i = 0; i < 16; ++i) e[i] = LD1(WS_QB + x * 32768 + (l16 >> 3) * 16384 + i * 1024 + lane * 16);
          SB();
          if (spin == 0) sig(S_QBISS, lane);
          asm volatile("" ::: "memory");
#pragma unroll
          for (int i = 0; i < 16; ++i) ok = ok && e[i].y == tag && e[i].w == tag;
          if (__all(ok)) break;
        }
#pragma unroll
        for (int i = 0; i < 4; ++i)
          *lp<u32>(L_QBL + i * 256 + lane * 4) = pk((f_(e[i].x) + f_(e[4 + i].x)) + (f_(e[8 + i].x) + f_(e[12 + i].x)), (f_(e[i].z) + f_(e[4 + i].z)) + (f_(e[8 + i].z) + f_(e[12 + i].z)));
        sig(S_QBRDY, lane);
        STAMP(3);
      } else {
        START(4);
        asm volatile("" :: "v"(warm));
        tag = __builtin_amdgcn_readfirstlane(TAGLOAD()) + 1;
      }
      STUB(10);
      waitw<true>(S_W2, 1);
      v4u a2[8];
#pragma unroll
      for (int s = 0; s < 8; ++s) { const u32 a = L_W2 + (ab ^ (32 * (s >> 2))) + (s & 3) * 8192; a2[s] = ldstr(a, a + 1024); }
      v4u e;
      for (int spin = 0; spin < SPIN_CAP; ++spin) {
        e = LD1(WS_H3B + ht * 4096 + w * 1024 + lane * 16);
        asm volatile("" ::: "memory");
        if (__all(e.y == tag && e.w == tag)) break;
      }
      lds64w(L_B2 + ((lane >> 5) * 4 + w) * 272 + (lane & 31) * 8, e.x, e.z);
      sig(S_TA, lane);
      waitw<false>(S_TA, 4);
      STAMP(11);
      v4u bq[4];
#pragma unroll
      for (int s = 0; s < 4; ++s) bq[s] = lds128(L_B2 + (n & 7) * 272 + g * 16 + s * 64);
      SB();
      f32x4 d0 = {0.f, 0.f, 0.f, 0.f}, d1 = d0;
#pragma unroll
      for (int s = 0; s < 4; ++s) { d0 = mfma16(a2[s], bq[s], d0); d1 = mfma16(a2[4 + s], bq[s], d1); }
      f32x4 d = (n & 8) ? d1 : d0;
#pragma unroll
      for (int k = 0; k < 4; ++k) { float t = d[k]; t += dpp<X1>(t); t += dpp<X2>(t); d[k] = t; }
      const bool k1 = (n & 1) != 0;
      if ((n & 2) == 0) ST1((v4u{u_(k1 ? d[2] : d[0]), tag, u_(k1 ? d[3] : d[1]), tag}),
                            WS_H4 + (((q4 * 8 + 2 * w + (n >> 3)) * 2 + ((n >> 2) & 1)) * 32 + ht) * 128 + g * 32 + (n & 1) * 16);
      STAMP(12);
    } else {
      START(5);
      tag = __builtin_amdgcn_readfirstlane(TAGLOAD()) + 1;
      __builtin_amdgcn_s_setprio(2);
      {
        waitw<false>(S_UK, 1);
        v4u uk[8];
#pragma unroll
        for (int t = 0; t < 2; ++t)
#pragma unroll
          for (int s = 0; s < 4; ++s) uk[t * 4 + s] = lds128(L_UK + (32 * w + 8 * (n >> 2) + 4 * t + (n & 3)) * 256 + 16 * ((4 * s + g) ^ n));
        v4u e0, e1;
        STAMP(10);
        waitw<false>(S_WQ, 1);
#define H1_LD() do { e0 = LD1(WS_H1 + ht * 8192 + g * 2048 + w * 256 + n * 16); e1 = LD1(WS_H1 + ht * 8192 + g * 2048 + 1024 + w * 256 + n * 16); \
                     asm volatile("" ::: "memory"); } while (0)
        PROJ(H1_LD(), WS_QB + x * 32768 + (n & 1) * 16384 + (q4 * 4 + (((l16 >> 2) + 1) & 3)) * 1024);
        STAMP(11);
        for (int spin = 0; spin < SPIN_CAP; ++spin) {
          if (__all(e0.y == tag && e0.w == tag && e1.y == tag && e1.w == tag)) break;
          H1_LD();
        }
        STAMP(14);
        {
          auto p = __builtin_amdgcn_permlane32_swap(e0.x, e1.x, false, false), q = __builtin_amdgcn_permlane32_swap(e0.z, e1.z, false, false);
          const float a = sum16(f_(p[0]) + f_(p[1])), c = sum16(f_(q[0]) + f_(q[1]));
          if ((g & 1) == 0) *lp<u32>(L_QL + (g >> 1) * 256 + w * 64 + n * 4) = pk(a, c);
        }
        sig(S_H1, lane);
        waitw<false>(S_H1, 4);
        v4u qb[4];
#pragma unroll
        for (int s = 0; s < 4; ++s) qb[s] = lds128(L_QL + (lane & 1) * 256 + g * 16 + s * 64);
        SB();
        f32x4 u0 = {0.f, 0.f, 0.f, 0.f}, u1 = u0;
#pragma unroll
        for (int s = 0; s < 4; ++s) { u0 = mfma16(uk[s], qb[s], u0); u1 = mfma16(uk[4 + s], qb[s], u1); }
        const v4u pv = {pk(u0[0], u0[1]), pk(u0[2], u0[3]), pk(u1[0], u1[1]), pk(u1[2], u1[3])};
        if (n < 2) lds128w(L_QX + n * 256 + w * 64 + g * 16, pv);
        sig(S_PUB, lane);
        STAMP(15);
      }
      STUB(5);
      waitw<true>(S_LAND, w + 1);
      f32x4 sc[4];
      {
        v4u kf[16], qf[4];
        const u32 ka = L_K + (64 * w + n) * 256 + 16 * (g ^ n);
#pragma unroll
        for (int i = 0; i < 16; ++i) kf[i] = lds128((ka ^ (64 * (i & 3))) + (i >> 2) * 4096);
        sig(S_KRD, lane);
        SB();
        waitw<false>(S_QBRDY, 1);
        STAMP(3);
#pragma unroll
        for (int s = 0; s < 4; ++s) qf[s] = lds128(L_QBL + (lane & 3) * 256 + g * 16 + s * 64);
        SB();
#pragma unroll
        for (int t = 0; t < 4; ++t) {
          sc[t] = f32x4{0.f, 0.f, 0.f, 0.f};
#pragma unroll
          for (int s = 0; s < 4; ++s) sc[t] = mfma16(kf[4 * t + s], qf[s], sc[t]);
        }
      }
      float m = sc[0][0];
#pragma unroll
      for (int i = 1; i < 16; ++i) m = fmaxf(m, sc[i >> 2][i & 3]);
      m = max32(max16(m));
      if (lane < 4) *lp<float>(L_MAXX + lane * 16 + w * 4) = m;
      sig(S_MAX, lane);
      STUB(6);
      waitw<false>(S_MAX, 4);
      const v4u mm = lds128(L_MAXX + (lane & 3) * 16);
      const float mk = fmaxf(fmaxf(f_(mm.x), f_(mm.y)), fmaxf(f_(mm.z), f_(mm.w))) * KSC;
      float ls = 0.f;
#pragma unroll
      for (int i = 0; i < 16; ++i) { const float p = __builtin_amdgcn_exp2f(fmaf(sc[i >> 2][i & 3], KSC, -mk)); sc[i >> 2][i & 3] = p; ls += p; }
      ls = sum32(sum16(ls));
      if (n < 4) {
#pragma unroll
        for (int u = 0; u < 2; ++u)
          lds128w(L_PB + (2 * w + u) * 256 + g * 64 + n * 16,
                  v4u{pk(sc[2 * u][0], sc[2 * u][1]), pk(sc[2 * u][2], sc[2 * u][3]), pk(sc[2 * u + 1][0], sc[2 * u + 1][1]), pk(sc[2 * u + 1][2], sc[2 * u + 1][3])});
      }
      if (lane < 4) *lp<float>(L_LSUM + lane * 16 + w * 4) = ls;
      sig(S_P, lane);
      STUB(7);
      waitw<false>(S_P, 4);
      STAMP(5);
      v4u pf[8];
#pragma unroll
      for (int k = 0; k < 8; ++k) pf[k] = lds128(L_PB + k * 256 + g * 64 + (lane & 3) * 16);
      if (w == 0) {
        const v4u lv = lds128(L_LSUM + (lane & 3) * 16);
        const float lsum = (f_(lv.x) + f_(lv.y)) + (f_(lv.z) + f_(lv.w));
        if (lane < 4) ST1((v4u{u_(mk), tag, u_(lsum), tag}), WS_BIN + (4 * x + lane) * 9216 + 8192 + l16 * 16);
      }
      f32x4 o0 = {0.f, 0.f, 0.f, 0.f}, o1 = o0;
      const u32 va = L_V + (4 * g + tq) * 256 + 16 * ((4 * w + (tp >> 1)) ^ ((4 * (g & 1) + tq) << 1)) + 8 * (tp & 1);
#pragma unroll
      for (int k2 = 0; k2 < 4; ++k2) {
        waitw<false>(S_LAND, k2 + 5);
        v4u vf[4];
#pragma unroll
        for (int i = 0; i < 4; ++i) { const u32 a = (va ^ (32 * (i & 1))) + (2 * k2 + (i >> 1)) * 8192; vf[i] = ldstr(a, a + 4096); }
        SB();
        o0 = mfma16(vf[0], pf[2 * k2], o0); o1 = mfma16(vf[1], pf[2 * k2], o1);
        o0 = mfma16(vf[2], pf[2 * k2 + 1], o0); o1 = mfma16(vf[3], pf[2 * k2 + 1], o1);
      }
      STAMP(6);
      {
        const f32x4 o = (n & 4) ? o1 : o0;
        if (n < 8) ST1((v4u{pk(o[0], o[1]), tag, pk(o[2], o[3]), tag}), WS_BIN + (4 * x + (n & 3)) * 9216 + l16 * 512 + (4 * (2 * w + (n >> 2)) + g) * 16);
      }
      STAMP(7);
      STUB(8);
      waitw<false>(S_W1, 1);
      STAMP(8);
      v4u a1[8];
#pragma unroll
      for (int s = 0; s < 8; ++s) { const u32 a = L_W1 + (ab ^ (32 * (s >> 2))) + (s & 3) * 8192; a1[s] = ldstr(a, a + 1024); }
      v4u e0, e1, st;
      for (int spin = 0; spin < SPIN_CAP; ++spin) {
        e0 = LD1(WS_BIN + ht * 9216 + w * 2048 + lane * 16);
        e1 = LD1(WS_BIN + ht * 9216 + w * 2048 + 1024 + lane * 16);
        st = LD1(WS_BIN + ht * 9216 + 8192 + n * 16);
        asm volatile("" ::: "memory");
        if (__all(e0.y == tag && e0.w == tag && e1.y == tag && e1.w == tag && st.y == tag && st.w == tag)) break;
      }
      STAMP(9);
      lds64w(L_BB + (4 * w + (lane >> 5)) * 272 + (lane & 31) * 8, e0.x, e0.z);
      lds64w(L_BB + (4 * w + 2 + (lane >> 5)) * 272 + (lane & 31) * 8, e1.x, e1.z);
      sig(S_TB, lane);
      float M = f_(st.x);
      M = fmaxf(M, dpp<X1>(M)); M = fmaxf(M, dpp<X2>(M)); M = fmaxf(M, dpp<X7>(M));
      const float ee = __builtin_amdgcn_exp2f(f_(st.x) - M);
      float S = ee * f_(st.z);
      S += dpp<X1>(S); S += dpp<X2>(S); S += dpp<X7>(S);
      const float wt = ee / S;
      STUB(9);
      waitw<false>(S_TB, 4);
      v4u bq[4];
#pragma unroll
      for (int s = 0; s < 4; ++s) bq[s] = lds128(L_BB + n * 272 + g * 16 + s * 64);
      SB();
      f32x4 d0 = {0.f, 0.f, 0.f, 0.f}, d1 = d0;
#pragma unroll
      for (int s = 0; s < 4; ++s) { d0 = mfma16(a1[s], bq[s], d0); d1 = mfma16(a1[4 + s], bq[s], d1); }
#pragma unroll
      for (int k = 0; k < 4; ++k) {
        float t = d0[k] * wt, u = d1[k] * wt;
        t += dpp<X1>(t); u += dpp<X1>(u); t += dpp<X2>(t); u += dpp<X2>(u); t += dpp<X7>(t); u += dpp<X7>(u);
        d0[k] = (n & 2) ? u : t;
      }
      const bool k1 = (n & 1) != 0;
      if ((n & 4) == 0) ST1((v4u{u_(k1 ? d0[2] : d0[0]), tag, u_(k1 ? d0[3] : d0[1]), tag}),
                            WS_H4 + 262144 + (((q4 * 8 + 2 * w + ((n >> 1) & 1)) * 2 + (n >> 3)) * 32 + ht) * 128 + g * 32 + (n & 1) * 16);
      STAMP(12);
    }
  }
}

extern "C" void kernel_launch(void* const* d_in, const int* in_sizes, int n_in, void* d_out, int out_size, void* d_ws, size_t ws_size,
                              hipStream_t stream) {
  (void)hipFuncSetAttribute((const void*)fused_decode, hipFuncAttributeMaxDynamicSharedMemorySize, 163840);
  fused_decode<<<256, 512, 163840, stream>>>((const u16*)d_in[0], (const u16*)d_in[2], (const u16*)d_in[6], (const u16*)d_in[9],
                                             (unsigned char*)d_ws, (const u16*)d_in[1], (const u16*)d_in[3], (const u16*)d_in[7],
                                             (const u16*)d_in[8], (const u16*)d_in[4], (const u16*)d_in[5], (const u16*)d_in[10],
                                             (u16*)d_out);
}
```

```cpp
#include <hip/hip_runtime.h>
#include <hip/hip_bf16.h>

typedef unsigned u32;
typedef unsigned short u16;
typedef unsigned long long u64;
typedef u32 v4u __attribute__((ext_vector_type(4)));
typedef u32 v2u __attribute__((ext_vector_type(2)));
typedef short s16x4 __attribute__((ext_vector_type(4)));
typedef short bf16x8 __attribute__((ext_vector_type(8)));
typedef __bf16 bf16x2 __attribute__((ext_vector_type(2)));
typedef float f32x4 __attribute__((ext_vector_type(4)));
typedef float f32x16 __attribute__((ext_vector_type(16)));
#define GAS __attribute__((address_space(1)))
#define LAS __attribute__((address_space(3)))
#define DI __device__ __forceinline__
#define SB() __builtin_amdgcn_sched_barrier(0)

#define WIN0 32
#define WINA0 24
#define NA0 2
#define WINA 32
#define WINB0 8
#define NB0 0
#define WINB 24
#define THOLD 50
#define BGATE 0
#define WINU 32
#define WUV_AT 8
#define PDELAY 70
#define ABL 0
#define FDELAY 160
#define KSTART 450
#define XFIRST 0
#define HPOLL 6
#define QBDELAY 70
#define H1DELAY 0
#define WINUK 63
#define XPOL ""
#define NC0 0
#define OPTQ 1
#define GATEL 5
#define PAGES 1
#define QBSTART 350
#define KEYMIX 1
#define WQMIX 0
#define KVMIX 1
#define QW 8
#define CODE_BYTES 23296

#define TYPEBIT 5
constexpr u32 WS_EPOCH = 0;
constexpr u32 WS_GATE = 4096;
constexpr u32 WS_HINT = 16384;
constexpr u32 WS_QABS = 65536;
constexpr u32 WS_ASTAT = 262144;
constexpr u32 WS_H3B = 524288;
constexpr u32 WS_BIN = 786432;
constexpr u32 WS_QB = 6291456 + 4096;
constexpr u32 WS_H4 = 1310720;
constexpr u32 WS_H1 = 1835008;
constexpr u32 WS_APART = 2097152;
constexpr u32 APART_BLK = 1048576 + 256;
constexpr u32 WS_BYTES = 6291456 + 4096 + 262144;
constexpr u32 WS_STAMP = 8388608;

constexpr u32 L_C = 0, L_K = 0, L_V = 65536, L_UK = 32768, L_WQ = 98304;
constexpr u32 L_Q = 131072;
constexpr u32 L_X = L_Q + 16384, L_QL = L_Q + 18432, L_QX = L_Q + 20480, L_QBL = L_Q + 22528, L_PB = L_Q + 24576;
constexpr u32 L_P = L_Q, L_SCR = L_Q + 8192, L_LSUM = L_Q + 28672, L_EXP = L_Q + 29696, L_GOT = L_Q + 29824;
constexpr u32 L_MAXX = L_Q + 31744, L_SYNC = L_MAXX + 512, L_PTR = L_SYNC + 256;
constexpr u32 L_W1 = 0, L_W2 = 32768, L_WGT = 65536, L_CK = 66048, L_BAD = 66560, L_B1 = 67584;
constexpr u32 L_BB = L_Q, L_B2 = L_Q + 8192;
enum { S_WQ = 0, S_UK, S_PUB, S_QBISS, S_H1, S_QBRDY, S_XRD, S_CISS, S_HINT, S_QGO, S_QISS, S_QST, S_LAND, S_MAX, S_BAD, S_P, S_PV, S_KRD, S_W1, S_W2, S_TB, S_TA, S_N };

constexpr float KSC = 0.08838834764831845f * 1.4426950408889634f;
constexpr int SPIN_CAP = 1 << 15;
constexpr u32 QTRIES = 64;

#define STAMP(i) do { } while (0)

template <u32 LDS> DI void dma1(const void* sb, u32 v) {
  u32 keep;
  asm volatile("s_mov_b32 %0, m0\n\ts_mov_b32 m0, %3\n\ts_nop 0\n\tglobal_load_lds_dwordx4 %1, %2" XPOL "\n\ts_mov_b32 m0, %0"
               : "=&s"(keep) : "v"(v), "s"(sb), "i"(LDS) : "memory");
}
DI void dma4s(const void* sb, u32 v0, u32 v1, u32 v2, u32 v3, u32 lds) {
  u32 keep;
  asm volatile("s_mov_b32 %0, m0\n\ts_mov_b32 m0, %6\n\ts_nop 0\n\tglobal_load_lds_dwordx4 %1, %5 sc1\n\t"
               "global_load_lds_dwordx4 %2, %5 offset:1024 sc1\n\tglobal_load_lds_dwordx4 %3, %5 offset:2048 sc1\n\t"
               "global_load_lds_dwordx4 %4, %5 offset:3072 sc1\n\ts_mov_b32 m0, %0"
               : "=&s"(keep) : "v"(v0), "v"(v1), "v"(v2), "v"(v3), "s"(sb), "s"(lds) : "memory");
}
template <int W, u32 LDS> DI void dma4(const void* sb, u32 v0, u32 v1, u32 v2, u32 v3) {
  u32 keep;
  asm volatile("s_mov_b32 %0, m0\n\ts_mov_b32 m0, %6\n\ts_waitcnt vmcnt(%7)\n\t"
               "global_load_lds_dwordx4 %1, %5 nt\n\tglobal_load_lds_dwordx4 %2, %5 offset:1024 nt\n\ts_waitcnt vmcnt(%7)\n\t"
               "global_load_lds_dwordx4 %3, %5 offset:2048 nt\n\tglobal_load_lds_dwordx4 %4, %5 offset:3072 nt\n\ts_mov_b32 m0, %0"
               : "=&s"(keep) : "v"(v0), "v"(v1), "v"(v2), "v"(v3), "s"(sb), "i"(LDS), "i"(W) : "memory");
}
template <int W> DI void dma4r(const void* sb, u32 v0, u32 v1, u32 v2, u32 v3, u32 lds) {
  u32 keep;
  asm volatile("s_mov_b32 %0, m0\n\ts_mov_b32 m0, %6\n\ts_waitcnt vmcnt(%7)\n\t"
               "global_load_lds_dwordx4 %1, %5 nt\n\tglobal_load_lds_dwordx4 %2, %5 offset:1024 nt\n\ts_waitcnt vmcnt(%7)\n\t"
               "global_load_lds_dwordx4 %3, %5 offset:2048 nt\n\tglobal_load_lds_dwordx4 %4, %5 offset:3072 nt\n\ts_mov_b32 m0, %0"
               : "=&s"(keep) : "v"(v0), "v"(v1), "v"(v2), "v"(v3), "s"(sb), "s"(lds), "i"(W) : "memory");
}
typedef const unsigned char* cptr;
DI cptr mkptr(u32 lo, u32 hi) { return (cptr)((u64)hi << 32 | lo); }
template <int I> struct IC { static constexpr int v = I; };
template <int B, int E, class F> DI void sfor(F&& f) { if constexpr (B < E) { f(IC<B>{}); sfor<B + 1, E>(f); } }
template <int N> DI void vmwait() { asm volatile("s_waitcnt vmcnt(%0)" :: "i"(N) : "memory"); }
DI u32 pk(float a, float b) { bf16x2 v = {(__bf16)a, (__bf16)b}; return __builtin_bit_cast(u32, v); }
DI float blo(u32 x) { return __builtin_bit_cast(float, x << 16); }
DI float bhi(u32 x) { return __builtin_bit_cast(float, x & 0xffff0000u); }
template <class T> DI LAS T* lp(u32 a) { return (LAS T*)(size_t)a; }
DI v4u lds128(u32 a) { return *lp<v4u>(a); }
DI void lds128w(u32 a, v4u v) { *lp<v4u>(a) = v; }
DI void lds64w(u32 a, u32 x, u32 y) { *lp<v2u>(a) = v2u{x, y}; }
DI v4u ldstr(u32 a0, u32 a1) {
  s16x4 lo = __builtin_amdgcn_ds_read_tr16_b64_v4i16(lp<s16x4>(a0));
  s16x4 hi = __builtin_amdgcn_ds_read_tr16_b64_v4i16(lp<s16x4>(a1));
  v2u l = __builtin_bit_cast(v2u, lo), h = __builtin_bit_cast(v2u, hi);
  return v4u{l.x, l.y, h.x, h.y};
}
DI f32x4 mfma16(v4u a, v4u b, f32x4 c) { return __builtin_amdgcn_mfma_f32_16x16x32_bf16(__builtin_bit_cast(bf16x8, a), __builtin_bit_cast(bf16x8, b), c, 0, 0, 0); }
DI f32x16 mfma32(v4u a, v4u b, f32x16 c) { return __builtin_amdgcn_mfma_f32_32x32x16_bf16(__builtin_bit_cast(bf16x8, a), __builtin_bit_cast(bf16x8, b), c, 0, 0, 0); }
template <int CTRL> DI u32 dppu(u32 x) { return (u32)__builtin_amdgcn_mov_dpp((int)x, CTRL, 0xf, 0xf, true); }
DI float f_(u32 x) { return __builtin_bit_cast(float, x); }
DI u32 u_(float x) { return __builtin_bit_cast(u32, x); }
template <int CTRL> DI float dpp(float x) { return f_(dppu<CTRL>(u_(x))); }
constexpr int X1 = 0xB1, X2 = 0x4E, X7 = 0x141, X8 = 0x128;
DI float sum32(float x) { auto t = __builtin_amdgcn_permlane32_swap(u_(x), u_(x), false, false); return f_(t[0]) + f_(t[1]); }
DI float max32(float x) { auto t = __builtin_amdgcn_permlane32_swap(u_(x), u_(x), false, false); return fmaxf(f_(t[0]), f_(t[1])); }
DI float sum16(float x) { auto t = __builtin_amdgcn_permlane16_swap(u_(x), u_(x), false, false); return f_(t[0]) + f_(t[1]); }
DI float max16(float x) { auto t = __builtin_amdgcn_permlane16_swap(u_(x), u_(x), false, false); return fmaxf(f_(t[0]), f_(t[1])); }
DI void sig(int word, int) {
  asm volatile("s_mov_b64 exec, 1\n\tds_add_u32 %0, %1\n\ts_mov_b64 exec, -1" :: "v"(L_SYNC + 4 * word), "v"(1u) : "memory");
}
template <bool SLEEP> DI void waitw(int word, u32 n) {
  u32 v, t, c = 1u << 20;
  asm volatile("1:\n\tds_read_b32 %0, %3\n\ts_waitcnt lgkmcnt(0)\n\tv_readfirstlane_b32 %1, %0\n\ts_cmp_ge_u32 %1, %4\n\ts_cbranch_scc1 2f\n\t"
               "s_sleep %5\n\ts_sub_u32 %2, %2, 1\n\ts_cbranch_scc0 1b\n2:"
               : "=&v"(v), "=&s"(t), "+s"(c) : "v"(L_SYNC + 4 * word), "s"(n), "i"(SLEEP ? 2 : 0) : "memory", "scc");
}
#define STUB(n) asm volatile("s_branch .Lsk" #n "\n.Lstub" #n ":\n\ts_setpc_b64 vcc\n.Lsk" #n ":")
#define TOUCH(n) asm volatile("s_call_b64 vcc, .Lstub" #n ::: "vcc")
#define BAR0(id) asm volatile("s_waitcnt lgkmcnt(0)\n\ts_barrier ; start " #id ::: "memory")
DI bool qbad(int lane) { return __any(*lp<u32>(L_EXP + (lane & 31) * 4) != *lp<u32>(L_GOT + (lane & 31) * 4)); }
DI void bar() { asm volatile("s_waitcnt lgkmcnt(0)\n\ts_barrier" ::: "memory"); }
#define LD1(off) __builtin_amdgcn_raw_buffer_load_b128(rs, (off), 0, 16)
#define ST1(v, off) __builtin_amdgcn_raw_buffer_store_b128((v), rs, (off), 0, 16)
#define TAGLOAD() __builtin_amdgcn_raw_buffer_load_b32(rs, WS_EPOCH, 0, 16)
DI v4u ldnt(const void* p, u32 off) { return __builtin_nontemporal_load((const GAS v4u*)((const GAS unsigned char*)p + off)); }
DI void stage_w(u32 base, const v4u (&park)[32], u32 g, u32 n) {
#pragma unroll
  for (int i = 0; i < 32; ++i) lds128w(base + g * 256 + i * 1024 + 16 * (n ^ ((g | (((i >> 1) & 1) << 2)) << 1)), park[i]);
}

__global__ void __launch_bounds__(512, 2)
fused_decode(const u16* __restrict__ xA, const u16* __restrict__ wqA, const u16* __restrict__ xB, const u16* __restrict__ wqB,
             unsigned char* ws_, const u16* __restrict__ ckv, const u16* __restrict__ wuk, const u16* __restrict__ kc,
             const u16* __restrict__ vc, const u16* __restrict__ wuv, const u16* __restrict__ woA, const u16* __restrict__ woB,
             u16* __restrict__ out) {
  typedef u32 s8u __attribute__((ext_vector_type(8)));
  typedef u32 s16u __attribute__((ext_vector_type(16)));
  const int tid = threadIdx.x, lane = tid & 63, wave = __builtin_amdgcn_readfirstlane(tid >> 6);
  s8u ka;
  s16u kb;
  if (wave == 0) asm volatile("s_load_dwordx8 %0, %2, 0x0\n\ts_load_dwordx16 %1, %2, 0x20" : "=&s"(ka), "=&s"(kb) : "s"(__builtin_amdgcn_kernarg_segment_ptr()));
  u64 pc0; asm volatile("s_getpc_b64 %0" : "=s"(pc0));
  const u32 gid = blockIdx.x, x = gid & 7, r = gid >> 3;
  const bool isA = (r & 4) == 0;
  const u32 l16 = (r & 3) | ((r >> 3) << 2);
  const u32 ht = 4 * x + (l16 >> 2), q4 = l16 & 3;
  const u32 g = lane >> 4, n = lane & 15, tq = n >> 2, tp = lane & 3;
  u32 warm = 0, ce = 0, cd = 0;
  u64 t0 = 0;
  unsigned char* ws;
  if (wave == 0) {
    const u32 vo = q4 * 1048576 + (((l16 >> 2) + 1) & 3) * 256 + x * 1024 + g * 8192;
    ce = vo + ((n ^ (g << 1)) * 16); cd = vo + ((n ^ ((g | 4) << 1)) * 16) + 63488;
    asm volatile("s_waitcnt lgkmcnt(0)" : "+s"(ka), "+s"(kb));
    STAMP(12);
    asm volatile("s_memrealtime %0" : "=s"(t0));
    if (THOLD > 0 && isA) {
      asm volatile("s_waitcnt lgkmcnt(0)" : "+s"(t0));
      while (__builtin_amdgcn_s_memrealtime() - t0 < THOLD) { }
    }
    dma1<L_X>(isA ? mkptr(ka[4], ka[5]) : mkptr(ka[0], ka[1]), (g & 1) * 1024 + q4 * 256 + n * 16);
    cptr wq = isA ? mkptr(ka[6], ka[7]) : mkptr(ka[2], ka[3]);
#pragma unroll 1
    for (u32 G = 0; G < (WQMIX && isA ? 0u : 8u); ++G) dma4r<63>(wq + G * 131072, ce, ce + 31744, cd, cd + 31744, L_WQ + G * 4096);
    if (lane < 2) *lp<u32>(L_PTR + 4 * lane) = lane ? kb[1] : kb[0];
    BAR0(0);
    asm volatile("s_waitcnt lgkmcnt(0)" : "+s"(t0));
    STAMP(15);
  } else if (wave == 1) {
    const u32 ln = (r & 3) * 64 + lane;
    warm = ((const GAS u32*)(pc0 & ~127ull))[(ln < CODE_BYTES / 128 ? ln : 0u) * 32];
    SB();
  } else if (wave == 2) {
    if (lane < S_N) *lp<u32>(L_SYNC + 4 * lane) = 0;
  } else if (wave == 3) {
    s16u kc3;
    if (PAGES) asm volatile("s_load_dwordx16 %0, %1, 0x20" : "=s"(kc3) : "s"(__builtin_amdgcn_kernarg_segment_ptr()));
    if (isA) TOUCH(1); else TOUCH(2);
    if (PAGES) {
      asm volatile("s_waitcnt lgkmcnt(0)" : "+s"(kc3));
      const size_t kvb = (((size_t)(l16 >> 3) * 8 + x) * 8192 + 6144 + (l16 & 7) * 256) * 256;
      cptr p0 = isA ? mkptr(kc3[2], kc3[3]) + (size_t)(x * 16 + l16) * 131072 : mkptr(kc3[6], kc3[7]) + kvb;
      cptr p1 = isA ? mkptr(kc3[10], kc3[11]) + q4 * 1048576 + ht * 256 : mkptr(kc3[8], kc3[9]) + kvb;
      cptr p2 = isA ? p1 + 1040384 : mkptr(kc3[12], kc3[13]) + ht * 131072 + q4 * 256;
      cptr p3 = isA ? p0 + 130944 : mkptr(kc3[14], kc3[15]) + ht * 131072 + q4 * 256;
      cptr pp = (lane & 2) ? ((lane & 1) ? p3 : p2) : ((lane & 1) ? p1 : p0);
      if (KEYMIX && isA && (lane & 3) == 0) pp = mkptr(kc3[2], kc3[3]) + (size_t)(x >> 2) * 8388608 + ((x & 3) * 16 + l16) * 16384 + ((lane >> 2) & 7) * 1048576;
      warm = __builtin_nontemporal_load((const GAS u32*)pp);
      SB();
    }
  }
  __amdgpu_buffer_rsrc_t rs;
#define SETWS(lo, hi) do { ws = (unsigned char*)mkptr(lo, hi); rs = __builtin_amdgcn_make_buffer_rsrc((void*)ws, 0, WS_BYTES, 0x00020000); } while (0)
#define START(id) do { BAR0(id); const v2u p_ = *lp<v2u>(L_PTR); SETWS(__builtin_amdgcn_readfirstlane(p_.x), __builtin_amdgcn_readfirstlane(p_.y)); } while (0)
  u32 tag;
#define SIG_AT(ISSUED, W, NEED, WORD) if constexpr ((ISSUED) - (W) >= (NEED) && (ISSUED) - 4 - (W) < (NEED)) { vmwait<W>(); sig(WORD, lane); }
  const u32 w = wave & 3;
  const u32 ab = (8 * g + tq) * 256 + 16 * ((4 * w + (tp >> 1)) ^ ((tq | ((g & 1) << 2)) << 1)) + 8 * (tp & 1);

#define PROJ(BEFORE_STORE, DST) do { \
    v4u xf[4], af[8]; \
    _Pragma("unroll") for (int s = 0; s < 4; ++s) xf[s] = lds128(L_X + (lane & 1) * 256 + g * 16 + s * 64); \
    _Pragma("unroll") for (int s = 0; s < 8; ++s) { const u32 a = L_WQ + (ab ^ (32 * (s & 1))) + (s >> 1) * 8192; af[s] = ldstr(a, a + 1024); } \
    SB(); \
    f32x4 c0 = {0.f, 0.f, 0.f, 0.f}, c1 = c0; \
    _Pragma("unroll") for (int s = 0; s < 4; ++s) { c0 = mfma16(af[2 * s], xf[s], c0); c1 = mfma16(af[2 * s + 1], xf[s], c1); } \
    const f32x4 c = (n & 4) ? c1 : c0; \
    BEFORE_STORE; \
    if (n < 8) ST1((v4u{u_((n & 2) ? c[2] : c[0]), tag, u_((n & 2) ? c[3] : c[1]), tag}), (DST) + (16 * w + 8 * (n >> 2) + 2 * g + ((n >> 1) & 1)) * 16); \
  } while (0)

  if (isA) {
    if (wave == 0) {
      STUB(1);
      SETWS(kb[0], kb[1]);
      __builtin_amdgcn_s_setprio(1);
      cptr src = mkptr(kb[2], kb[3]) + (KEYMIX ? (size_t)(x >> 2) * 8388608 + ((x & 3) * 16 + l16) * 16384 : (size_t)(x * 16 + l16) * 131072);
      constexpr u32 HS = KEYMIX ? 1048576 : 16384;
      u32 cv[16];
#pragma unroll
      for (int i = 0; i < 16; ++i) cv[i] = (lane ^ (((i & 3) << 2) | (i >> 2))) * 16;
#pragma unroll 1
      for (u32 H = 0; H < 8; ++H) {
        if (H == 6) { waitw<false>(S_XRD, 6); sig(S_CISS, lane); }
#pragma unroll
        for (int G = 0; G < 4; ++G) {
          dma4r<WINA>(src + H * HS + G * 4096, cv[4 * G], cv[4 * G + 1], cv[4 * G + 2], cv[4 * G + 3], L_C + H * 16384 + G * 4096);
          if (WQMIX && G < 3 && 3 * H + G < 8) dma4r<WINA>(mkptr(ka[6], ka[7]) + (3 * H + G) * 131072, ce, ce + 31744, cd, cd + 31744, L_WQ + (3 * H + G) * 4096);
          constexpr int WQAT = (WQMIX ? 9 : -1) + WINA / 4;
          if (G == (WQAT & 3) && H == WQAT / 4) { vmwait<WINA>(); sig(S_WQ, lane); STAMP(6); }
        }
      }
      sig(S_CISS, lane);
      STAMP(1);
      vmwait<0>();
      STAMP(2);
      bar();
      v4u park[32];
      const u32 vo = (128 * q4 + g) * 8192 + ht * 256 + n * 16;
#pragma unroll
      for (int i = 0; i < 32; ++i) park[i] = ldnt(mkptr(kb[10], kb[11]), vo + i * 32768);
      const u32 tv = TAGLOAD();
      SB();
      STAMP(3);
      for (u32 it = 1; it <= QTRIES; ++it) {
        bar(); bar();
        if (!qbad(lane) || it == QTRIES) break;
        bar(); bar();
      }
      bar(); bar();
      stage_w(L_W1, park, g, n);
      tag = __builtin_amdgcn_readfirstlane(tv) + 1;
      STAMP(4);
    } else if (wave < 4) {
      START(1);
      if (wave == 1) {
        asm volatile("" :: "v"(warm));
        TOUCH(11); TOUCH(12); TOUCH(13); TOUCH(14); TOUCH(15); TOUCH(16); TOUCH(17); TOUCH(18); TOUCH(19);
        if (lane < 6) { u32 t = __builtin_amdgcn_raw_buffer_load_b32(rs, lane < 2 ? lane * 1048576 + 262144 - 64 : WS_APART + (lane - 1) * APART_BLK - 128, 0, 16); asm volatile("" :: "v"(t)); }
      }
      if (wave != 2) sig(S_XRD, lane);
      asm volatile("" :: "v"(warm));
      tag = __builtin_amdgcn_readfirstlane(TAGLOAD()) + 1;
      v4u h0, h1;
      cptr qs = ws + WS_QABS + (x >> 2) * 32768;
#define Q_ISSUE() _Pragma("unroll 1") for (u32 G = 0; G < 8; ++G) { const u32 l0 = (lane ^ (G & 3)) * 16; \
          dma4s(qs + G * 4096, l0, l0 ^ 64, l0 ^ 128, l0 ^ 192, L_Q + G * 4096); }
#define HINT_LD(p, q) do { p = LD1(WS_HINT + lane * 128); q = LD1(WS_HINT + 8192 + lane * 128); asm volatile("" ::: "memory"); } while (0)
      if (wave == 2) {
        __builtin_amdgcn_s_setprio(2);
        waitw<true>(S_CISS, 1);
#pragma unroll 1
        for (int spin = 0; spin < 64; ++spin) {
          const u32 gt = __builtin_amdgcn_raw_buffer_load_b32(rs, WS_GATE + (lane & 7) * 128, 0, 16);
          asm volatile("" ::: "memory");
          const bool ok = __all(gt == tag);
          if (spin == 0) waitw<false>(S_CISS, 2);
          if (ok) break;
        }
        STAMP(3);
        if (OPTQ) {
          asm volatile("global_load_dwordx4 %0, %2, %4 sc1\n\tglobal_load_dwordx4 %1, %3, %4 sc1"
                       : "=&v"(h0), "=&v"(h1) : "v"(WS_HINT + lane * 128), "v"(WS_HINT + 8192 + lane * 128), "s"(ws) : "memory");
          Q_ISSUE()
          asm volatile("s_waitcnt vmcnt(32)" : "+v"(h0), "+v"(h1) :: "memory");
        } else HINT_LD(h0, h1);
        if (!OPTQ || !__all(h0.z == tag && h1.z == tag)) {
#pragma unroll 1
          for (int spin = 0; spin < 64; ++spin) {
            if (__all(h0.z == tag && h1.z == tag)) break;
            HINT_LD(h0, h1);
          }
          Q_ISSUE()
        }
        STAMP(1);
        vmwait<0>();
      }
      bar();
      for (u32 it = 1; it <= QTRIES; ++it) {
        bar();
        if (wave == 2) {
          u32 e0 = (x & 4 ? h0.y : h0.x) - tag, e1 = (x & 4 ? h1.y : h1.x) - tag;
          e0 += dppu<X1>(e0); e1 += dppu<X1>(e1); e0 += dppu<X2>(e0); e1 += dppu<X2>(e1);
          if (tp < 2) *lp<u32>(L_EXP + (lane >> 2) * 4 + tp * 64) = tp ? e1 : e0;
        }
        bar();
        if (!qbad(lane) || it == QTRIES) break;
        bar();
        if (wave == 2) { HINT_LD(h0, h1); Q_ISSUE() vmwait<0>(); }
        bar();
      }
      bar(); bar();
      if (wave == 1) *lp<u32>(L_BAD) = 0;
    } else {
      START(2);
      tag = __builtin_amdgcn_readfirstlane(TAGLOAD()) + 1;
      const u32 bs = x * 16 + l16, b = bs >> 6, hh = lane >> 5, hd = lane & 31;
      __builtin_amdgcn_s_setprio(2);
      waitw<false>(S_WQ, 1);
      PROJ(sig(S_XRD, lane), WS_QB + x * 32768 + (n & 1) * 16384 + (q4 * 4 + (((l16 >> 2) + 1) & 3)) * 1024);
      STAMP(13);
      STUB(11);
      f32x16 s;
      v4u mm;
      for (u32 it = 1; it <= QTRIES; ++it) {
        u32 cs = 0;
        bar();
        STAMP(3);
#pragma unroll
        for (int i = 0; i < 16; ++i) s[i] = 0.f;
        {
          const u32 fr = ((hd & 3) << 2) | ((hd >> 2) & 3);
          const u32 qa = L_Q + hd * 1024 + 16 * (hh ^ fr), ca = qa - L_Q + L_C + w * 32768;
          v4u af[32], bf[32];
          constexpr int PD = 6;
#define S_RD(k) do { bf[k] = lds128((qa ^ (32 * ((k) & 7))) + 256 * ((k) >> 3)); af[k] = lds128((ca ^ (32 * ((k) & 7))) + 256 * ((k) >> 3)); } while (0)
#pragma unroll
          for (int k = 0; k < PD; ++k) S_RD(k);
          SB();
#pragma unroll
          for (int k = 0; k < 32; ++k) {
            if (k + PD < 32) S_RD(k + PD);
            s = mfma32(af[k], bf[k], s);
            cs = cs + bf[k].x + bf[k].y; cs = cs + bf[k].z + bf[k].w;
            if (k == 4) STUB(12);
            if (k == 26) { STUB(13); }
            if (k == 29) bar();
            SB();
          }
        }
        { auto t = __builtin_amdgcn_permlane32_swap(cs, cs, false, false); cs = t[0] + t[1]; }
        float m = s[0];
#pragma unroll
        for (int i = 1; i < 16; ++i) m = fmaxf(m, s[i]);
        m = max32(m);
        if (hh == 0) *lp<float>(L_MAXX + hd * 16 + w * 4) = m;
        if (w == 0 && hh == 0) *lp<u32>(L_GOT + hd * 4) = cs;
        STAMP(4);
        bar();
        mm = lds128(L_MAXX + hd * 16);
        if (!qbad(lane) || it == QTRIES) break;
        bar();
      }
      const float mk = fmaxf(fmaxf(f_(mm.x), f_(mm.y)), fmaxf(f_(mm.z), f_(mm.w))) * KSC;
      float ls = 0.f;
#pragma unroll
      for (int i = 0; i < 16; ++i) { s[i] = __builtin_amdgcn_exp2f(fmaf(s[i], KSC, -mk)); ls += s[i]; }
      lds128w(L_P + (2 * w) * 1024 + lane * 16, v4u{pk(s[0], s[1]), pk(s[2], s[3]), pk(s[4], s[5]), pk(s[6], s[7])});
      lds128w(L_P + (2 * w + 1) * 1024 + lane * 16, v4u{pk(s[8], s[9]), pk(s[10], s[11]), pk(s[12], s[13]), pk(s[14], s[15])});
      ls = sum32(ls);
      if (hh == 0) *lp<float>(L_LSUM + hd * 16 + w * 4) = ls;
      const u32 blk = (lane >> 4) & 1;
      u32 ta[2];
#pragma unroll
      for (int t = 0; t < 2; ++t)
        ta[t] = L_C + (4 * hh + 8 * t + tq) * 1024 + 256 * w + 16 * ((2 * blk + (tp >> 1)) ^ ((tq << 2) | (hh + 2 * t))) + 8 * (tp & 1);
      v4u cf[32];
      constexpr int PV_PD = 5;
#define PV_RD(kk) cf[kk] = ldstr((ta[0] ^ (64 * ((kk) >> 3))) + ((kk) & 7) * 16384, (ta[1] ^ (64 * ((kk) >> 3))) + ((kk) & 7) * 16384)
#pragma unroll
      for (int k = 0; k < PV_PD; ++k) PV_RD(k);
      SB();
      STUB(14);
      bar();
      STAMP(5);
      v4u pf[8];
#pragma unroll
      for (int k = 0; k < 8; ++k) pf[k] = lds128(L_P + k * 1024 + lane * 16);
      const v4u lv = lds128(L_LSUM + hd * 16);
      SB();
      const u32 scw = L_SCR + w * 4096 + hd * 128 + 8 * hh + 16 * (hd & 7);
      const u32 scr = L_SCR + w * 4096 + (lane >> 3) * 128 + 16 * ((lane & 7) ^ (lane >> 3));
      const u32 sto = WS_APART + (((lane >> 3) * 4 + w) * 128 + bs) * 256 + 16 * (lane & 7);
      f32x16 o[4];
      v4u ov[4];
      u32 cs = tag;
#define EPI_W(c, gg) do { \
        const u32 p0 = pk(o[c][4 * (gg)], o[c][4 * (gg) + 1]), p1 = pk(o[c][4 * (gg) + 2], o[c][4 * (gg) + 3]); \
        lds64w(scw ^ (64 * ((c) & 1) + 16 * (gg)), p0, p1); cs += p0 + p1; asm volatile("" : "+v"(cs)); } while (0)
#define EPI_R() _Pragma("unroll") for (int i = 0; i < 4; ++i) ov[i] = lds128(scr + i * 1024);
#define EPI_S(p, i) ST1(ov[i], sto + 128 * (p) + (i) * APART_BLK)
#pragma unroll
      for (int kk = 0; kk < 32; ++kk) {
        const int c = kk >> 3, k = kk & 7;
        if (kk + PV_PD < 32) PV_RD(kk + PV_PD);
        if (k == 0) {
          f32x16 z;
#pragma unroll
          for (int i = 0; i < 16; ++i) z[i] = 0.f;
          o[c] = mfma32(cf[kk], pf[k], z);
        } else o[c] = mfma32(cf[kk], pf[k], o[c]);
        if (c > 0 && k >= 1 && k <= 4) EPI_W(c > 0 ? c - 1 : 0, (k - 1) & 3);
        if (kk == 20) { EPI_R() }
        if (kk == 23) { EPI_S(0, 0); EPI_S(0, 1); }
        if (kk == 24) { EPI_S(0, 2); EPI_S(0, 3); }
        if (kk == 13) STUB(15);
        if (kk == 22) STUB(16);
        SB();
      }
      EPI_W(3, 0); EPI_W(3, 1); EPI_W(3, 2); EPI_W(3, 3); EPI_R()
      const float lsum = (f_(lv.x) + f_(lv.y)) + (f_(lv.z) + f_(lv.w));
      { auto t = __builtin_amdgcn_permlane32_swap(cs, cs, false, false); cs = t[0] + t[1] - tag + u_(mk) + u_(lsum); }
      EPI_S(1, 0); EPI_S(1, 1); EPI_S(1, 2); EPI_S(1, 3);
      if (hh == 0) ST1((v4u{u_(mk), u_(lsum), cs, tag}), WS_ASTAT + bs * 2048 + w * 512 + hd * 16);
      STAMP(7);
      bar();
    }
    const u32 v = wave;
    v4u a1[8], bq[4];
    STUB(17);
    const u64 tpv = __builtin_amdgcn_s_memrealtime();
    bar();
    if (wave >= 4) {
#pragma unroll
      for (int s = 0; s < 8; ++s) { const u32 a = L_W1 + (ab ^ (32 * (s >> 2))) + (s & 3) * 8192; a1[s] = ldstr(a, a + 1024); }
    }
    if (wave == 3) {
      while (__builtin_amdgcn_s_memrealtime() - tpv < PDELAY) { }
    }
    for (u32 it = 257; it < 320; ++it) {
      v4u st0, st1;
      if (wave == 3) {
        for (int spin = 0; spin < SPIN_CAP; ++spin) {
          st0 = LD1(WS_ASTAT + lane * 2048 + q4 * 512 + ht * 16);
          st1 = LD1(WS_ASTAT + 131072 + lane * 2048 + q4 * 512 + ht * 16);
          asm volatile("" ::: "memory");
          if (__all(st0.w == tag && st1.w == tag)) break;
        }
        STAMP(2);
      }
      bar();
      STAMP(8);
      v4u pd[4];
#pragma unroll
      for (int i = 0; i < 4; ++i) pd[i] = LD1(WS_APART + (ht >> 3) * APART_BLK + ((ht & 7) * 4 + q4) * 32768 + v * 4096 + i * 1024 + lane * 16);
      SB();
      if (wave == 3) {
#pragma unroll
        for (int i = 0; i < 2; ++i) {
          const v4u st = i ? st1 : st0;
          float M = f_(st.x);
          M = fmaxf(M, dpp<X1>(M)); M = fmaxf(M, dpp<X2>(M)); M = fmaxf(M, dpp<X7>(M)); M = fmaxf(M, dpp<X8>(M)); M = max16(M); M = max32(M);
          const float e = __builtin_amdgcn_exp2f(f_(st.x) - M);
          float S = e * f_(st.y);
          S += dpp<X1>(S); S += dpp<X2>(S); S += dpp<X7>(S); S += dpp<X8>(S); S = sum16(S); S = sum32(S);
          *lp<float>(L_WGT + i * 256 + lane * 4) = e / S;
          *lp<u32>(L_CK + i * 256 + lane * 4) = st.z - tag - st.x - st.y;
        }
      }
      STUB(18);
      bar();
      float acc[8], wt[4];
#pragma unroll
      for (int i = 0; i < 4; ++i) wt[i] = *lp<float>(L_WGT + (v * 16 + 4 * i + g) * 4);
      const u32 ck = *lp<u32>(L_CK + (v * 16 + 4 * (lane & 3) + g) * 4);
#pragma unroll
      for (int k = 0; k < 4; ++k) { acc[2 * k] = wt[0] * blo(pd[0][k]); acc[2 * k + 1] = wt[0] * bhi(pd[0][k]); }
#pragma unroll
      for (int i = 1; i < 4; ++i)
#pragma unroll
        for (int k = 0; k < 4; ++k) { acc[2 * k] = fmaf(wt[i], blo(pd[i][k]), acc[2 * k]); acc[2 * k + 1] = fmaf(wt[i], bhi(pd[i][k]), acc[2 * k + 1]); }
      STAMP(9);
      {
        float y[4];
#pragma unroll
        for (int k = 0; k < 4; ++k) { auto t = __builtin_amdgcn_permlane32_swap(u_(acc[k]), u_(acc[k + 4]), false, false); y[k] = f_(t[0]) + f_(t[1]); }
        float z[2];
#pragma unroll
        for (int k = 0; k < 2; ++k) { auto t = __builtin_amdgcn_permlane16_swap(u_(y[k]), u_(y[k + 2]), false, false); z[k] = f_(t[0]) + f_(t[1]); }
        const u32 hi = pk(z[0], z[1]), lo = pk(z[0] - blo(hi), z[1] - bhi(hi));
        *lp<u32>(L_B1 + v * 272 + (8 * n + 2 * g) * 2) = hi;
        *lp<u32>(L_B1 + (8 + v) * 272 + (8 * n + 2 * g) * 2) = lo;
        u32 c4[4], c2[2];
#pragma unroll
        for (int i = 0; i < 4; ++i) c4[i] = (pd[i].x + pd[i].y) + (pd[i].z + pd[i].w);
#pragma unroll
        for (int i = 0; i < 2; ++i) { const u32 p = c4[i] + dppu<X2>(c4[i]), q = c4[i + 2] + dppu<X2>(c4[i + 2]); c2[i] = (lane & 2) ? q : p; }
        const u32 p1 = c2[0] + dppu<X1>(c2[0]), q1 = c2[1] + dppu<X1>(c2[1]);
        u32 c1 = (lane & 1) ? q1 : p1;
        c1 += dppu<X8>(c1);
        c1 += dppu<0x124>(c1);
        if (c1 != ck) *lp<u32>(L_BAD) = it;
      }
      STUB(19);
      bar();
      const u32 bad = *lp<u32>(L_BAD);
#pragma unroll
      for (int s = 0; s < 4; ++s) bq[s] = lds128(L_B1 + n * 272 + g * 16 + s * 64);
      SB();
      if (__builtin_amdgcn_readfirstlane(bad) != it) break;
    }
    if (wave >= 4) {
      f32x4 d0 = {0.f, 0.f, 0.f, 0.f}, d1 = d0;
#pragma unroll
      for (int s = 0; s < 4; ++s) { d0 = mfma16(a1[s], bq[s], d0); d1 = mfma16(a1[4 + s], bq[s], d1); }
#pragma unroll
      for (int k = 0; k < 4; ++k) {
        const float t = d0[k] + dpp<X1>(d0[k]), u = d1[k] + dpp<X1>(d1[k]);
        float z = (n & 1) ? u : t;
        z += dpp<X2>(z); z += dpp<X8>(z); d0[k] = z;
      }
      if ((n & 10) == 0) ST1((v4u{pk(d0[0], d0[1]), tag, pk(d0[2], d0[3]), tag}), WS_H3B + ht * 4096 + q4 * 1024 + (n >> 2) * 512 + (8 * w + 4 * (n & 1) + g) * 16);
    }
    STAMP(10);
    if (wave == 0) {
      const u32 rho = x * 8 + (l16 >> 1), part = l16 & 1;
      u16* outp = out; asm volatile("" : "+s"(outp));
      { const u64 t0 = __builtin_amdgcn_s_memrealtime(); while (__builtin_amdgcn_s_memrealtime() - t0 < (part ? 0 : FDELAY)) { } }
      v4u e[4];
      for (int spin = 0; spin < SPIN_CAP; ++spin) {
        bool ok = true;
#pragma unroll
        for (int i = 0; i < 4; ++i) e[i] = LD1(WS_H4 + part * 262144 + rho * 4096 + i * 1024 + lane * 16);
        asm volatile("" ::: "memory");
#pragma unroll
        for (int i = 0; i < 4; ++i) ok = ok && e[i].y == tag && e[i].w == tag;
        if (__all(ok)) break;
      }
      float s0 = (f_(e[0].x) + f_(e[1].x)) + (f_(e[2].x) + f_(e[3].x)), s1 = (f_(e[0].z) + f_(e[1].z)) + (f_(e[2].z) + f_(e[3].z));
      s0 += dpp<X8>(s0); s1 += dpp<X8>(s1);
      s0 = sum32(sum16(s0)); s1 = sum32(sum16(s1));
      if (lane < 8) *(GAS u32*)(outp + (rho & 1) * 1024 + part * 512 + (rho >> 1) * 16 + 2 * lane) = pk(s0, s1);
      if (gid == 0 && lane == 0) __builtin_amdgcn_raw_buffer_store_b32(tag, rs, WS_EPOCH, 0, 16);
      STAMP(13);
    }
  } else {
    if (wave < 4) {
      if (wave == 0) {
        STUB(2);
        SETWS(kb[0], kb[1]);
        __builtin_amdgcn_s_setprio(1);
        {
          const u32 uo = q4 * 1048576 + ht * 256 + g * 8192;
          u32 uv[4];
#pragma unroll
          for (int i = 0; i < 4; ++i) uv[i] = uo + ((n ^ (g | (i << 2))) * 16) + (i & 1) * 63488 + (i >> 1) * 131072;
          cptr uk = mkptr(kb[4], kb[5]);
#pragma unroll 1
          for (u32 H = 0; H < 4; ++H) {
            dma4r<32>(uk + H * 262144, uv[0], uv[0] + 31744, uv[1], uv[1] + 31744, L_UK + H * 8192);
            dma4r<32>(uk + H * 262144, uv[2], uv[2] + 31744, uv[3], uv[3] + 31744, L_UK + H * 8192 + 4096);
          }
        }
        vmwait<32>(); sig(S_WQ, lane);
        STAMP(14);
        vmwait<0>(); sig(S_UK, lane);
        STAMP(5);
        tag = __builtin_amdgcn_readfirstlane(TAGLOAD()) + 1;
        STUB(3);
        waitw<false>(S_PUB, 5);
        while (__builtin_amdgcn_s_memrealtime() - t0 < KSTART) __builtin_amdgcn_s_sleep(1);
        STAMP(13);
        const size_t base = (((size_t)(l16 >> 3) * 8 + x) * 8192 + 6144 + (l16 & 7) * (KVMIX ? 16 : 256)) * 256;
        constexpr u32 GS = KVMIX ? 32768 : 4096;
        cptr ks = mkptr(kb[6], kb[7]) + base, vs = mkptr(kb[8], kb[9]) + base;
        static_assert(WINB % 4 == 0 && NB0 <= 3 + WINB / 4, "block signals");
        {
          u32 kv[4];
#pragma unroll
          for (int i = 0; i < 4; ++i) kv[i] = g * 256 + ((n ^ (4 * i + g)) * 16);
#pragma unroll 1
          for (u32 G = 0; G < NB0; ++G) dma4r<WINB0>(ks + G * GS, kv[0], kv[1], kv[2], kv[3], L_K + G * 4096);
#pragma unroll 1
          for (u32 G = NB0; G < 16; ++G) {
            dma4r<WINB>(ks + G * GS, kv[0], kv[1], kv[2], kv[3], L_K + G * 4096);
            if (((G + 1 - WINB / 4) & 3) == 0 && G >= 3 + WINB / 4) { vmwait<WINB>(); sig(S_LAND, lane); }
          }
        }
        STAMP(1);
        {
          const u32 v0 = g * 256 + ((n ^ (g << 1)) * 16), v1 = g * 256 + ((n ^ ((4 + g) << 1)) * 16);
#pragma unroll 1
          for (u32 G = 0; G < 16; ++G) {
            if (ABL != 3) dma4r<WINB>(vs + G * GS, v0, v1, v0, v1, L_V + G * 4096);
            if (((G + 1 - WINB / 4) & 3) == 0 && G + 16 >= 3 + WINB / 4) { vmwait<WINB>(); sig(S_LAND, lane); }
          }
        }
        STAMP(2);
        waitw<false>(S_KRD, 4);
        {
          const u32 vo = (ht * 128 + g) * 1024 + q4 * 256, w0 = vo + ((n ^ (g << 1)) * 16), w1 = vo + ((n ^ ((g | 4) << 1)) * 16) + 6144;
#pragma unroll 1
          for (u32 G = 0; G < 16; ++G) {
            cptr wp = (G < 8 ? mkptr(kb[14], kb[15]) : mkptr(kb[12], kb[13])) + (G & 7) * 16384;
            if (ABL != 3) dma4r<WINB>(wp, w0, w0 + 3072, w1, w1 + 3072, L_W1 + G * 4096);
            if (((G + 1 - WINB / 4) & 3) == 0) { vmwait<WINB>(); sig(S_LAND, lane); }
          }
        }
        STAMP(3);
        vmwait<32>(); sig(S_W1, lane);
        vmwait<0>(); sig(S_W2, lane);
        STAMP(4);
      } else if (wave == 1) {
        START(3);
        const u64 t1 = __builtin_amdgcn_s_memrealtime();
        __builtin_amdgcn_s_setprio(2);
        asm volatile("" :: "v"(warm));
        TOUCH(4); TOUCH(3); TOUCH(5); TOUCH(6); TOUCH(7); TOUCH(8); TOUCH(9); TOUCH(10);
        if (lane < 6) { u32 t = __builtin_amdgcn_raw_buffer_load_b32(rs, lane < 2 ? lane * 1048576 + 262144 - 64 : WS_APART + (lane - 1) * APART_BLK - 128, 0, 16); asm volatile("" :: "v"(t)); }
        tag = __builtin_amdgcn_readfirstlane(TAGLOAD()) + 1;
        waitw<false>(S_PUB, 4);
        {
          const v4u pv = lds128(L_QX + (lane & 31) * 16);
          if (lane < 32) ST1(pv, WS_QABS + (lane >> 4) * 32768 + ht * 1024 + q4 * 256 + n * 16);
          u32 cs = (pv.x + pv.y) + (pv.z + pv.w);
          cs += dppu<X1>(cs); cs += dppu<X2>(cs); cs += dppu<X7>(cs); cs += dppu<X8>(cs);
          auto t = __builtin_amdgcn_permlane16_swap(cs, cs, false, false);
          if (lane == 0) ST1((v4u{t[0] + tag, t[1] + tag, tag, tag}), WS_HINT + (x * 16 + l16) * 128);
          if (l16 == GATEL && lane == 0) __builtin_amdgcn_raw_buffer_store_b32(tag, rs, WS_GATE + x * 128, 0, 16);
        }
        sig(S_PUB, lane);
        STAMP(2);
        while (__builtin_amdgcn_s_memrealtime() - t1 < QBSTART) __builtin_amdgcn_s_sleep(1);
        v4u e[16];
        for (int spin = 0; spin < SPIN_CAP; ++spin) {
          bool ok = true;
#pragma unroll
          for (int i = 0; i < 16; ++i) e[i] = LD1(WS_QB + x * 32768 + (l16 >> 3) * 16384 + i * 1024 + lane * 16);
          asm volatile("" ::: "memory");
#pragma unroll
          for (int i = 0; i < 16; ++i) ok = ok && e[i].y == tag && e[i].w == tag;
          if (__all(ok)) break;
        }
#pragma unroll
        for (int i = 0; i < 4; ++i)
          *lp<u32>(L_QBL + i * 256 + lane * 4) = pk((f_(e[i].x) + f_(e[4 + i].x)) + (f_(e[8 + i].x) + f_(e[12 + i].x)), (f_(e[i].z) + f_(e[4 + i].z)) + (f_(e[8 + i].z) + f_(e[12 + i].z)));
        sig(S_QBRDY, lane);
        STAMP(3);
      } else {
        START(4);
        asm volatile("" :: "v"(warm));
        tag = __builtin_amdgcn_readfirstlane(TAGLOAD()) + 1;
      }
      STUB(10);
      waitw<true>(S_W2, 1);
      v4u a2[8];
#pragma unroll
      for (int s = 0; s < 8; ++s) { const u32 a = L_W2 + (ab ^ (32 * (s >> 2))) + (s & 3) * 8192; a2[s] = ldstr(a, a + 1024); }
      v4u e;
      for (int spin = 0; spin < SPIN_CAP; ++spin) {
        e = LD1(WS_H3B + ht * 4096 + w * 1024 + lane * 16);
        asm volatile("" ::: "memory");
        if (__all(e.y == tag && e.w == tag)) break;
      }
      lds64w(L_B2 + ((lane >> 5) * 4 + w) * 272 + (lane & 31) * 8, e.x, e.z);
      sig(S_TA, lane);
      waitw<false>(S_TA, 4);
      STAMP(11);
      v4u bq[4];
#pragma unroll
      for (int s = 0; s < 4; ++s) bq[s] = lds128(L_B2 + (n & 7) * 272 + g * 16 + s * 64);
      SB();
      f32x4 d0 = {0.f, 0.f, 0.f, 0.f}, d1 = d0;
#pragma unroll
      for (int s = 0; s < 4; ++s) { d0 = mfma16(a2[s], bq[s], d0); d1 = mfma16(a2[4 + s], bq[s], d1); }
      f32x4 d = (n & 8) ? d1 : d0;
#pragma unroll
      for (int k = 0; k < 4; ++k) { float t = d[k]; t += dpp<X1>(t); t += dpp<X2>(t); d[k] = t; }
      const bool k1 = (n & 1) != 0;
      if ((n & 2) == 0) ST1((v4u{u_(k1 ? d[2] : d[0]), tag, u_(k1 ? d[3] : d[1]), tag}),
                            WS_H4 + (((q4 * 8 + 2 * w + (n >> 3)) * 2 + ((n >> 2) & 1)) * 32 + ht) * 128 + g * 32 + (n & 1) * 16);
      STAMP(12);
    } else {
      START(5);
      tag = __builtin_amdgcn_readfirstlane(TAGLOAD()) + 1;
      __builtin_amdgcn_s_setprio(2);
      {
        waitw<false>(S_WQ, 1);
        STAMP(10);
        PROJ((void)0, WS_H1 + (4 * x + (((l16 >> 2) + 1) & 3)) * 8192 + q4 * 2048 + (n & 1) * 1024);
        STAMP(11);
        STUB(4);
        waitw<false>(S_UK, 1);
        v4u uk[8];
#pragma unroll
        for (int t = 0; t < 2; ++t)
#pragma unroll
          for (int s = 0; s < 4; ++s) uk[t * 4 + s] = lds128(L_UK + (32 * w + 8 * (n >> 2) + 4 * t + (n & 3)) * 256 + 16 * ((4 * s + g) ^ n));
        v4u e0, e1;
#define H1_LD() do { e0 = LD1(WS_H1 + ht * 8192 + g * 2048 + w * 256 + n * 16); e1 = LD1(WS_H1 + ht * 8192 + g * 2048 + 1024 + w * 256 + n * 16); \
                     asm volatile("" ::: "memory"); } while (0)
        H1_LD();
        for (int spin = 0; spin < SPIN_CAP; ++spin) {
          if (__all(e0.y == tag && e0.w == tag && e1.y == tag && e1.w == tag)) break;
          H1_LD();
        }
        STAMP(14);
        {
          auto p = __builtin_amdgcn_permlane32_swap(e0.x, e1.x, false, false), q = __builtin_amdgcn_permlane32_swap(e0.z, e1.z, false, false);
          const float a = sum16(f_(p[0]) + f_(p[1])), c = sum16(f_(q[0]) + f_(q[1]));
          if ((g & 1) == 0) *lp<u32>(L_QL + (g >> 1) * 256 + w * 64 + n * 4) = pk(a, c);
        }
        sig(S_H1, lane);
        waitw<false>(S_H1, 4);
        v4u qb[4];
#pragma unroll
        for (int s = 0; s < 4; ++s) qb[s] = lds128(L_QL + (lane & 1) * 256 + g * 16 + s * 64);
        SB();
        f32x4 u0 = {0.f, 0.f, 0.f, 0.f}, u1 = u0;
#pragma unroll
        for (int s = 0; s < 4; ++s) { u0 = mfma16(uk[s], qb[s], u0); u1 = mfma16(uk[4 + s], qb[s], u1); }
        const v4u pv = {pk(u0[0], u0[1]), pk(u0[2], u0[3]), pk(u1[0], u1[1]), pk(u1[2], u1[3])};
        if (n < 2) lds128w(L_QX + n * 256 + w * 64 + g * 16, pv);
        sig(S_PUB, lane);
        STAMP(15);
      }
      STUB(5);
      waitw<true>(S_LAND, w + 1);
      f32x4 sc[4];
      {
        v4u kf[16], qf[4];
        const u32 ka = L_K + (64 * w + n) * 256 + 16 * (g ^ n);
#pragma unroll
        for (int i = 0; i < 16; ++i) kf[i] = lds128((ka ^ (64 * (i & 3))) + (i >> 2) * 4096);
        sig(S_KRD, lane);
        SB();
        waitw<false>(S_QBRDY, 1);
        STAMP(3);
#pragma unroll
        for (int s = 0; s < 4; ++s) qf[s] = lds128(L_QBL + (lane & 3) * 256 + g * 16 + s * 64);
        SB();
#pragma unroll
        for (int t = 0; t < 4; ++t) {
          sc[t] = f32x4{0.f, 0.f, 0.f, 0.f};
#pragma unroll
          for (int s = 0; s < 4; ++s) sc[t] = mfma16(kf[4 * t + s], qf[s], sc[t]);
        }
      }
      float m = sc[0][0];
#pragma unroll
      for (int i = 1; i < 16; ++i) m = fmaxf(m, sc[i >> 2][i & 3]);
      m = max32(max16(m));
      if (lane < 4) *lp<float>(L_MAXX + lane * 16 + w * 4) = m;
      sig(S_MAX, lane);
      STUB(6);
      waitw<false>(S_MAX, 4);
      const v4u mm = lds128(L_MAXX + (lane & 3) * 16);
      const float mk = fmaxf(fmaxf(f_(mm.x), f_(mm.y)), fmaxf(f_(mm.z), f_(mm.w))) * KSC;
      float ls = 0.f;
#pragma unroll
      for (int i = 0; i < 16; ++i) { const float p = __builtin_amdgcn_exp2f(fmaf(sc[i >> 2][i & 3], KSC, -mk)); sc[i >> 2][i & 3] = p; ls += p; }
      ls = sum32(sum16(ls));
      if (n < 4) {
#pragma unroll
        for (int u = 0; u < 2; ++u)
          lds128w(L_PB + (2 * w + u) * 256 + g * 64 + n * 16,
                  v4u{pk(sc[2 * u][0], sc[2 * u][1]), pk(sc[2 * u][2], sc[2 * u][3]), pk(sc[2 * u + 1][0], sc[2 * u + 1][1]), pk(sc[2 * u + 1][2], sc[2 * u + 1][3])});
      }
      if (lane < 4) *lp<float>(L_LSUM + lane * 16 + w * 4) = ls;
      sig(S_P, lane);
      STUB(7);
      waitw<false>(S_P, 4);
      STAMP(5);
      v4u pf[8];
#pragma unroll
      for (int k = 0; k < 8; ++k) pf[k] = lds128(L_PB + k * 256 + g * 64 + (lane & 3) * 16);
      if (w == 0) {
        const v4u lv = lds128(L_LSUM + (lane & 3) * 16);
        const float lsum = (f_(lv.x) + f_(lv.y)) + (f_(lv.z) + f_(lv.w));
        if (lane < 4) ST1((v4u{u_(mk), tag, u_(lsum), tag}), WS_BIN + (4 * x + lane) * 9216 + 8192 + l16 * 16);
      }
      f32x4 o0 = {0.f, 0.f, 0.f, 0.f}, o1 = o0;
      const u32 va = L_V + (4 * g + tq) * 256 + 16 * ((4 * w + (tp >> 1)) ^ ((4 * (g & 1) + tq) << 1)) + 8 * (tp & 1);
#pragma unroll
      for (int k2 = 0; k2 < 4; ++k2) {
        waitw<false>(S_LAND, k2 + 5);
        v4u vf[4];
#pragma unroll
        for (int i = 0; i < 4; ++i) { const u32 a = (va ^ (32 * (i & 1))) + (2 * k2 + (i >> 1)) * 8192; vf[i] = ldstr(a, a + 4096); }
        SB();
        o0 = mfma16(vf[0], pf[2 * k2], o0); o1 = mfma16(vf[1], pf[2 * k2], o1);
        o0 = mfma16(vf[2], pf[2 * k2 + 1], o0); o1 = mfma16(vf[3], pf[2 * k2 + 1], o1);
      }
      STAMP(6);
      {
        const f32x4 o = (n & 4) ? o1 : o0;
        if (n < 8) ST1((v4u{pk(o[0], o[1]), tag, pk(o[2], o[3]), tag}), WS_BIN + (4 * x + (n & 3)) * 9216 + l16 * 512 + (4 * (2 * w + (n >> 2)) + g) * 16);
      }
      STAMP(7);
      STUB(8);
      waitw<false>(S_W1, 1);
      STAMP(8);
      v4u a1[8];
#pragma unroll
      for (int s = 0; s < 8; ++s) { const u32 a = L_W1 + (ab ^ (32 * (s >> 2))) + (s & 3) * 8192; a1[s] = ldstr(a, a + 1024); }
      v4u e0, e1, st;
      for (int spin = 0; spin < SPIN_CAP; ++spin) {
        e0 = LD1(WS_BIN + ht * 9216 + w * 2048 + lane * 16);
        e1 = LD1(WS_BIN + ht * 9216 + w * 2048 + 1024 + lane * 16);
        st = LD1(WS_BIN + ht * 9216 + 8192 + n * 16);
        asm volatile("" ::: "memory");
        if (__all(e0.y == tag && e0.w == tag && e1.y == tag && e1.w == tag && st.y == tag && st.w == tag)) break;
      }
      STAMP(9);
      lds64w(L_BB + (4 * w + (lane >> 5)) * 272 + (lane & 31) * 8, e0.x, e0.z);
      lds64w(L_BB + (4 * w + 2 + (lane >> 5)) * 272 + (lane & 31) * 8, e1.x, e1.z);
      sig(S_TB, lane);
      float M = f_(st.x);
      M = fmaxf(M, dpp<X1>(M)); M = fmaxf(M, dpp<X2>(M)); M = fmaxf(M, dpp<X7>(M));
      const float ee = __builtin_amdgcn_exp2f(f_(st.x) - M);
      float S = ee * f_(st.z);
      S += dpp<X1>(S); S += dpp<X2>(S); S += dpp<X7>(S);
      const float wt = ee / S;
      STUB(9);
      waitw<false>(S_TB, 4);
      v4u bq[4];
#pragma unroll
      for (int s = 0; s < 4; ++s) bq[s] = lds128(L_BB + n * 272 + g * 16 + s * 64);
      SB();
      f32x4 d0 = {0.f, 0.f, 0.f, 0.f}, d1 = d0;
#pragma unroll
      for (int s = 0; s < 4; ++s) { d0 = mfma16(a1[s], bq[s], d0); d1 = mfma16(a1[4 + s], bq[s], d1); }
#pragma unroll
      for (int k = 0; k < 4; ++k) {
        float t = d0[k] * wt, u = d1[k] * wt;
        t += dpp<X1>(t); u += dpp<X1>(u); t += dpp<X2>(t); u += dpp<X2>(u); t += dpp<X7>(t); u += dpp<X7>(u);
        d0[k] = (n & 2) ? u : t;
      }
      const bool k1 = (n & 1) != 0;
      if ((n & 4) == 0) ST1((v4u{u_(k1 ? d0[2] : d0[0]), tag, u_(k1 ? d0[3] : d0[1]), tag}),
                            WS_H4 + 262144 + (((q4 * 8 + 2 * w + ((n >> 1) & 1)) * 2 + (n >> 3)) * 32 + ht) * 128 + g * 32 + (n & 1) * 16);
      STAMP(12);
    }
  }
}

extern "C" void kernel_launch(void* const* d_in, const int* in_sizes, int n_in, void* d_out, int out_size, void* d_ws, size_t ws_size,
                              hipStream_t stream) {
  (void)hipFuncSetAttribute((const void*)fused_decode, hipFuncAttributeMaxDynamicSharedMemorySize, 163840);
  fused_decode<<<256, 512, 163840, stream>>>((const u16*)d_in[0], (const u16*)d_in[2], (const u16*)d_in[6], (const u16*)d_in[9],
                                             (unsigned char*)d_ws, (const u16*)d_in[1], (const u16*)d_in[3], (const u16*)d_in[7],
                                             (const u16*)d_in[8], (const u16*)d_in[4], (const u16*)d_in[5], (const u16*)d_in[10],
                                             (u16*)d_out);
}
```

```cpp
#define STAMPS 2
#define XG 0
#include <hip/hip_runtime.h>
#include <hip/hip_bf16.h>

typedef unsigned u32;
typedef unsigned short u16;
typedef unsigned long long u64;
typedef u32 v4u __attribute__((ext_vector_type(4)));
typedef u32 v2u __attribute__((ext_vector_type(2)));
typedef short s16x4 __attribute__((ext_vector_type(4)));
typedef short bf16x8 __attribute__((ext_vector_type(8)));
typedef __bf16 bf16x2 __attribute__((ext_vector_type(2)));
typedef float f32x4 __attribute__((ext_vector_type(4)));
typedef float f32x16 __attribute__((ext_vector_type(16)));
#define GAS __attribute__((address_space(1)))
#define LAS __attribute__((address_space(3)))
#define DI __device__ __forceinline__
#define SB() __builtin_amdgcn_sched_barrier(0)

#define WIN0 32
#define WINA0 24
#define NA0 2
#define WINA 32
#define WINB0 8
#define NB0 0
#define WINB 24
#define THOLD 45
#define BGATE 0
#define WINU 32
#define WUV_AT 8
#define PDELAY 70
#define ABL 0
#define FDELAY 160
#define KSTART 450
#define XFIRST 0
#define HPOLL 6
#define QBDELAY 70
#define H1DELAY 0
#define WINUK 63
#define XPOL ""
#define NC0 0
#define OPTQ 1
#define GATEL 5
#define PAGES 1
#define QBSTART 350
#define KEYMIX 1
#define WQMIX 0
#define KVMIX 1
#define SOON 0
#define QW 8
#define CODE_BYTES 23552

#define TYPEBIT 5
constexpr u32 WS_EPOCH = 0;
constexpr u32 WS_GATE = 4096;
constexpr u32 WS_SOON = 8192;
constexpr u32 WS_HINT = 16384;
constexpr u32 WS_QABS = 65536;
constexpr u32 WS_ASTAT = 262144;
constexpr u32 WS_H3B = 524288;
constexpr u32 WS_BIN = 786432;
constexpr u32 WS_QB = 6291456 + 4096;
constexpr u32 WS_H4 = 1310720;
constexpr u32 WS_H1 = 1835008;
constexpr u32 WS_APART = 2097152;
constexpr u32 APART_BLK = 1048576 + 256;
constexpr u32 WS_BYTES = 6291456 + 4096 + 262144;
constexpr u32 WS_STAMP = 8388608;

constexpr u32 L_C = 0, L_K = 0, L_V = 65536, L_UK = 32768, L_WQ = 98304;
constexpr u32 L_Q = 131072;
constexpr u32 L_X = L_Q + 16384, L_QL = L_Q + 18432, L_QX = L_Q + 20480, L_QBL = L_Q + 22528, L_PB = L_Q + 24576;
constexpr u32 L_P = L_Q, L_SCR = L_Q + 8192, L_LSUM = L_Q + 28672, L_EXP = L_Q + 29696, L_GOT = L_Q + 29824;
constexpr u32 L_MAXX = L_Q + 31744, L_SYNC = L_MAXX + 512, L_PTR = L_SYNC + 256;
constexpr u32 L_W1 = 0, L_W2 = 32768, L_WGT = 65536, L_CK = 66048, L_BAD = 66560, L_B1 = 67584;
constexpr u32 L_BB = L_Q, L_B2 = L_Q + 8192;
enum { S_WQ = 0, S_UK, S_PUB, S_TAG, S_H1, S_QBRDY, S_XRD, S_CISS, S_HINT, S_QGO, S_QISS, S_QST, S_LAND, S_MAX, S_BAD, S_P, S_PV, S_KRD, S_W1, S_W2, S_TB, S_TA, S_N };

constexpr float KSC = 0.08838834764831845f * 1.4426950408889634f;
constexpr int SPIN_CAP = 1 << 15;
constexpr u32 QTRIES = 64;

#define STAMP(i) do { if (STAMPS == 2 && ((i) != 13 || !isA)) break; u64 t_; SB(); asm volatile("s_memrealtime %0\n\ts_waitcnt lgkmcnt(0)" : "=s"(t_) :: "memory"); SB(); if (lane == (i)) tsv = (u32)t_; } while (0)

template <u32 LDS> DI void dma1(const void* sb, u32 v) {
  asm volatile("s_mov_b32 m0, %2\n\ts_nop 0\n\tglobal_load_lds_dwordx4 %0, %1" :: "v"(v), "s"(sb), "i"(LDS) : "memory");
}
DI void dma4s(const void* sb, u32 v0, u32 v1, u32 v2, u32 v3, u32 lds) {
  asm volatile("s_mov_b32 m0, %5\n\ts_nop 0\n\tglobal_load_lds_dwordx4 %0, %4 sc1\n\tglobal_load_lds_dwordx4 %1, %4 offset:1024 sc1\n\t"
               "global_load_lds_dwordx4 %2, %4 offset:2048 sc1\n\tglobal_load_lds_dwordx4 %3, %4 offset:3072 sc1"
               :: "v"(v0), "v"(v1), "v"(v2), "v"(v3), "s"(sb), "s"(lds) : "memory");
}
template <int W> DI void dma4r(const void* sb, u32 v0, u32 v1, u32 v2, u32 v3, u32 lds) {
  asm volatile("s_mov_b32 m0, %5\n\ts_waitcnt vmcnt(%6)\n\tglobal_load_lds_dwordx4 %0, %4 nt\n\tglobal_load_lds_dwordx4 %1, %4 offset:1024 nt\n\t"
               "s_waitcnt vmcnt(%6)\n\tglobal_load_lds_dwordx4 %2, %4 offset:2048 nt\n\tglobal_load_lds_dwordx4 %3, %4 offset:3072 nt"
               :: "v"(v0), "v"(v1), "v"(v2), "v"(v3), "s"(sb), "s"(lds), "i"(W) : "memory");
}
typedef const unsigned char* cptr;
DI cptr mkptr(u32 lo, u32 hi) { return (cptr)((u64)hi << 32 | lo); }
template <int N> DI void vmwait() { asm volatile("s_waitcnt vmcnt(%0)" :: "i"(N) : "memory"); }
DI u32 pk(float a, float b) { bf16x2 v = {(__bf16)a, (__bf16)b}; return __builtin_bit_cast(u32, v); }
DI float blo(u32 x) { return __builtin_bit_cast(float, x << 16); }
DI float bhi(u32 x) { return __builtin_bit_cast(float, x & 0xffff0000u); }
template <class T> DI LAS T* lp(u32 a) { return (LAS T*)(size_t)a; }
DI v4u lds128(u32 a) { return *lp<v4u>(a); }
DI void lds128w(u32 a, v4u v) { *lp<v4u>(a) = v; }
DI void lds64w(u32 a, u32 x, u32 y) { *lp<v2u>(a) = v2u{x, y}; }
DI v4u ldstr(u32 a0, u32 a1) {
  s16x4 lo = __builtin_amdgcn_ds_read_tr16_b64_v4i16(lp<s16x4>(a0));
  s16x4 hi = __builtin_amdgcn_ds_read_tr16_b64_v4i16(lp<s16x4>(a1));
  v2u l = __builtin_bit_cast(v2u, lo), h = __builtin_bit_cast(v2u, hi);
  return v4u{l.x, l.y, h.x, h.y};
}
DI f32x4 mfma16(v4u a, v4u b, f32x4 c) { return __builtin_amdgcn_mfma_f32_16x16x32_bf16(__builtin_bit_cast(bf16x8, a), __builtin_bit_cast(bf16x8, b), c, 0, 0, 0); }
DI f32x16 mfma32(v4u a, v4u b, f32x16 c) { return __builtin_amdgcn_mfma_f32_32x32x16_bf16(__builtin_bit_cast(bf16x8, a), __builtin_bit_cast(bf16x8, b), c, 0, 0, 0); }
template <int CTRL> DI u32 dppu(u32 x) { return (u32)__builtin_amdgcn_mov_dpp((int)x, CTRL, 0xf, 0xf, true); }
DI float f_(u32 x) { return __builtin_bit_cast(float, x); }
DI u32 u_(float x) { return __builtin_bit_cast(u32, x); }
template <int CTRL> DI float dpp(float x) { return f_(dppu<CTRL>(u_(x))); }
constexpr int X1 = 0xB1, X2 = 0x4E, X7 = 0x141, X8 = 0x128;
DI float max3(float a, float b, float c) { float r; asm("v_max3_f32 %0, %1, %2, %3" : "=v"(r) : "v"(a), "v"(b), "v"(c)); return r; }
DI float sum32(float x) { auto t = __builtin_amdgcn_permlane32_swap(u_(x), u_(x), false, false); return f_(t[0]) + f_(t[1]); }
DI float max32(float x) { auto t = __builtin_amdgcn_permlane32_swap(u_(x), u_(x), false, false); return fmaxf(f_(t[0]), f_(t[1])); }
DI float sum16(float x) { auto t = __builtin_amdgcn_permlane16_swap(u_(x), u_(x), false, false); return f_(t[0]) + f_(t[1]); }
DI float max16(float x) { auto t = __builtin_amdgcn_permlane16_swap(u_(x), u_(x), false, false); return fmaxf(f_(t[0]), f_(t[1])); }
DI void sig(int word, int) {
  asm volatile("s_mov_b64 exec, 1\n\tds_add_u32 %0, %1\n\ts_mov_b64 exec, -1" :: "v"(L_SYNC + 4 * word), "v"(1u) : "memory");
}
template <bool SLEEP> DI void waitw(int word, u32 n) {
  u32 v, t, c = 1u << 20;
  asm volatile("1:\n\tds_read_b32 %0, %3\n\ts_waitcnt lgkmcnt(0)\n\tv_readfirstlane_b32 %1, %0\n\ts_cmp_ge_u32 %1, %4\n\ts_cbranch_scc1 2f\n\t"
               "s_sleep %5\n\ts_sub_u32 %2, %2, 1\n\ts_cbranch_scc0 1b\n2:"
               : "=&v"(v), "=&s"(t), "+s"(c) : "v"(L_SYNC + 4 * word), "s"(n), "i"(SLEEP ? 2 : 0) : "memory", "scc");
}
#define STUB(n) asm volatile("s_branch .Lsk" #n "\n.Lstub" #n ":\n\ts_setpc_b64 vcc\n.Lsk" #n ":")
#define TOUCH(n) asm volatile("s_call_b64 vcc, .Lstub" #n ::: "vcc")
#define BAR0(id) asm volatile("s_waitcnt lgkmcnt(0)\n\ts_barrier ; start " #id ::: "memory")
DI bool qbad(int lane) { return __any(*lp<u32>(L_EXP + (lane & 31) * 4) != *lp<u32>(L_GOT + (lane & 31) * 4)); }
DI void bar() { asm volatile("s_waitcnt lgkmcnt(0)\n\ts_barrier" ::: "memory"); }
#define LD1(off) __builtin_amdgcn_raw_buffer_load_b128(rs, (off), 0, 16)
#define ST1(v, off) __builtin_amdgcn_raw_buffer_store_b128((v), rs, (off), 0, 16)
#define TAGLOAD() __builtin_amdgcn_raw_buffer_load_b32(rs, WS_EPOCH, 0, 16)
DI v4u ldnt(const void* p, u32 off) { return __builtin_nontemporal_load((const GAS v4u*)((const GAS unsigned char*)p + off)); }
DI void stage_w(u32 base, const v4u (&park)[32], u32 g, u32 n) {
#pragma unroll
  for (int i = 0; i < 32; ++i) lds128w(base + g * 256 + i * 1024 + 16 * (n ^ ((g | (((i >> 1) & 1) << 2)) << 1)), park[i]);
}

__global__ void __launch_bounds__(512, 2)
fused_decode(const u16* __restrict__ xA, const u16* __restrict__ wqA, const u16* __restrict__ xB, const u16* __restrict__ wqB,
             unsigned char* ws_, const u16* __restrict__ ckv, const u16* __restrict__ wuk, const u16* __restrict__ kc,
             const u16* __restrict__ vc, const u16* __restrict__ wuv, const u16* __restrict__ woA, const u16* __restrict__ woB,
             u16* __restrict__ out) {
  typedef u32 s8u __attribute__((ext_vector_type(8)));
  typedef u32 s16u __attribute__((ext_vector_type(16)));
  const int tid = threadIdx.x, lane = tid & 63, wave = __builtin_amdgcn_readfirstlane(tid >> 6);
  s8u ka;
  s16u kb;
  if (wave == 0) asm volatile("s_load_dwordx8 %0, %2, 0x0\n\ts_load_dwordx16 %1, %2, 0x20" : "=&s"(ka), "=&s"(kb) : "s"(__builtin_amdgcn_kernarg_segment_ptr()));
  u64 pc0; asm volatile("s_getpc_b64 %0" : "=s"(pc0));
  u32 tsv = 0;
  { u64 t_; asm volatile("s_memrealtime %0" : "=s"(t_)); asm volatile("s_waitcnt lgkmcnt(0)" : "+s"(t_) :: "memory"); if (lane == 0) tsv = (u32)t_; }
  const u32 gid = blockIdx.x, x = gid & 7, r = gid >> 3;
  const bool isA = (r & 4) == 0;
  const u32 l16 = (r & 3) | ((r >> 3) << 2);
  const u32 ht = 4 * x + (l16 >> 2), q4 = l16 & 3;
  const u32 g = lane >> 4, n = lane & 15, tq = n >> 2, tp = lane & 3;
  u32 warm = 0, ce = 0, cd = 0;
  u64 t0 = 0;
  unsigned char* ws;
  if (wave == 0) {
    const u32 vo = q4 * 1048576 + (((l16 >> 2) + 1) & 3) * 256 + x * 1024 + g * 8192;
    ce = vo + ((n ^ (g << 1)) * 16); cd = vo + ((n ^ ((g | 4) << 1)) * 16) + 63488;
    asm volatile("s_waitcnt lgkmcnt(0)" : "+s"(ka), "+s"(kb));
    STAMP(12);
    asm volatile("s_memrealtime %0" : "=s"(t0));
    if (THOLD > 0 && isA) {
      asm volatile("s_waitcnt lgkmcnt(0)" : "+s"(t0));
      while (__builtin_amdgcn_s_memrealtime() - t0 < THOLD) { }
    }
    dma1<L_X>(isA ? mkptr(ka[4], ka[5]) : mkptr(ka[0], ka[1]), (g & 1) * 1024 + q4 * 256 + n * 16);
    cptr wq = isA ? mkptr(ka[6], ka[7]) : mkptr(ka[2], ka[3]);
#pragma unroll 1
    for (u32 G = 0; G < (WQMIX && isA ? 0u : 8u); ++G) dma4r<63>(wq + G * 131072, ce, ce + 31744, cd, cd + 31744, L_WQ + G * 4096);
    if (lane < 2) *lp<u32>(L_PTR + 4 * lane) = lane ? kb[1] : kb[0];
    BAR0(0);
    asm volatile("s_waitcnt lgkmcnt(0)" : "+s"(t0));
    STAMP(15);
  } else if (wave == 1) {
    const u32 ln = (r & 3) * 64 + lane;
    warm = ((const GAS u32*)(pc0 & ~127ull))[(ln < CODE_BYTES / 128 ? ln : 0u) * 32];
    SB();
  } else if (wave == 2) {
    if (lane < S_N) *lp<u32>(L_SYNC + 4 * lane) = 0;
  } else if (wave == 3) {
    s16u kc3;
    if (PAGES) asm volatile("s_load_dwordx16 %0, %1, 0x20" : "=s"(kc3) : "s"(__builtin_amdgcn_kernarg_segment_ptr()));
    if (isA) TOUCH(1); else TOUCH(2);
    if (PAGES) {
      asm volatile("s_waitcnt lgkmcnt(0)" : "+s"(kc3));
      const size_t kvb = (((size_t)(l16 >> 3) * 8 + x) * 8192 + 6144 + (l16 & 7) * 256) * 256;
      cptr p0 = isA ? mkptr(kc3[2], kc3[3]) + (size_t)(x * 16 + l16) * 131072 : mkptr(kc3[6], kc3[7]) + kvb;
      cptr p1 = isA ? mkptr(kc3[10], kc3[11]) + q4 * 1048576 + ht * 256 : mkptr(kc3[8], kc3[9]) + kvb;
      cptr p2 = isA ? p1 + 1040384 : mkptr(kc3[12], kc3[13]) + ht * 131072 + q4 * 256;
      cptr p3 = isA ? p0 + 130944 : mkptr(kc3[14], kc3[15]) + ht * 131072 + q4 * 256;
      cptr pp = (lane & 2) ? ((lane & 1) ? p3 : p2) : ((lane & 1) ? p1 : p0);
      if (KEYMIX && isA && (lane & 3) == 0) pp = mkptr(kc3[2], kc3[3]) + (size_t)(x >> 2) * 8388608 + ((x & 3) * 16 + l16) * 16384 + ((lane >> 2) & 7) * 1048576;
      warm = __builtin_nontemporal_load((const GAS u32*)pp);
      SB();
    }
  }
  __amdgpu_buffer_rsrc_t rs;
#define SETWS(lo, hi) do { ws = (unsigned char*)mkptr(lo, hi); rs = __builtin_amdgcn_make_buffer_rsrc((void*)ws, 0, WS_BYTES, 0x00020000); } while (0)
#define START(id) do { BAR0(id); const v2u p_ = *lp<v2u>(L_PTR); SETWS(__builtin_amdgcn_readfirstlane(p_.x), __builtin_amdgcn_readfirstlane(p_.y)); } while (0)
  u32 tag;
#define SIG_AT(ISSUED, W, NEED, WORD) if constexpr ((ISSUED) - (W) >= (NEED) && (ISSUED) - 4 - (W) < (NEED)) { vmwait<W>(); sig(WORD, lane); }
  const u32 w = wave & 3;
  const u32 ab = (8 * g + tq) * 256 + 16 * ((4 * w + (tp >> 1)) ^ ((tq | ((g & 1) << 2)) << 1)) + 8 * (tp & 1);

#define PROJ(BEFORE_STORE, DST) do { \
    v4u xf[4], af[8]; \
    _Pragma("unroll") for (int s = 0; s < 4; ++s) xf[s] = lds128(L_X + (lane & 1) * 256 + g * 16 + s * 64); \
    _Pragma("unroll") for (int s = 0; s < 8; ++s) { const u32 a = L_WQ + (ab ^ (32 * (s & 1))) + (s >> 1) * 8192; af[s] = ldstr(a, a + 1024); } \
    SB(); \
    f32x4 c0 = {0.f, 0.f, 0.f, 0.f}, c1 = c0; \
    _Pragma("unroll") for (int s = 0; s < 4; ++s) { c0 = mfma16(af[2 * s], xf[s], c0); c1 = mfma16(af[2 * s + 1], xf[s], c1); } \
    const f32x4 c = (n & 4) ? c1 : c0; \
    BEFORE_STORE; \
    if (n < 8) ST1((v4u{u_((n & 2) ? c[2] : c[0]), tag, u_((n & 2) ? c[3] : c[1]), tag}), (DST) + (16 * w + 8 * (n >> 2) + 2 * g + ((n >> 1) & 1)) * 16); \
  } while (0)

  if (isA) {
    if (wave == 0) {
      STUB(1);
      SETWS(kb[0], kb[1]);
      __builtin_amdgcn_s_setprio(1);
      cptr src = mkptr(kb[2], kb[3]) + (KEYMIX ? (size_t)(x >> 2) * 8388608 + ((x & 3) * 16 + l16) * 16384 : (size_t)(x * 16 + l16) * 131072);
      constexpr u32 HS = KEYMIX ? 1048576 : 16384;
      u32 cv[16];
#pragma unroll
      for (int i = 0; i < 16; ++i) cv[i] = (lane ^ (((i & 3) << 2) | (i >> 2))) * 16;
#pragma unroll 1
      for (u32 H = 0; H < 8; ++H) {
        if (H == 6) { waitw<false>(S_XRD, 6); sig(S_CISS, lane); }
#pragma unroll
        for (int G = 0; G < 4; ++G) {
          dma4r<WINA>(src + H * HS + G * 4096, cv[4 * G], cv[4 * G + 1], cv[4 * G + 2], cv[4 * G + 3], L_C + H * 16384 + G * 4096);
          if (WQMIX && G < 3 && 3 * H + G < 8) dma4r<WINA>(mkptr(ka[6], ka[7]) + (3 * H + G) * 131072, ce, ce + 31744, cd, cd + 31744, L_WQ + (3 * H + G) * 4096);
          constexpr int WQAT = (WQMIX ? 9 : -1) + WINA / 4;
          if (G == (WQAT & 3) && H == WQAT / 4) { vmwait<WINA>(); sig(S_WQ, lane); STAMP(6); }
        }
      }
      sig(S_CISS, lane);
      STAMP(1);
      vmwait<0>();
      STAMP(2);
      bar();
      v4u park[32];
      const u32 vo = (128 * q4 + g) * 8192 + ht * 256 + n * 16;
#pragma unroll
      for (int i = 0; i < 32; ++i) park[i] = ldnt(mkptr(kb[10], kb[11]), vo + i * 32768);
      const u32 tv = TAGLOAD();
      SB();
      STAMP(3);
      for (u32 it = 1; it <= QTRIES; ++it) {
        bar(); bar();
        if (!qbad(lane) || it == QTRIES) break;
        bar(); bar();
      }
      bar(); bar();
      stage_w(L_W1, park, g, n);
      tag = __builtin_amdgcn_readfirstlane(tv) + 1;
      STAMP(4);
    } else if (wave < 4) {
      START(1);
      if (wave == 1) {
        asm volatile("" :: "v"(warm));
        TOUCH(11); TOUCH(12); TOUCH(13); TOUCH(14); TOUCH(15); TOUCH(16); TOUCH(17); TOUCH(18); TOUCH(19);
        if (lane < 6) { u32 t = __builtin_amdgcn_raw_buffer_load_b32(rs, lane < 2 ? lane * 1048576 + 262144 - 64 : WS_APART + (lane - 1) * APART_BLK - 128, 0, 16); asm volatile("" :: "v"(t)); }
      }
      if (wave != 2) sig(S_XRD, lane);
      asm volatile("" :: "v"(warm));
      tag = __builtin_amdgcn_readfirstlane(TAGLOAD()) + 1;
      v4u h0, h1;
      cptr qs = ws + WS_QABS + (x >> 2) * 32768;
#define Q_ISSUE() _Pragma("unroll 1") for (u32 G = 0; G < 8; ++G) { const u32 l0 = (lane ^ (G & 3)) * 16; \
          dma4s(qs + G * 4096, l0, l0 ^ 64, l0 ^ 128, l0 ^ 192, L_Q + G * 4096); }
#define HINT_LD(p, q) do { p = LD1(WS_HINT + lane * 128); q = LD1(WS_HINT + 8192 + lane * 128); asm volatile("" ::: "memory"); } while (0)
      if (wave == 2) {
        __builtin_amdgcn_s_setprio(2);
        waitw<true>(S_CISS, 1);
#pragma unroll 1
        for (int spin = 0; spin < 64; ++spin) {
          const u32 gt = __builtin_amdgcn_raw_buffer_load_b32(rs, WS_GATE + (lane & 7) * 128, 0, 16);
          asm volatile("" ::: "memory");
          const bool ok = __all(gt == tag);
          if (spin == 0) waitw<false>(S_CISS, 2);
          if (ok) break;
        }
        STAMP(3);
        if (OPTQ) {
          asm volatile("global_load_dwordx4 %0, %2, %4 sc1\n\tglobal_load_dwordx4 %1, %3, %4 sc1"
                       : "=&v"(h0), "=&v"(h1) : "v"(WS_HINT + lane * 128), "v"(WS_HINT + 8192 + lane * 128), "s"(ws) : "memory");
          Q_ISSUE()
          asm volatile("s_waitcnt vmcnt(32)" : "+v"(h0), "+v"(h1) :: "memory");
        } else HINT_LD(h0, h1);
        if (!OPTQ || !__all(h0.z == tag && h1.z == tag)) {
#pragma unroll 1
          for (int spin = 0; spin < 64; ++spin) {
            if (__all(h0.z == tag && h1.z == tag)) break;
            HINT_LD(h0, h1);
          }
          Q_ISSUE()
        }
        STAMP(1);
        vmwait<0>();
      }
      bar();
      for (u32 it = 1; it <= QTRIES; ++it) {
        bar();
        if (wave == 2) {
          u32 e0 = (x & 4 ? h0.y : h0.x) - tag, e1 = (x & 4 ? h1.y : h1.x) - tag;
          e0 += dppu<X1>(e0); e1 += dppu<X1>(e1); e0 += dppu<X2>(e0); e1 += dppu<X2>(e1);
          if (tp < 2) *lp<u32>(L_EXP + (lane >> 2) * 4 + tp * 64) = tp ? e1 : e0;
        }
        bar();
        if (!qbad(lane) || it == QTRIES) break;
        bar();
        if (wave == 2) { HINT_LD(h0, h1); Q_ISSUE() vmwait<0>(); }
        bar();
      }
      bar(); bar();
      if (wave == 1) *lp<u32>(L_BAD) = 0;
    } else {
      START(2);
      tag = __builtin_amdgcn_readfirstlane(TAGLOAD()) + 1;
      const u32 bs = x * 16 + l16, b = bs >> 6, hh = lane >> 5, hd = lane & 31;
      __builtin_amdgcn_s_setprio(2);
      waitw<false>(S_WQ, 1);
      PROJ(sig(S_XRD, lane), WS_QB + x * 32768 + (n & 1) * 16384 + (q4 * 4 + (((l16 >> 2) + 1) & 3)) * 1024);
      STAMP(13);
      STUB(11);
      f32x16 s;
      v4u mm;
      for (u32 it = 1; it <= QTRIES; ++it) {
        u32 cs = 0;
        bar();
        STAMP(3);
#pragma unroll
        for (int i = 0; i < 16; ++i) s[i] = 0.f;
        {
          const u32 fr = ((hd & 3) << 2) | ((hd >> 2) & 3);
          const u32 qa = L_Q + hd * 1024 + 16 * (hh ^ fr), ca = qa - L_Q + L_C + w * 32768;
          v4u af[32], bf[32];
          constexpr int PD = 6;
#define S_RD(k) do { bf[k] = lds128((qa ^ (32 * ((k) & 7))) + 256 * ((k) >> 3)); af[k] = lds128((ca ^ (32 * ((k) & 7))) + 256 * ((k) >> 3)); } while (0)
#pragma unroll
          for (int k = 0; k < PD; ++k) S_RD(k);
          SB();
#pragma unroll
          for (int k = 0; k < 32; ++k) {
            if (k + PD < 32) S_RD(k + PD);
            s = mfma32(af[k], bf[k], s);
            cs = cs + bf[k].x + bf[k].y; cs = cs + bf[k].z + bf[k].w;
            if (k == 4) STUB(12);
            if (k == 26) { STUB(13); }
            if (k == 29) bar();
            SB();
          }
        }
        { auto t = __builtin_amdgcn_permlane32_swap(cs, cs, false, false); cs = t[0] + t[1]; }
        asm volatile("s_nop 11" : "+v"(s));
        float m = max3(max3(max3(s[0], s[1], s[2]), max3(s[3], s[4], s[5]), max3(s[6], s[7], s[8])),
                       max3(max3(s[9], s[10], s[11]), max3(s[12], s[13], s[14]), s[15]), s[15]);
        m = max32(m);
        if (hh == 0) *lp<float>(L_MAXX + hd * 16 + w * 4) = m;
        if (w == 0 && hh == 0) *lp<u32>(L_GOT + hd * 4) = cs;
        STAMP(4);
        bar();
        mm = lds128(L_MAXX + hd * 16);
        if (!qbad(lane) || it == QTRIES) break;
        bar();
        if (lane == 15) tsv += 1;
      }
      const float mk = fmaxf(fmaxf(f_(mm.x), f_(mm.y)), fmaxf(f_(mm.z), f_(mm.w))) * KSC;
      float ls = 0.f;
#pragma unroll
      for (int i = 0; i < 16; ++i) { s[i] = __builtin_amdgcn_exp2f(fmaf(s[i], KSC, -mk)); ls += s[i]; }
      lds128w(L_P + (2 * w) * 1024 + lane * 16, v4u{pk(s[0], s[1]), pk(s[2], s[3]), pk(s[4], s[5]), pk(s[6], s[7])});
      lds128w(L_P + (2 * w + 1) * 1024 + lane * 16, v4u{pk(s[8], s[9]), pk(s[10], s[11]), pk(s[12], s[13]), pk(s[14], s[15])});
      ls = sum32(ls);
      if (hh == 0) *lp<float>(L_LSUM + hd * 16 + w * 4) = ls;
      const u32 blk = (lane >> 4) & 1;
      u32 ta[2];
#pragma unroll
      for (int t = 0; t < 2; ++t)
        ta[t] = L_C + (4 * hh + 8 * t + tq) * 1024 + 256 * w + 16 * ((2 * blk + (tp >> 1)) ^ ((tq << 2) | (hh + 2 * t))) + 8 * (tp & 1);
      v4u cf[32];
      constexpr int PV_PD = 5;
#define PV_RD(kk) cf[kk] = ldstr((ta[0] ^ (64 * ((kk) >> 3))) + ((kk) & 7) * 16384, (ta[1] ^ (64 * ((kk) >> 3))) + ((kk) & 7) * 16384)
#pragma unroll
      for (int k = 0; k < PV_PD; ++k) PV_RD(k);
      SB();
      STUB(14);
      bar();
      STAMP(5);
      v4u pf[8];
#pragma unroll
      for (int k = 0; k < 8; ++k) pf[k] = lds128(L_P + k * 1024 + lane * 16);
      const v4u lv = lds128(L_LSUM + hd * 16);
      SB();
      const u32 scw = L_SCR + w * 4096 + hd * 128 + 8 * hh + 16 * (hd & 7);
      const u32 scr = L_SCR + w * 4096 + (lane >> 3) * 128 + 16 * ((lane & 7) ^ (lane >> 3));
      const u32 sto = WS_APART + (((lane >> 3) * 4 + w) * 128 + bs) * 256 + 16 * (lane & 7);
      f32x16 o[4];
      v4u ov[4];
      u32 cs = tag;
#define EPI_W(c, gg) do { \
        const u32 p0 = pk(o[c][4 * (gg)], o[c][4 * (gg) + 1]), p1 = pk(o[c][4 * (gg) + 2], o[c][4 * (gg) + 3]); \
        lds64w(scw ^ (64 * ((c) & 1) + 16 * (gg)), p0, p1); cs += p0 + p1; asm volatile("" : "+v"(cs)); } while (0)
#define EPI_R() _Pragma("unroll") for (int i = 0; i < 4; ++i) ov[i] = lds128(scr + i * 1024);
#define EPI_S(p, i) ST1(ov[i], sto + 128 * (p) + (i) * APART_BLK)
#pragma unroll
      for (int kk = 0; kk < 32; ++kk) {
        const int c = kk >> 3, k = kk & 7;
        if (kk + PV_PD < 32) PV_RD(kk + PV_PD);
        if (k == 0) {
          f32x16 z;
#pragma unroll
          for (int i = 0; i < 16; ++i) z[i] = 0.f;
          o[c] = mfma32(cf[kk], pf[k], z);
        } else o[c] = mfma32(cf[kk], pf[k], o[c]);
        if (c > 0 && c != 2 && (k & 1) == 1) EPI_W(c > 0 ? c - 1 : 0, (k >> 1) & 3);
        if (c == 2 && k >= 1 && k <= 4) EPI_W(1, (k - 1) & 3);
        if (kk == 20) { EPI_R() }
        if (kk == 23) EPI_S(0, 0);
        if (kk == 24) EPI_S(0, 1);
        if (kk == 26) EPI_S(0, 2);
        if (kk == 28) EPI_S(0, 3);
        if (kk == 13) STUB(15);
        if (kk == 22) STUB(16);
        SB();
      }
      EPI_W(3, 0); EPI_W(3, 1); EPI_W(3, 2); EPI_W(3, 3); EPI_R()
      const float lsum = (f_(lv.x) + f_(lv.y)) + (f_(lv.z) + f_(lv.w));
      { auto t = __builtin_amdgcn_permlane32_swap(cs, cs, false, false); cs = t[0] + t[1] - tag + u_(mk) + u_(lsum); }
      EPI_S(1, 0); EPI_S(1, 1); EPI_S(1, 2); EPI_S(1, 3);
      if (hh == 0) ST1((v4u{u_(mk), u_(lsum), cs, tag}), WS_ASTAT + bs * 2048 + w * 512 + hd * 16);
      STAMP(7);
      bar();
    }
    const u32 v = wave;
    v4u a1[8], bq[4];
    STUB(17);
    const u64 tpv = __builtin_amdgcn_s_memrealtime();
    bar();
    if (wave >= 4) {
#pragma unroll
      for (int s = 0; s < 8; ++s) { const u32 a = L_W1 + (ab ^ (32 * (s >> 2))) + (s & 3) * 8192; a1[s] = ldstr(a, a + 1024); }
    }
    if (wave == 3) {
      while (__builtin_amdgcn_s_memrealtime() - tpv < PDELAY) { }
    }
    for (u32 it = 257; it < 320; ++it) {
      v4u st0, st1;
      if (wave == 3) {
        for (int spin = 0; spin < SPIN_CAP; ++spin) {
          st0 = LD1(WS_ASTAT + lane * 2048 + q4 * 512 + ht * 16);
          st1 = LD1(WS_ASTAT + 131072 + lane * 2048 + q4 * 512 + ht * 16);
          asm volatile("" ::: "memory");
          if (__all(st0.w == tag && st1.w == tag)) break;
        }
        STAMP(2);
      }
      if (SOON && wave == 3 && q4 == 0 && lane == 0) __builtin_amdgcn_raw_buffer_store_b32(tag, rs, WS_SOON + ht * 128, 0, 16);
      bar();
      STAMP(8);
      v4u pd[4];
#pragma unroll
      for (int i = 0; i < 4; ++i) pd[i] = LD1(WS_APART + (ht >> 3) * APART_BLK + ((ht & 7) * 4 + q4) * 32768 + v * 4096 + i * 1024 + lane * 16);
      SB();
      if (wave == 3) {
#pragma unroll
        for (int i = 0; i < 2; ++i) {
          const v4u st = i ? st1 : st0;
          float M = f_(st.x);
          M = fmaxf(M, dpp<X1>(M)); M = fmaxf(M, dpp<X2>(M)); M = fmaxf(M, dpp<X7>(M)); M = fmaxf(M, dpp<X8>(M)); M = max16(M); M = max32(M);
          const float e = __builtin_amdgcn_exp2f(f_(st.x) - M);
          float S = e * f_(st.y);
          S += dpp<X1>(S); S += dpp<X2>(S); S += dpp<X7>(S); S += dpp<X8>(S); S = sum16(S); S = sum32(S);
          *lp<float>(L_WGT + i * 256 + lane * 4) = e / S;
          *lp<u32>(L_CK + i * 256 + lane * 4) = st.z - tag - st.x - st.y;
        }
      }
      STUB(18);
      bar();
      float acc[8], wt[4];
#pragma unroll
      for (int i = 0; i < 4; ++i) wt[i] = *lp<float>(L_WGT + (v * 16 + 4 * i + g) * 4);
      const u32 ck = *lp<u32>(L_CK + (v * 16 + 4 * (lane & 3) + g) * 4);
#pragma unroll
      for (int k = 0; k < 4; ++k) { acc[2 * k] = wt[0] * blo(pd[0][k]); acc[2 * k + 1] = wt[0] * bhi(pd[0][k]); }
#pragma unroll
      for (int i = 1; i < 4; ++i)
#pragma unroll
        for (int k = 0; k < 4; ++k) { acc[2 * k] = fmaf(wt[i], blo(pd[i][k]), acc[2 * k]); acc[2 * k + 1] = fmaf(wt[i], bhi(pd[i][k]), acc[2 * k + 1]); }
      STAMP(9);
      {
        float y[4];
#pragma unroll
        for (int k = 0; k < 4; ++k) { auto t = __builtin_amdgcn_permlane32_swap(u_(acc[k]), u_(acc[k + 4]), false, false); y[k] = f_(t[0]) + f_(t[1]); }
        float z[2];
#pragma unroll
        for (int k = 0; k < 2; ++k) { auto t = __builtin_amdgcn_permlane16_swap(u_(y[k]), u_(y[k + 2]), false, false); z[k] = f_(t[0]) + f_(t[1]); }
        const u32 hi = pk(z[0], z[1]), lo = pk(z[0] - blo(hi), z[1] - bhi(hi));
        *lp<u32>(L_B1 + v * 272 + (8 * n + 2 * g) * 2) = hi;
        *lp<u32>(L_B1 + (8 + v) * 272 + (8 * n + 2 * g) * 2) = lo;
        u32 c4[4], c2[2];
#pragma unroll
        for (int i = 0; i < 4; ++i) c4[i] = (pd[i].x + pd[i].y) + (pd[i].z + pd[i].w);
#pragma unroll
        for (int i = 0; i < 2; ++i) { const u32 p = c4[i] + dppu<X2>(c4[i]), q = c4[i + 2] + dppu<X2>(c4[i + 2]); c2[i] = (lane & 2) ? q : p; }
        const u32 p1 = c2[0] + dppu<X1>(c2[0]), q1 = c2[1] + dppu<X1>(c2[1]);
        u32 c1 = (lane & 1) ? q1 : p1;
        c1 += dppu<X8>(c1);
        c1 += dppu<0x124>(c1);
        if (c1 != ck) *lp<u32>(L_BAD) = it;
      }
      STUB(19);
      bar();
      const u32 bad = *lp<u32>(L_BAD);
#pragma unroll
      for (int s = 0; s < 4; ++s) bq[s] = lds128(L_B1 + n * 272 + g * 16 + s * 64);
      SB();
      if (__builtin_amdgcn_readfirstlane(bad) != it) break;
      if (lane == 15) tsv += 100;
    }
    if (wave >= 4) {
      f32x4 d0 = {0.f, 0.f, 0.f, 0.f}, d1 = d0;
#pragma unroll
      for (int s = 0; s < 4; ++s) { d0 = mfma16(a1[s], bq[s], d0); d1 = mfma16(a1[4 + s], bq[s], d1); }
#pragma unroll
      for (int k = 0; k < 4; ++k) {
        const float t = d0[k] + dpp<X1>(d0[k]), u = d1[k] + dpp<X1>(d1[k]);
        float z = (n & 1) ? u : t;
        z += dpp<X2>(z); z += dpp<X8>(z); d0[k] = z;
      }
      if ((n & 10) == 0) ST1((v4u{pk(d0[0], d0[1]), tag, pk(d0[2], d0[3]), tag}), WS_H3B + ht * 4096 + q4 * 1024 + (n >> 2) * 512 + (8 * w + 4 * (n & 1) + g) * 16);
    }
    STAMP(10);
    if (wave == 0) {
      const u32 rho = x * 8 + (l16 >> 1), part = l16 & 1;
      u16* outp = out; asm volatile("" : "+s"(outp));
      { const u64 t0 = __builtin_amdgcn_s_memrealtime(); while (__builtin_amdgcn_s_memrealtime() - t0 < (part ? 0 : FDELAY)) { } }
      v4u e[4];
      for (int spin = 0; spin < SPIN_CAP; ++spin) {
        bool ok = true;
#pragma unroll
        for (int i = 0; i < 4; ++i) e[i] = LD1(WS_H4 + part * 262144 + rho * 4096 + i * 1024 + lane * 16);
        asm volatile("" ::: "memory");
#pragma unroll
        for (int i = 0; i < 4; ++i) ok = ok && e[i].y == tag && e[i].w == tag;
        if (__all(ok)) break;
      }
      float s0 = (f_(e[0].x) + f_(e[1].x)) + (f_(e[2].x) + f_(e[3].x)), s1 = (f_(e[0].z) + f_(e[1].z)) + (f_(e[2].z) + f_(e[3].z));
      s0 += dpp<X8>(s0); s1 += dpp<X8>(s1);
      s0 = sum32(sum16(s0)); s1 = sum32(sum16(s1));
      if (lane < 8) *(GAS u32*)(outp + (rho & 1) * 1024 + part * 512 + (rho >> 1) * 16 + 2 * lane) = pk(s0, s1);
      if (gid == 0 && lane == 0) __builtin_amdgcn_raw_buffer_store_b32(tag, rs, WS_EPOCH, 0, 16);
      STAMP(13);
    }
  } else {
    if (wave < 4) {
      if (wave == 0) {
        STUB(2);
        SETWS(kb[0], kb[1]);
        __builtin_amdgcn_s_setprio(1);
        {
          const u32 uo = q4 * 1048576 + ht * 256 + g * 8192;
          u32 uv[4];
#pragma unroll
          for (int i = 0; i < 4; ++i) uv[i] = uo + ((n ^ (g | (i << 2))) * 16) + (i & 1) * 63488 + (i >> 1) * 131072;
          cptr uk = mkptr(kb[4], kb[5]);
#pragma unroll 1
          for (u32 H = 0; H < 4; ++H) {
            dma4r<32>(uk + H * 262144, uv[0], uv[0] + 31744, uv[1], uv[1] + 31744, L_UK + H * 8192);
            dma4r<32>(uk + H * 262144, uv[2], uv[2] + 31744, uv[3], uv[3] + 31744, L_UK + H * 8192 + 4096);
          }
        }
        vmwait<32>(); sig(S_WQ, lane);
        STAMP(14);
        vmwait<0>(); sig(S_UK, lane);
        STAMP(5);
        tag = __builtin_amdgcn_readfirstlane(TAGLOAD()) + 1;
        sig(S_TAG, lane);
        STUB(3);
        waitw<false>(S_PUB, 5);
        while (__builtin_amdgcn_s_memrealtime() - t0 < KSTART) __builtin_amdgcn_s_sleep(1);
        STAMP(13);
        const size_t base = (((size_t)(l16 >> 3) * 8 + x) * 8192 + 6144 + (l16 & 7) * (KVMIX ? 16 : 256)) * 256;
        constexpr u32 GS = KVMIX ? 32768 : 4096;
        cptr ks = mkptr(kb[6], kb[7]) + base, vs = mkptr(kb[8], kb[9]) + base;
        static_assert(WINB % 4 == 0 && NB0 <= 3 + WINB / 4, "block signals");
        {
          u32 kv[4];
#pragma unroll
          for (int i = 0; i < 4; ++i) kv[i] = g * 256 + ((n ^ (4 * i + g)) * 16);
#pragma unroll 1
          for (u32 G = 0; G < NB0; ++G) dma4r<WINB0>(ks + G * GS, kv[0], kv[1], kv[2], kv[3], L_K + G * 4096);
#pragma unroll 1
          for (u32 G = NB0; G < 16; ++G) {
            dma4r<WINB>(ks + G * GS, kv[0], kv[1], kv[2], kv[3], L_K + G * 4096);
            if (((G + 1 - WINB / 4) & 3) == 0 && G >= 3 + WINB / 4) { vmwait<WINB>(); sig(S_LAND, lane); }
          }
        }
        STAMP(1);
        {
          const u32 v0 = g * 256 + ((n ^ (g << 1)) * 16), v1 = g * 256 + ((n ^ ((4 + g) << 1)) * 16);
#pragma unroll 1
          for (u32 G = 0; G < 16; ++G) {
            if (ABL != 3) dma4r<WINB>(vs + G * GS, v0, v1, v0, v1, L_V + G * 4096);
            if (((G + 1 - WINB / 4) & 3) == 0 && G + 16 >= 3 + WINB / 4) { vmwait<WINB>(); sig(S_LAND, lane); }
          }
        }
        STAMP(2);
        waitw<false>(S_KRD, 4);
        {
          const u32 vo = (ht * 128 + g) * 1024 + q4 * 256, w0 = vo + ((n ^ (g << 1)) * 16), w1 = vo + ((n ^ ((g | 4) << 1)) * 16) + 6144;
#pragma unroll 1
          for (u32 G = 0; G < 16; ++G) {
            cptr wp = (G < 8 ? mkptr(kb[14], kb[15]) : mkptr(kb[12], kb[13])) + (G & 7) * 16384;
            if (ABL != 3) dma4r<WINB>(wp, w0, w0 + 3072, w1, w1 + 3072, L_W1 + G * 4096);
            if (((G + 1 - WINB / 4) & 3) == 0) { vmwait<WINB>(); sig(S_LAND, lane); }
          }
        }
        STAMP(3);
        vmwait<32>(); sig(S_W1, lane);
        vmwait<0>(); sig(S_W2, lane);
        STAMP(4);
      } else if (wave == 1) {
        START(3);
        const u64 t1 = __builtin_amdgcn_s_memrealtime();
        __builtin_amdgcn_s_setprio(2);
        asm volatile("" :: "v"(warm));
        TOUCH(4); TOUCH(3); TOUCH(5); TOUCH(6); TOUCH(7); TOUCH(8); TOUCH(9); TOUCH(10);
        if (lane < 6) { u32 t = __builtin_amdgcn_raw_buffer_load_b32(rs, lane < 2 ? lane * 1048576 + 262144 - 64 : WS_APART + (lane - 1) * APART_BLK - 128, 0, 16); asm volatile("" :: "v"(t)); }
        tag = __builtin_amdgcn_readfirstlane(TAGLOAD()) + 1;
        waitw<false>(S_TAG, 3);
        waitw<false>(S_PUB, 4);
        {
          const v4u pv = lds128(L_QX + (lane & 31) * 16);
          if (lane < 32) ST1(pv, WS_QABS + (lane >> 4) * 32768 + ht * 1024 + q4 * 256 + n * 16);
          u32 cs = (pv.x + pv.y) + (pv.z + pv.w);
          cs += dppu<X1>(cs); cs += dppu<X2>(cs); cs += dppu<X7>(cs); cs += dppu<X8>(cs);
          auto t = __builtin_amdgcn_permlane16_swap(cs, cs, false, false);
          if (lane == 0) ST1((v4u{t[0] + tag, t[1] + tag, tag, tag}), WS_HINT + (x * 16 + l16) * 128);
          if (l16 == GATEL && lane == 0) __builtin_amdgcn_raw_buffer_store_b32(tag, rs, WS_GATE + x * 128, 0, 16);
        }
        sig(S_PUB, lane);
        STAMP(2);
        while (__builtin_amdgcn_s_memrealtime() - t1 < QBSTART) __builtin_amdgcn_s_sleep(1);
        v4u e[16];
        for (int spin = 0; spin < SPIN_CAP; ++spin) {
          bool ok = true;
#pragma unroll
          for (int i = 0; i < 16; ++i) e[i] = LD1(WS_QB + x * 32768 + (l16 >> 3) * 16384 + i * 1024 + lane * 16);
          asm volatile("" ::: "memory");
#pragma unroll
          for (int i = 0; i < 16; ++i) ok = ok && e[i].y == tag && e[i].w == tag;
          if (__all(ok)) break;
        }
#pragma unroll
        for (int i = 0; i < 4; ++i)
          *lp<u32>(L_QBL + i * 256 + lane * 4) = pk((f_(e[i].x) + f_(e[4 + i].x)) + (f_(e[8 + i].x) + f_(e[12 + i].x)), (f_(e[i].z) + f_(e[4 + i].z)) + (f_(e[8 + i].z) + f_(e[12 + i].z)));
        sig(S_QBRDY, lane);
        STAMP(3);
      } else {
        START(4);
        asm volatile("" :: "v"(warm));
        tag = __builtin_amdgcn_readfirstlane(TAGLOAD()) + 1;
        sig(S_TAG, lane);
      }
      STUB(10);
      waitw<true>(S_W2, 1);
      v4u a2[8];
#pragma unroll
      for (int s = 0; s < 8; ++s) { const u32 a = L_W2 + (ab ^ (32 * (s >> 2))) + (s & 3) * 8192; a2[s] = ldstr(a, a + 1024); }
      if (SOON) {
        if (w == 2) {
          for (int spin = 0; spin < SPIN_CAP; ++spin) {
            const u32 f = __builtin_amdgcn_raw_buffer_load_b32(rs, WS_SOON + ht * 128, 0, 16);
            asm volatile("" ::: "memory");
            if (__builtin_amdgcn_readfirstlane(f) == tag) break;
          }
          const u64 ts = __builtin_amdgcn_s_memrealtime();
          while (__builtin_amdgcn_s_memrealtime() - ts < SOON) { }
          sig(S_QST, lane);
        }
        waitw<false>(S_QST, 1);
      }
      v4u e;
      for (int spin = 0; spin < SPIN_CAP; ++spin) {
        e = LD1(WS_H3B + ht * 4096 + w * 1024 + lane * 16);
        asm volatile("" ::: "memory");
        if (__all(e.y == tag && e.w == tag)) break;
      }
      lds64w(L_B2 + ((lane >> 5) * 4 + w) * 272 + (lane & 31) * 8, e.x, e.z);
      sig(S_TA, lane);
      waitw<false>(S_TA, 4);
      STAMP(11);
      v4u bq[4];
#pragma unroll
      for (int s = 0; s < 4; ++s) bq[s] = lds128(L_B2 + (n & 7) * 272 + g * 16 + s * 64);
      SB();
      f32x4 d0 = {0.f, 0.f, 0.f, 0.f}, d1 = d0;
#pragma unroll
      for (int s = 0; s < 4; ++s) { d0 = mfma16(a2[s], bq[s], d0); d1 = mfma16(a2[4 + s], bq[s], d1); }
      f32x4 d = (n & 8) ? d1 : d0;
#pragma unroll
      for (int k = 0; k < 4; ++k) { float t = d[k]; t += dpp<X1>(t); t += dpp<X2>(t); d[k] = t; }
      const bool k1 = (n & 1) != 0;
      if ((n & 2) == 0) ST1((v4u{u_(k1 ? d[2] : d[0]), tag, u_(k1 ? d[3] : d[1]), tag}),
                            WS_H4 + (((q4 * 8 + 2 * w + (n >> 3)) * 2 + ((n >> 2) & 1)) * 32 + ht) * 128 + g * 32 + (n & 1) * 16);
      STAMP(12);
    } else {
      START(5);
      tag = __builtin_amdgcn_readfirstlane(TAGLOAD()) + 1;
      __builtin_amdgcn_s_setprio(2);
      {
        waitw<false>(S_WQ, 1);
        STAMP(10);
        PROJ((void)0, WS_H1 + (4 * x + (((l16 >> 2) + 1) & 3)) * 8192 + q4 * 2048 + (n & 1) * 1024);
        STAMP(11);
        STUB(4);
        waitw<false>(S_UK, 1);
        v4u uk[8];
#pragma unroll
        for (int t = 0; t < 2; ++t)
#pragma unroll
          for (int s = 0; s < 4; ++s) uk[t * 4 + s] = lds128(L_UK + (32 * w + 8 * (n >> 2) + 4 * t + (n & 3)) * 256 + 16 * ((4 * s + g) ^ n));
        v4u e0, e1;
#define H1_LD() do { e0 = LD1(WS_H1 + ht * 8192 + g * 2048 + w * 256 + n * 16); e1 = LD1(WS_H1 + ht * 8192 + g * 2048 + 1024 + w * 256 + n * 16); \
                     asm volatile("" ::: "memory"); } while (0)
        H1_LD();
        for (int spin = 0; spin < SPIN_CAP; ++spin) {
          if (__all(e0.y == tag && e0.w == tag && e1.y == tag && e1.w == tag)) break;
          H1_LD();
        }
        STAMP(14);
        {
          auto p = __builtin_amdgcn_permlane32_swap(e0.x, e1.x, false, false), q = __builtin_amdgcn_permlane32_swap(e0.z, e1.z, false, false);
          const float a = sum16(f_(p[0]) + f_(p[1])), c = sum16(f_(q[0]) + f_(q[1]));
          if ((g & 1) == 0) *lp<u32>(L_QL + (g >> 1) * 256 + w * 64 + n * 4) = pk(a, c);
        }
        sig(S_H1, lane);
        waitw<false>(S_H1, 4);
        v4u qb[4];
#pragma unroll
        for (int s = 0; s < 4; ++s) qb[s] = lds128(L_QL + (lane & 1) * 256 + g * 16 + s * 64);
        SB();
        f32x4 u0 = {0.f, 0.f, 0.f, 0.f}, u1 = u0;
#pragma unroll
        for (int s = 0; s < 4; ++s) { u0 = mfma16(uk[s], qb[s], u0); u1 = mfma16(uk[4 + s], qb[s], u1); }
        const v4u pv = {pk(u0[0], u0[1]), pk(u0[2], u0[3]), pk(u1[0], u1[1]), pk(u1[2], u1[3])};
        if (n < 2) lds128w(L_QX + n * 256 + w * 64 + g * 16, pv);
        sig(S_PUB, lane);
        STAMP(15);
      }
      STUB(5);
      waitw<true>(S_LAND, w + 1);
      f32x4 sc[4];
      {
        v4u kf[16], qf[4];
        const u32 ka = L_K + (64 * w + n) * 256 + 16 * (g ^ n);
#pragma unroll
        for (int i = 0; i < 16; ++i) kf[i] = lds128((ka ^ (64 * (i & 3))) + (i >> 2) * 4096);
        sig(S_KRD, lane);
        SB();
        waitw<false>(S_QBRDY, 1);
        STAMP(3);
#pragma unroll
        for (int s = 0; s < 4; ++s) qf[s] = lds128(L_QBL + (lane & 3) * 256 + g * 16 + s * 64);
        SB();
#pragma unroll
        for (int t = 0; t < 4; ++t) {
          sc[t] = f32x4{0.f, 0.f, 0.f, 0.f};
#pragma unroll
          for (int s = 0; s < 4; ++s) sc[t] = mfma16(kf[4 * t + s], qf[s], sc[t]);
        }
      }
      float m = sc[0][0];
#pragma unroll
      for (int i = 1; i < 16; ++i) m = fmaxf(m, sc[i >> 2][i & 3]);
      m = max32(max16(m));
      if (lane < 4) *lp<float>(L_MAXX + lane * 16 + w * 4) = m;
      sig(S_MAX, lane);
      STUB(6);
      waitw<false>(S_MAX, 4);
      const v4u mm = lds128(L_MAXX + (lane & 3) * 16);
      const float mk = fmaxf(fmaxf(f_(mm.x), f_(mm.y)), fmaxf(f_(mm.z), f_(mm.w))) * KSC;
      float ls = 0.f;
#pragma unroll
      for (int i = 0; i < 16; ++i) { const float p = __builtin_amdgcn_exp2f(fmaf(sc[i >> 2][i & 3], KSC, -mk)); sc[i >> 2][i & 3] = p; ls += p; }
      ls = sum32(sum16(ls));
      if (n < 4) {
#pragma unroll
        for (int u = 0; u < 2; ++u)
          lds128w(L_PB + (2 * w + u) * 256 + g * 64 + n * 16,
                  v4u{pk(sc[2 * u][0], sc[2 * u][1]), pk(sc[2 * u][2], sc[2 * u][3]), pk(sc[2 * u + 1][0], sc[2 * u + 1][1]), pk(sc[2 * u + 1][2], sc[2 * u + 1][3])});
      }
      if (lane < 4) *lp<float>(L_LSUM + lane * 16 + w * 4) = ls;
      sig(S_P, lane);
      STUB(7);
      waitw<false>(S_P, 4);
      STAMP(5);
      v4u pf[8];
#pragma unroll
      for (int k = 0; k < 8; ++k) pf[k] = lds128(L_PB + k * 256 + g * 64 + (lane & 3) * 16);
      if (w == 0) {
        const v4u lv = lds128(L_LSUM + (lane & 3) * 16);
        const float lsum = (f_(lv.x) + f_(lv.y)) + (f_(lv.z) + f_(lv.w));
        if (lane < 4) ST1((v4u{u_(mk), tag, u_(lsum), tag}), WS_BIN + (4 * x + lane) * 9216 + 8192 + l16 * 16);
      }
      f32x4 o0 = {0.f, 0.f, 0.f, 0.f}, o1 = o0;
      const u32 va = L_V + (4 * g + tq) * 256 + 16 * ((4 * w + (tp >> 1)) ^ ((4 * (g & 1) + tq) << 1)) + 8 * (tp & 1);
#pragma unroll
      for (int k2 = 0; k2 < 4; ++k2) {
        waitw<false>(S_LAND, k2 + 5);
        v4u vf[4];
#pragma unroll
        for (int i = 0; i < 4; ++i) { const u32 a = (va ^ (32 * (i & 1))) + (2 * k2 + (i >> 1)) * 8192; vf[i] = ldstr(a, a + 4096); }
        SB();
        o0 = mfma16(vf[0], pf[2 * k2], o0); o1 = mfma16(vf[1], pf[2 * k2], o1);
        o0 = mfma16(vf[2], pf[2 * k2 + 1], o0); o1 = mfma16(vf[3], pf[2 * k2 + 1], o1);
      }
      STAMP(6);
      {
        const f32x4 o = (n & 4) ? o1 : o0;
        if (n < 8) ST1((v4u{pk(o[0], o[1]), tag, pk(o[2], o[3]), tag}), WS_BIN + (4 * x + (n & 3)) * 9216 + l16 * 512 + (4 * (2 * w + (n >> 2)) + g) * 16);
      }
      STAMP(7);
      STUB(8);
      waitw<false>(S_W1, 1);
      STAMP(8);
      v4u a1[8];
#pragma unroll
      for (int s = 0; s < 8; ++s) { const u32 a = L_W1 + (ab ^ (32 * (s >> 2))) + (s & 3) * 8192; a1[s] = ldstr(a, a + 1024); }
      v4u e0, e1, st;
      for (int spin = 0; spin < SPIN_CAP; ++spin) {
        e0 = LD1(WS_BIN + ht * 9216 + w * 2048 + lane * 16);
        e1 = LD1(WS_BIN + ht * 9216 + w * 2048 + 1024 + lane * 16);
        st = LD1(WS_BIN + ht * 9216 + 8192 + n * 16);
        asm volatile("" ::: "memory");
        if (__all(e0.y == tag && e0.w == tag && e1.y == tag && e1.w == tag && st.y == tag && st.w == tag)) break;
      }
      STAMP(9);
      lds64w(L_BB + (4 * w + (lane >> 5)) * 272 + (lane & 31) * 8, e0.x, e0.z);
      lds64w(L_BB + (4 * w + 2 + (lane >> 5)) * 272 + (lane & 31) * 8, e1.x, e1.z);
      sig(S_TB, lane);
      float M = f_(st.x);
      M = fmaxf(M, dpp<X1>(M)); M = fmaxf(M, dpp<X2>(M)); M = fmaxf(M, dpp<X7>(M));
      const float ee = __builtin_amdgcn_exp2f(f_(st.x) - M);
      float S = ee * f_(st.z);
      S += dpp<X1>(S); S += dpp<X2>(S); S += dpp<X7>(S);
      const float wt = ee / S;
      STUB(9);
      waitw<false>(S_TB, 4);
      v4u bq[4];
#pragma unroll
      for (int s = 0; s < 4; ++s) bq[s] = lds128(L_BB + n * 272 + g * 16 + s * 64);
      SB();
      f32x4 d0 = {0.f, 0.f, 0.f, 0.f}, d1 = d0;
#pragma unroll
      for (int s = 0; s < 4; ++s) { d0 = mfma16(a1[s], bq[s], d0); d1 = mfma16(a1[4 + s], bq[s], d1); }
#pragma unroll
      for (int k = 0; k < 4; ++k) {
        float t = d0[k] * wt, u = d1[k] * wt;
        t += dpp<X1>(t); u += dpp<X1>(u); t += dpp<X2>(t); u += dpp<X2>(u); t += dpp<X7>(t); u += dpp<X7>(u);
        d0[k] = (n & 2) ? u : t;
      }
      const bool k1 = (n & 1) != 0;
      if ((n & 4) == 0) ST1((v4u{u_(k1 ? d0[2] : d0[0]), tag, u_(k1 ? d0[3] : d0[1]), tag}),
                            WS_H4 + 262144 + (((q4 * 8 + 2 * w + ((n >> 1) & 1)) * 2 + (n >> 3)) * 32 + ht) * 128 + g * 32 + (n & 1) * 16);
      STAMP(12);
    }
  }
  {
    const __amdgpu_buffer_rsrc_t rs2 = __builtin_amdgcn_make_buffer_rsrc((void*)(ws + WS_STAMP), 0, 1 << 20, 0x00020000);
    if (wave == 0 && lane == 0) __builtin_amdgcn_raw_buffer_store_b64(v2u{tsv, tag}, rs2, gid * 128, 0, 16);
    if (gid == 0 && wave == 0) {
      u32 st[4];
      for (int spin = 0; spin < SPIN_CAP; ++spin) {
        bool ok = true;
#pragma unroll
        for (int i = 0; i < 4; ++i) { const v2u a = __builtin_amdgcn_raw_buffer_load_b64(rs2, (lane + 64 * i) * 128, 0, 16); st[i] = a.x; ok = ok && a.y == tag; }
        asm volatile("" ::: "memory");
        if (__all(ok)) break;
      }
      const u32 base = __builtin_amdgcn_readfirstlane(st[0]) - 100000u;
      u32 mn = min(min(st[0] - base, st[1] - base), min(st[2] - base, st[3] - base));
      u32 mk = (lane & 7) == XG ? mn : 0xffffffffu;
      for (int o = 32; o > 0; o >>= 1) { mn = min(mn, (u32)__shfl_xor(mn, o)); mk = min(mk, (u32)__shfl_xor(mk, o)); }
      const u32 X = __builtin_amdgcn_readfirstlane(mk - mn);
      const u64 t9 = __builtin_amdgcn_s_memrealtime();
      while (__builtin_amdgcn_s_memrealtime() - t9 < (u64)X * 10) { }
    }
  }
}

extern "C" void kernel_launch(void* const* d_in, const int* in_sizes, int n_in, void* d_out, int out_size, void* d_ws, size_t ws_size,
                              hipStream_t stream) {
  (void)hipFuncSetAttribute((const void*)fused_decode, hipFuncAttributeMaxDynamicSharedMemorySize, 163840);
  fused_decode<<<256, 512, 163840, stream>>>((const u16*)d_in[0], (const u16*)d_in[2], (const u16*)d_in[6], (const u16*)d_in[9],
                                             (unsigned char*)d_ws, (const u16*)d_in[1], (const u16*)d_in[3], (const u16*)d_in[7],
                                             (const u16*)d_in[8], (const u16*)d_in[4], (const u16*)d_in[5], (const u16*)d_in[10],
                                             (u16*)d_out);
}
```

```cpp
#define DIRTY 0
#define OUTSC1 0
#include <hip/hip_runtime.h>
#include <hip/hip_bf16.h>

typedef unsigned u32;
typedef unsigned short u16;
typedef unsigned long long u64;
typedef u32 v4u __attribute__((ext_vector_type(4)));
typedef u32 v2u __attribute__((ext_vector_type(2)));
typedef short s16x4 __attribute__((ext_vector_type(4)));
typedef short bf16x8 __attribute__((ext_vector_type(8)));
typedef __bf16 bf16x2 __attribute__((ext_vector_type(2)));
typedef float f32x4 __attribute__((ext_vector_type(4)));
typedef float f32x16 __attribute__((ext_vector_type(16)));
#define GAS __attribute__((address_space(1)))
#define LAS __attribute__((address_space(3)))
#define DI __device__ __forceinline__
#define SB() __builtin_amdgcn_sched_barrier(0)

#define WIN0 32
#define WINA0 24
#define NA0 2
#define WINA 32
#define WINB0 8
#define NB0 0
#define WINB 24
#define THOLD 45
#define BGATE 0
#define WINU 32
#define WUV_AT 8
#define PDELAY 70
#define ABL 0
#define FDELAY 160
#define KSTART 450
#define XFIRST 0
#define HPOLL 6
#define QBDELAY 70
#define H1DELAY 0
#define WINUK 63
#define XPOL ""
#define NC0 0
#define OPTQ 1
#define GATEL 5
#define PAGES 1
#define QBSTART 350
#define KEYMIX 1
#define WQMIX 0
#define KVMIX 1
#define SOON 0
#define QW 8
#define CODE_BYTES 23552

#define TYPEBIT 5
constexpr u32 WS_EPOCH = 0;
constexpr u32 WS_GATE = 4096;
constexpr u32 WS_SOON = 8192;
constexpr u32 WS_HINT = 16384;
constexpr u32 WS_QABS = 65536;
constexpr u32 WS_ASTAT = 262144;
constexpr u32 WS_H3B = 524288;
constexpr u32 WS_BIN = 786432;
constexpr u32 WS_QB = 6291456 + 4096;
constexpr u32 WS_H4 = 1310720;
constexpr u32 WS_H1 = 1835008;
constexpr u32 WS_APART = 2097152;
constexpr u32 APART_BLK = 1048576 + 256;
constexpr u32 WS_BYTES = 6291456 + 4096 + 262144;
constexpr u32 WS_STAMP = 8388608;

constexpr u32 L_C = 0, L_K = 0, L_V = 65536, L_UK = 32768, L_WQ = 98304;
constexpr u32 L_Q = 131072;
constexpr u32 L_X = L_Q + 16384, L_QL = L_Q + 18432, L_QX = L_Q + 20480, L_QBL = L_Q + 22528, L_PB = L_Q + 24576;
constexpr u32 L_P = L_Q, L_SCR = L_Q + 8192, L_LSUM = L_Q + 28672, L_EXP = L_Q + 29696, L_GOT = L_Q + 29824;
constexpr u32 L_MAXX = L_Q + 31744, L_SYNC = L_MAXX + 512, L_PTR = L_SYNC + 256;
constexpr u32 L_W1 = 0, L_W2 = 32768, L_WGT = 65536, L_CK = 66048, L_BAD = 66560, L_B1 = 67584;
constexpr u32 L_BB = L_Q, L_B2 = L_Q + 8192;
enum { S_WQ = 0, S_UK, S_PUB, S_TAG, S_H1, S_QBRDY, S_XRD, S_CISS, S_HINT, S_QGO, S_QISS, S_QST, S_LAND, S_MAX, S_BAD, S_P, S_PV, S_KRD, S_W1, S_W2, S_TB, S_TA, S_N };

constexpr float KSC = 0.08838834764831845f * 1.4426950408889634f;
constexpr int SPIN_CAP = 1 << 15;
constexpr u32 QTRIES = 64;

#define STAMP(i) do { } while (0)

template <u32 LDS> DI void dma1(const void* sb, u32 v) {
  asm volatile("s_mov_b32 m0, %2\n\ts_nop 0\n\tglobal_load_lds_dwordx4 %0, %1" :: "v"(v), "s"(sb), "i"(LDS) : "memory");
}
DI void dma4s(const void* sb, u32 v0, u32 v1, u32 v2, u32 v3, u32 lds) {
  asm volatile("s_mov_b32 m0, %5\n\ts_nop 0\n\tglobal_load_lds_dwordx4 %0, %4 sc1\n\tglobal_load_lds_dwordx4 %1, %4 offset:1024 sc1\n\t"
               "global_load_lds_dwordx4 %2, %4 offset:2048 sc1\n\tglobal_load_lds_dwordx4 %3, %4 offset:3072 sc1"
               :: "v"(v0), "v"(v1), "v"(v2), "v"(v3), "s"(sb), "s"(lds) : "memory");
}
template <int W> DI void dma4r(const void* sb, u32 v0, u32 v1, u32 v2, u32 v3, u32 lds) {
  asm volatile("s_mov_b32 m0, %5\n\ts_waitcnt vmcnt(%6)\n\tglobal_load_lds_dwordx4 %0, %4 nt\n\tglobal_load_lds_dwordx4 %1, %4 offset:1024 nt\n\t"
               "s_waitcnt vmcnt(%6)\n\tglobal_load_lds_dwordx4 %2, %4 offset:2048 nt\n\tglobal_load_lds_dwordx4 %3, %4 offset:3072 nt"
               :: "v"(v0), "v"(v1), "v"(v2), "v"(v3), "s"(sb), "s"(lds), "i"(W) : "memory");
}
typedef const unsigned char* cptr;
DI cptr mkptr(u32 lo, u32 hi) { return (cptr)((u64)hi << 32 | lo); }
template <int N> DI void vmwait() { asm volatile("s_waitcnt vmcnt(%0)" :: "i"(N) : "memory"); }
DI u32 pk(float a, float b) { bf16x2 v = {(__bf16)a, (__bf16)b}; return __builtin_bit_cast(u32, v); }
DI float blo(u32 x) { return __builtin_bit_cast(float, x << 16); }
DI float bhi(u32 x) { return __builtin_bit_cast(float, x & 0xffff0000u); }
template <class T> DI LAS T* lp(u32 a) { return (LAS T*)(size_t)a; }
DI v4u lds128(u32 a) { return *lp<v4u>(a); }
DI void lds128w(u32 a, v4u v) { *lp<v4u>(a) = v; }
DI void lds64w(u32 a, u32 x, u32 y) { *lp<v2u>(a) = v2u{x, y}; }
DI v4u ldstr(u32 a0, u32 a1) {
  s16x4 lo = __builtin_amdgcn_ds_read_tr16_b64_v4i16(lp<s16x4>(a0));
  s16x4 hi = __builtin_amdgcn_ds_read_tr16_b64_v4i16(lp<s16x4>(a1));
  v2u l = __builtin_bit_cast(v2u, lo), h = __builtin_bit_cast(v2u, hi);
  return v4u{l.x, l.y, h.x, h.y};
}
DI f32x4 mfma16(v4u a, v4u b, f32x4 c) { return __builtin_amdgcn_mfma_f32_16x16x32_bf16(__builtin_bit_cast(bf16x8, a), __builtin_bit_cast(bf16x8, b), c, 0, 0, 0); }
DI f32x16 mfma32(v4u a, v4u b, f32x16 c) { return __builtin_amdgcn_mfma_f32_32x32x16_bf16(__builtin_bit_cast(bf16x8, a), __builtin_bit_cast(bf16x8, b), c, 0, 0, 0); }
template <int CTRL> DI u32 dppu(u32 x) { return (u32)__builtin_amdgcn_mov_dpp((int)x, CTRL, 0xf, 0xf, true); }
DI float f_(u32 x) { return __builtin_bit_cast(float, x); }
DI u32 u_(float x) { return __builtin_bit_cast(u32, x); }
template <int CTRL> DI float dpp(float x) { return f_(dppu<CTRL>(u_(x))); }
constexpr int X1 = 0xB1, X2 = 0x4E, X7 = 0x141, X8 = 0x128;
DI float max3(float a, float b, float c) { float r; asm("v_max3_f32 %0, %1, %2, %3" : "=v"(r) : "v"(a), "v"(b), "v"(c)); return r; }
DI float sum32(float x) { auto t = __builtin_amdgcn_permlane32_swap(u_(x), u_(x), false, false); return f_(t[0]) + f_(t[1]); }
DI float max32(float x) { auto t = __builtin_amdgcn_permlane32_swap(u_(x), u_(x), false, false); return fmaxf(f_(t[0]), f_(t[1])); }
DI float sum16(float x) { auto t = __builtin_amdgcn_permlane16_swap(u_(x), u_(x), false, false); return f_(t[0]) + f_(t[1]); }
DI float max16(float x) { auto t = __builtin_amdgcn_permlane16_swap(u_(x), u_(x), false, false); return fmaxf(f_(t[0]), f_(t[1])); }
DI void sig(int word, int) {
  asm volatile("s_mov_b64 exec, 1\n\tds_add_u32 %0, %1\n\ts_mov_b64 exec, -1" :: "v"(L_SYNC + 4 * word), "v"(1u) : "memory");
}
template <bool SLEEP> DI void waitw(int word, u32 n) {
  u32 v, t, c = 1u << 20;
  asm volatile("1:\n\tds_read_b32 %0, %3\n\ts_waitcnt lgkmcnt(0)\n\tv_readfirstlane_b32 %1, %0\n\ts_cmp_ge_u32 %1, %4\n\ts_cbranch_scc1 2f\n\t"
               "s_sleep %5\n\ts_sub_u32 %2, %2, 1\n\ts_cbranch_scc0 1b\n2:"
               : "=&v"(v), "=&s"(t), "+s"(c) : "v"(L_SYNC + 4 * word), "s"(n), "i"(SLEEP ? 2 : 0) : "memory", "scc");
}
#define STUB(n) asm volatile("s_branch .Lsk" #n "\n.Lstub" #n ":\n\ts_setpc_b64 vcc\n.Lsk" #n ":")
#define TOUCH(n) asm volatile("s_call_b64 vcc, .Lstub" #n ::: "vcc")
#define BAR0(id) asm volatile("s_waitcnt lgkmcnt(0)\n\ts_barrier ; start " #id ::: "memory")
DI bool qbad(int lane) { return __any(*lp<u32>(L_EXP + (lane & 31) * 4) != *lp<u32>(L_GOT + (lane & 31) * 4)); }
DI void bar() { asm volatile("s_waitcnt lgkmcnt(0)\n\ts_barrier" ::: "memory"); }
#define LD1(off) __builtin_amdgcn_raw_buffer_load_b128(rs, (off), 0, 16)
#define ST1(v, off) __builtin_amdgcn_raw_buffer_store_b128((v), rs, (off), 0, 16)
#define TAGLOAD() __builtin_amdgcn_raw_buffer_load_b32(rs, WS_EPOCH, 0, 16)
DI v4u ldnt(const void* p, u32 off) { return __builtin_nontemporal_load((const GAS v4u*)((const GAS unsigned char*)p + off)); }
DI void stage_w(u32 base, const v4u (&park)[32], u32 g, u32 n) {
#pragma unroll
  for (int i = 0; i < 32; ++i) lds128w(base + g * 256 + i * 1024 + 16 * (n ^ ((g | (((i >> 1) & 1) << 2)) << 1)), park[i]);
}

__global__ void __launch_bounds__(512, 2)
fused_decode(const u16* __restrict__ xA, const u16* __restrict__ wqA, const u16* __restrict__ xB, const u16* __restrict__ wqB,
             unsigned char* ws_, const u16* __restrict__ ckv, const u16* __restrict__ wuk, const u16* __restrict__ kc,
             const u16* __restrict__ vc, const u16* __restrict__ wuv, const u16* __restrict__ woA, const u16* __restrict__ woB,
             u16* __restrict__ out) {
  typedef u32 s8u __attribute__((ext_vector_type(8)));
  typedef u32 s16u __attribute__((ext_vector_type(16)));
  const int tid = threadIdx.x, lane = tid & 63, wave = __builtin_amdgcn_readfirstlane(tid >> 6);
  s8u ka;
  s16u kb;
  if (wave == 0) asm volatile("s_load_dwordx8 %0, %2, 0x0\n\ts_load_dwordx16 %1, %2, 0x20" : "=&s"(ka), "=&s"(kb) : "s"(__builtin_amdgcn_kernarg_segment_ptr()));
  u64 pc0; asm volatile("s_getpc_b64 %0" : "=s"(pc0));
  const u32 gid = blockIdx.x, x = gid & 7, r = gid >> 3;
  const bool isA = (r & 4) == 0;
  const u32 l16 = (r & 3) | ((r >> 3) << 2);
  const u32 ht = 4 * x + (l16 >> 2), q4 = l16 & 3;
  const u32 g = lane >> 4, n = lane & 15, tq = n >> 2, tp = lane & 3;
  u32 warm = 0, ce = 0, cd = 0;
  u64 t0 = 0;
  unsigned char* ws;
  if (wave == 0) {
    const u32 vo = q4 * 1048576 + (((l16 >> 2) + 1) & 3) * 256 + x * 1024 + g * 8192;
    ce = vo + ((n ^ (g << 1)) * 16); cd = vo + ((n ^ ((g | 4) << 1)) * 16) + 63488;
    asm volatile("s_waitcnt lgkmcnt(0)" : "+s"(ka), "+s"(kb));
    STAMP(12);
    asm volatile("s_memrealtime %0" : "=s"(t0));
    if (THOLD > 0 && isA) {
      asm volatile("s_waitcnt lgkmcnt(0)" : "+s"(t0));
      while (__builtin_amdgcn_s_memrealtime() - t0 < THOLD) { }
    }
    dma1<L_X>(isA ? mkptr(ka[4], ka[5]) : mkptr(ka[0], ka[1]), (g & 1) * 1024 + q4 * 256 + n * 16);
    cptr wq = isA ? mkptr(ka[6], ka[7]) : mkptr(ka[2], ka[3]);
#pragma unroll 1
    for (u32 G = 0; G < (WQMIX && isA ? 0u : 8u); ++G) dma4r<63>(wq + G * 131072, ce, ce + 31744, cd, cd + 31744, L_WQ + G * 4096);
    if (lane < 2) *lp<u32>(L_PTR + 4 * lane) = lane ? kb[1] : kb[0];
    BAR0(0);
    asm volatile("s_waitcnt lgkmcnt(0)" : "+s"(t0));
    STAMP(15);
  } else if (wave == 1) {
    const u32 ln = (r & 3) * 64 + lane;
    warm = ((const GAS u32*)(pc0 & ~127ull))[(ln < CODE_BYTES / 128 ? ln : 0u) * 32];
    SB();
  } else if (wave == 2) {
    if (lane < S_N) *lp<u32>(L_SYNC + 4 * lane) = 0;
  } else if (wave == 3) {
    s16u kc3;
    if (PAGES) asm volatile("s_load_dwordx16 %0, %1, 0x20" : "=s"(kc3) : "s"(__builtin_amdgcn_kernarg_segment_ptr()));
    if (isA) TOUCH(1); else TOUCH(2);
    if (PAGES) {
      asm volatile("s_waitcnt lgkmcnt(0)" : "+s"(kc3));
      const size_t kvb = (((size_t)(l16 >> 3) * 8 + x) * 8192 + 6144 + (l16 & 7) * 256) * 256;
      cptr p0 = isA ? mkptr(kc3[2], kc3[3]) + (size_t)(x * 16 + l16) * 131072 : mkptr(kc3[6], kc3[7]) + kvb;
      cptr p1 = isA ? mkptr(kc3[10], kc3[11]) + q4 * 1048576 + ht * 256 : mkptr(kc3[8], kc3[9]) + kvb;
      cptr p2 = isA ? p1 + 1040384 : mkptr(kc3[12], kc3[13]) + ht * 131072 + q4 * 256;
      cptr p3 = isA ? p0 + 130944 : mkptr(kc3[14], kc3[15]) + ht * 131072 + q4 * 256;
      cptr pp = (lane & 2) ? ((lane & 1) ? p3 : p2) : ((lane & 1) ? p1 : p0);
      if (KEYMIX && isA && (lane & 3) == 0) pp = mkptr(kc3[2], kc3[3]) + (size_t)(x >> 2) * 8388608 + ((x & 3) * 16 + l16) * 16384 + ((lane >> 2) & 7) * 1048576;
      warm = __builtin_nontemporal_load((const GAS u32*)pp);
      SB();
    }
  }
  __amdgpu_buffer_rsrc_t rs;
#define SETWS(lo, hi) do { ws = (unsigned char*)mkptr(lo, hi); rs = __builtin_amdgcn_make_buffer_rsrc((void*)ws, 0, WS_BYTES, 0x00020000); } while (0)
#define START(id) do { BAR0(id); const v2u p_ = *lp<v2u>(L_PTR); SETWS(__builtin_amdgcn_readfirstlane(p_.x), __builtin_amdgcn_readfirstlane(p_.y)); } while (0)
  u32 tag;
#define SIG_AT(ISSUED, W, NEED, WORD) if constexpr ((ISSUED) - (W) >= (NEED) && (ISSUED) - 4 - (W) < (NEED)) { vmwait<W>(); sig(WORD, lane); }
  const u32 w = wave & 3;
  const u32 ab = (8 * g + tq) * 256 + 16 * ((4 * w + (tp >> 1)) ^ ((tq | ((g & 1) << 2)) << 1)) + 8 * (tp & 1);

#define PROJ(BEFORE_STORE, DST) do { \
    v4u xf[4], af[8]; \
    _Pragma("unroll") for (int s = 0; s < 4; ++s) xf[s] = lds128(L_X + (lane & 1) * 256 + g * 16 + s * 64); \
    _Pragma("unroll") for (int s = 0; s < 8; ++s) { const u32 a = L_WQ + (ab ^ (32 * (s & 1))) + (s >> 1) * 8192; af[s] = ldstr(a, a + 1024); } \
    SB(); \
    f32x4 c0 = {0.f, 0.f, 0.f, 0.f}, c1 = c0; \
    _Pragma("unroll") for (int s = 0; s < 4; ++s) { c0 = mfma16(af[2 * s], xf[s], c0); c1 = mfma16(af[2 * s + 1], xf[s], c1); } \
    const f32x4 c = (n & 4) ? c1 : c0; \
    BEFORE_STORE; \
    if (n < 8) ST1((v4u{u_((n & 2) ? c[2] : c[0]), tag, u_((n & 2) ? c[3] : c[1]), tag}), (DST) + (16 * w + 8 * (n >> 2) + 2 * g + ((n >> 1) & 1)) * 16); \
  } while (0)

  if (isA) {
    if (wave == 0) {
      STUB(1);
      SETWS(kb[0], kb[1]);
      __builtin_amdgcn_s_setprio(1);
      cptr src = mkptr(kb[2], kb[3]) + (KEYMIX ? (size_t)(x >> 2) * 8388608 + ((x & 3) * 16 + l16) * 16384 : (size_t)(x * 16 + l16) * 131072);
      constexpr u32 HS = KEYMIX ? 1048576 : 16384;
      u32 cv[16];
#pragma unroll
      for (int i = 0; i < 16; ++i) cv[i] = (lane ^ (((i & 3) << 2) | (i >> 2))) * 16;
#pragma unroll 1
      for (u32 H = 0; H < 8; ++H) {
        if (H == 6) { waitw<false>(S_XRD, 6); sig(S_CISS, lane); }
#pragma unroll
        for (int G = 0; G < 4; ++G) {
          dma4r<WINA>(src + H * HS + G * 4096, cv[4 * G], cv[4 * G + 1], cv[4 * G + 2], cv[4 * G + 3], L_C + H * 16384 + G * 4096);
          if (WQMIX && G < 3 && 3 * H + G < 8) dma4r<WINA>(mkptr(ka[6], ka[7]) + (3 * H + G) * 131072, ce, ce + 31744, cd, cd + 31744, L_WQ + (3 * H + G) * 4096);
          constexpr int WQAT = (WQMIX ? 9 : -1) + WINA / 4;
          if (G == (WQAT & 3) && H == WQAT / 4) { vmwait<WINA>(); sig(S_WQ, lane); STAMP(6); }
        }
      }
      sig(S_CISS, lane);
      STAMP(1);
      vmwait<0>();
      STAMP(2);
      bar();
      v4u park[32];
      const u32 vo = (128 * q4 + g) * 8192 + ht * 256 + n * 16;
#pragma unroll
      for (int i = 0; i < 32; ++i) park[i] = ldnt(mkptr(kb[10], kb[11]), vo + i * 32768);
      const u32 tv = TAGLOAD();
      SB();
      STAMP(3);
      for (u32 it = 1; it <= QTRIES; ++it) {
        bar(); bar();
        if (!qbad(lane) || it == QTRIES) break;
        bar(); bar();
      }
      bar(); bar();
      stage_w(L_W1, park, g, n);
      tag = __builtin_amdgcn_readfirstlane(tv) + 1;
      STAMP(4);
    } else if (wave < 4) {
      START(1);
      if (wave == 1) {
        asm volatile("" :: "v"(warm));
        TOUCH(11); TOUCH(12); TOUCH(13); TOUCH(14); TOUCH(15); TOUCH(16); TOUCH(17); TOUCH(18); TOUCH(19);
        if (lane < 6) { u32 t = __builtin_amdgcn_raw_buffer_load_b32(rs, lane < 2 ? lane * 1048576 + 262144 - 64 : WS_APART + (lane - 1) * APART_BLK - 128, 0, 16); asm volatile("" :: "v"(t)); }
      }
      if (wave != 2) sig(S_XRD, lane);
      asm volatile("" :: "v"(warm));
      tag = __builtin_amdgcn_readfirstlane(TAGLOAD()) + 1;
      v4u h0, h1;
      cptr qs = ws + WS_QABS + (x >> 2) * 32768;
#define Q_ISSUE() _Pragma("unroll 1") for (u32 G = 0; G < 8; ++G) { const u32 l0 = (lane ^ (G & 3)) * 16; \
          dma4s(qs + G * 4096, l0, l0 ^ 64, l0 ^ 128, l0 ^ 192, L_Q + G * 4096); }
#define HINT_LD(p, q) do { p = LD1(WS_HINT + lane * 128); q = LD1(WS_HINT + 8192 + lane * 128); asm volatile("" ::: "memory"); } while (0)
      if (wave == 2) {
        __builtin_amdgcn_s_setprio(2);
        waitw<true>(S_CISS, 1);
#pragma unroll 1
        for (int spin = 0; spin < 64; ++spin) {
          const u32 gt = __builtin_amdgcn_raw_buffer_load_b32(rs, WS_GATE + (lane & 7) * 128, 0, 16);
          asm volatile("" ::: "memory");
          const bool ok = __all(gt == tag);
          if (spin == 0) waitw<false>(S_CISS, 2);
          if (ok) break;
        }
        STAMP(3);
        if (OPTQ) {
          asm volatile("global_load_dwordx4 %0, %2, %4 sc1\n\tglobal_load_dwordx4 %1, %3, %4 sc1"
                       : "=&v"(h0), "=&v"(h1) : "v"(WS_HINT + lane * 128), "v"(WS_HINT + 8192 + lane * 128), "s"(ws) : "memory");
          Q_ISSUE()
          asm volatile("s_waitcnt vmcnt(32)" : "+v"(h0), "+v"(h1) :: "memory");
        } else HINT_LD(h0, h1);
        if (!OPTQ || !__all(h0.z == tag && h1.z == tag)) {
#pragma unroll 1
          for (int spin = 0; spin < 64; ++spin) {
            if (__all(h0.z == tag && h1.z == tag)) break;
            HINT_LD(h0, h1);
          }
          Q_ISSUE()
        }
        STAMP(1);
        vmwait<0>();
      }
      bar();
      for (u32 it = 1; it <= QTRIES; ++it) {
        bar();
        if (wave == 2) {
          u32 e0 = (x & 4 ? h0.y : h0.x) - tag, e1 = (x & 4 ? h1.y : h1.x) - tag;
          e0 += dppu<X1>(e0); e1 += dppu<X1>(e1); e0 += dppu<X2>(e0); e1 += dppu<X2>(e1);
          if (tp < 2) *lp<u32>(L_EXP + (lane >> 2) * 4 + tp * 64) = tp ? e1 : e0;
        }
        bar();
        if (!qbad(lane) || it == QTRIES) break;
        bar();
        if (wave == 2) { HINT_LD(h0, h1); Q_ISSUE() vmwait<0>(); }
        bar();
      }
      bar(); bar();
      if (wave == 1) *lp<u32>(L_BAD) = 0;
    } else {
      START(2);
      tag = __builtin_amdgcn_readfirstlane(TAGLOAD()) + 1;
      const u32 bs = x * 16 + l16, b = bs >> 6, hh = lane >> 5, hd = lane & 31;
      __builtin_amdgcn_s_setprio(2);
      waitw<false>(S_WQ, 1);
      PROJ(sig(S_XRD, lane), WS_QB + x * 32768 + (n & 1) * 16384 + (q4 * 4 + (((l16 >> 2) + 1) & 3)) * 1024);
      STAMP(13);
      STUB(11);
      f32x16 s;
      v4u mm;
      for (u32 it = 1; it <= QTRIES; ++it) {
        u32 cs = 0;
        bar();
        STAMP(3);
#pragma unroll
        for (int i = 0; i < 16; ++i) s[i] = 0.f;
        {
          const u32 fr = ((hd & 3) << 2) | ((hd >> 2) & 3);
          const u32 qa = L_Q + hd * 1024 + 16 * (hh ^ fr), ca = qa - L_Q + L_C + w * 32768;
          v4u af[32], bf[32];
          constexpr int PD = 6;
#define S_RD(k) do { bf[k] = lds128((qa ^ (32 * ((k) & 7))) + 256 * ((k) >> 3)); af[k] = lds128((ca ^ (32 * ((k) & 7))) + 256 * ((k) >> 3)); } while (0)
#pragma unroll
          for (int k = 0; k < PD; ++k) S_RD(k);
          SB();
#pragma unroll
          for (int k = 0; k < 32; ++k) {
            if (k + PD < 32) S_RD(k + PD);
            s = mfma32(af[k], bf[k], s);
            cs = cs + bf[k].x + bf[k].y; cs = cs + bf[k].z + bf[k].w;
            if (k == 4) STUB(12);
            if (k == 26) { STUB(13); }
            if (k == 29) bar();
            SB();
          }
        }
        { auto t = __builtin_amdgcn_permlane32_swap(cs, cs, false, false); cs = t[0] + t[1]; }
        asm volatile("s_nop 11" : "+v"(s));
        float m = max3(max3(max3(s[0], s[1], s[2]), max3(s[3], s[4], s[5]), max3(s[6], s[7], s[8])),
                       max3(max3(s[9], s[10], s[11]), max3(s[12], s[13], s[14]), s[15]), s[15]);
        m = max32(m);
        if (hh == 0) *lp<float>(L_MAXX + hd * 16 + w * 4) = m;
        if (w == 0 && hh == 0) *lp<u32>(L_GOT + hd * 4) = cs;
        STAMP(4);
        bar();
        mm = lds128(L_MAXX + hd * 16);
        if (!qbad(lane) || it == QTRIES) break;
        bar();
      }
      const float mk = fmaxf(fmaxf(f_(mm.x), f_(mm.y)), fmaxf(f_(mm.z), f_(mm.w))) * KSC;
      float ls = 0.f;
#pragma unroll
      for (int i = 0; i < 16; ++i) { s[i] = __builtin_amdgcn_exp2f(fmaf(s[i], KSC, -mk)); ls += s[i]; }
      lds128w(L_P + (2 * w) * 1024 + lane * 16, v4u{pk(s[0], s[1]), pk(s[2], s[3]), pk(s[4], s[5]), pk(s[6], s[7])});
      lds128w(L_P + (2 * w + 1) * 1024 + lane * 16, v4u{pk(s[8], s[9]), pk(s[10], s[11]), pk(s[12], s[13]), pk(s[14], s[15])});
      ls = sum32(ls);
      if (hh == 0) *lp<float>(L_LSUM + hd * 16 + w * 4) = ls;
      const u32 blk = (lane >> 4) & 1;
      u32 ta[2];
#pragma unroll
      for (int t = 0; t < 2; ++t)
        ta[t] = L_C + (4 * hh + 8 * t + tq) * 1024 + 256 * w + 16 * ((2 * blk + (tp >> 1)) ^ ((tq << 2) | (hh + 2 * t))) + 8 * (tp & 1);
      v4u cf[32];
      constexpr int PV_PD = 5;
#define PV_RD(kk) cf[kk] = ldstr((ta[0] ^ (64 * ((kk) >> 3))) + ((kk) & 7) * 16384, (ta[1] ^ (64 * ((kk) >> 3))) + ((kk) & 7) * 16384)
#pragma unroll
      for (int k = 0; k < PV_PD; ++k) PV_RD(k);
      SB();
      STUB(14);
      bar();
      STAMP(5);
      v4u pf[8];
#pragma unroll
      for (int k = 0; k < 8; ++k) pf[k] = lds128(L_P + k * 1024 + lane * 16);
      const v4u lv = lds128(L_LSUM + hd * 16);
      SB();
      const u32 scw = L_SCR + w * 4096 + hd * 128 + 8 * hh + 16 * (hd & 7);
      const u32 scr = L_SCR + w * 4096 + (lane >> 3) * 128 + 16 * ((lane & 7) ^ (lane >> 3));
      const u32 sto = WS_APART + (((lane >> 3) * 4 + w) * 128 + bs) * 256 + 16 * (lane & 7);
      f32x16 o[4];
      v4u ov[4];
      u32 cs = tag;
#define EPI_W(c, gg) do { \
        const u32 p0 = pk(o[c][4 * (gg)], o[c][4 * (gg) + 1]), p1 = pk(o[c][4 * (gg) + 2], o[c][4 * (gg) + 3]); \
        lds64w(scw ^ (64 * ((c) & 1) + 16 * (gg)), p0, p1); cs += p0 + p1; asm volatile("" : "+v"(cs)); } while (0)
#define EPI_R() _Pragma("unroll") for (int i = 0; i < 4; ++i) ov[i] = lds128(scr + i * 1024);
#define EPI_S(p, i) ST1(ov[i], sto + 128 * (p) + (i) * APART_BLK)
#pragma unroll
      for (int kk = 0; kk < 32; ++kk) {
        const int c = kk >> 3, k = kk & 7;
        if (kk + PV_PD < 32) PV_RD(kk + PV_PD);
        if (k == 0) {
          f32x16 z;
#pragma unroll
          for (int i = 0; i < 16; ++i) z[i] = 0.f;
          o[c] = mfma32(cf[kk], pf[k], z);
        } else o[c] = mfma32(cf[kk], pf[k], o[c]);
        if (c > 0 && c != 2 && (k & 1) == 1) EPI_W(c > 0 ? c - 1 : 0, (k >> 1) & 3);
        if (c == 2 && k >= 1 && k <= 4) EPI_W(1, (k - 1) & 3);
        if (kk == 20) { EPI_R() }
        if (kk == 23) EPI_S(0, 0);
        if (kk == 24) EPI_S(0, 1);
        if (kk == 26) EPI_S(0, 2);
        if (kk == 28) EPI_S(0, 3);
        if (kk == 13) STUB(15);
        if (kk == 22) STUB(16);
        SB();
      }
      EPI_W(3, 0); EPI_W(3, 1); EPI_W(3, 2); EPI_W(3, 3); EPI_R()
      const float lsum = (f_(lv.x) + f_(lv.y)) + (f_(lv.z) + f_(lv.w));
      { auto t = __builtin_amdgcn_permlane32_swap(cs, cs, false, false); cs = t[0] + t[1] - tag + u_(mk) + u_(lsum); }
      EPI_S(1, 0); EPI_S(1, 1); EPI_S(1, 2); EPI_S(1, 3);
      if (hh == 0) ST1((v4u{u_(mk), u_(lsum), cs, tag}), WS_ASTAT + bs * 2048 + w * 512 + hd * 16);
      STAMP(7);
      bar();
    }
    const u32 v = wave;
    v4u a1[8], bq[4];
    STUB(17);
    const u64 tpv = __builtin_amdgcn_s_memrealtime();
    bar();
    if (wave >= 4) {
#pragma unroll
      for (int s = 0; s < 8; ++s) { const u32 a = L_W1 + (ab ^ (32 * (s >> 2))) + (s & 3) * 8192; a1[s] = ldstr(a, a + 1024); }
    }
    if (wave == 3) {
      while (__builtin_amdgcn_s_memrealtime() - tpv < PDELAY) { }
    }
    for (u32 it = 257; it < 320; ++it) {
      v4u st0, st1;
      if (wave == 3) {
        for (int spin = 0; spin < SPIN_CAP; ++spin) {
          st0 = LD1(WS_ASTAT + lane * 2048 + q4 * 512 + ht * 16);
          st1 = LD1(WS_ASTAT + 131072 + lane * 2048 + q4 * 512 + ht * 16);
          asm volatile("" ::: "memory");
          if (__all(st0.w == tag && st1.w == tag)) break;
        }
        STAMP(2);
      }
      if (SOON && wave == 3 && q4 == 0 && lane == 0) __builtin_amdgcn_raw_buffer_store_b32(tag, rs, WS_SOON + ht * 128, 0, 16);
      bar();
      STAMP(8);
      v4u pd[4];
#pragma unroll
      for (int i = 0; i < 4; ++i) pd[i] = LD1(WS_APART + (ht >> 3) * APART_BLK + ((ht & 7) * 4 + q4) * 32768 + v * 4096 + i * 1024 + lane * 16);
      SB();
      if (wave == 3) {
#pragma unroll
        for (int i = 0; i < 2; ++i) {
          const v4u st = i ? st1 : st0;
          float M = f_(st.x);
          M = fmaxf(M, dpp<X1>(M)); M = fmaxf(M, dpp<X2>(M)); M = fmaxf(M, dpp<X7>(M)); M = fmaxf(M, dpp<X8>(M)); M = max16(M); M = max32(M);
          const float e = __builtin_amdgcn_exp2f(f_(st.x) - M);
          float S = e * f_(st.y);
          S += dpp<X1>(S); S += dpp<X2>(S); S += dpp<X7>(S); S += dpp<X8>(S); S = sum16(S); S = sum32(S);
          *lp<float>(L_WGT + i * 256 + lane * 4) = e / S;
          *lp<u32>(L_CK + i * 256 + lane * 4) = st.z - tag - st.x - st.y;
        }
      }
      STUB(18);
      bar();
      float acc[8], wt[4];
#pragma unroll
      for (int i = 0; i < 4; ++i) wt[i] = *lp<float>(L_WGT + (v * 16 + 4 * i + g) * 4);
      const u32 ck = *lp<u32>(L_CK + (v * 16 + 4 * (lane & 3) + g) * 4);
#pragma unroll
      for (int k = 0; k < 4; ++k) { acc[2 * k] = wt[0] * blo(pd[0][k]); acc[2 * k + 1] = wt[0] * bhi(pd[0][k]); }
#pragma unroll
      for (int i = 1; i < 4; ++i)
#pragma unroll
        for (int k = 0; k < 4; ++k) { acc[2 * k] = fmaf(wt[i], blo(pd[i][k]), acc[2 * k]); acc[2 * k + 1] = fmaf(wt[i], bhi(pd[i][k]), acc[2 * k + 1]); }
      STAMP(9);
      {
        float y[4];
#pragma unroll
        for (int k = 0; k < 4; ++k) { auto t = __builtin_amdgcn_permlane32_swap(u_(acc[k]), u_(acc[k + 4]), false, false); y[k] = f_(t[0]) + f_(t[1]); }
        float z[2];
#pragma unroll
        for (int k = 0; k < 2; ++k) { auto t = __builtin_amdgcn_permlane16_swap(u_(y[k]), u_(y[k + 2]), false, false); z[k] = f_(t[0]) + f_(t[1]); }
        const u32 hi = pk(z[0], z[1]), lo = pk(z[0] - blo(hi), z[1] - bhi(hi));
        *lp<u32>(L_B1 + v * 272 + (8 * n + 2 * g) * 2) = hi;
        *lp<u32>(L_B1 + (8 + v) * 272 + (8 * n + 2 * g) * 2) = lo;
        u32 c4[4], c2[2];
#pragma unroll
        for (int i = 0; i < 4; ++i) c4[i] = (pd[i].x + pd[i].y) + (pd[i].z + pd[i].w);
#pragma unroll
        for (int i = 0; i < 2; ++i) { const u32 p = c4[i] + dppu<X2>(c4[i]), q = c4[i + 2] + dppu<X2>(c4[i + 2]); c2[i] = (lane & 2) ? q : p; }
        const u32 p1 = c2[0] + dppu<X1>(c2[0]), q1 = c2[1] + dppu<X1>(c2[1]);
        u32 c1 = (lane & 1) ? q1 : p1;
        c1 += dppu<X8>(c1);
        c1 += dppu<0x124>(c1);
        if (c1 != ck) *lp<u32>(L_BAD) = it;
      }
      STUB(19);
      bar();
      const u32 bad = *lp<u32>(L_BAD);
#pragma unroll
      for (int s = 0; s < 4; ++s) bq[s] = lds128(L_B1 + n * 272 + g * 16 + s * 64);
      SB();
      if (__builtin_amdgcn_readfirstlane(bad) != it) break;
    }
    if (wave >= 4) {
      f32x4 d0 = {0.f, 0.f, 0.f, 0.f}, d1 = d0;
#pragma unroll
      for (int s = 0; s < 4; ++s) { d0 = mfma16(a1[s], bq[s], d0); d1 = mfma16(a1[4 + s], bq[s], d1); }
#pragma unroll
      for (int k = 0; k < 4; ++k) {
        const float t = d0[k] + dpp<X1>(d0[k]), u = d1[k] + dpp<X1>(d1[k]);
        float z = (n & 1) ? u : t;
        z += dpp<X2>(z); z += dpp<X8>(z); d0[k] = z;
      }
      if ((n & 10) == 0) ST1((v4u{pk(d0[0], d0[1]), tag, pk(d0[2], d0[3]), tag}), WS_H3B + ht * 4096 + q4 * 1024 + (n >> 2) * 512 + (8 * w + 4 * (n & 1) + g) * 16);
    }
    STAMP(10);
    if (wave == 0) {
      const u32 rho = x * 8 + (l16 >> 1), part = l16 & 1;
      u16* outp = out; asm volatile("" : "+s"(outp));
      { const u64 t0 = __builtin_amdgcn_s_memrealtime(); while (__builtin_amdgcn_s_memrealtime() - t0 < (part ? 0 : FDELAY)) { } }
      v4u e[4];
      for (int spin = 0; spin < SPIN_CAP; ++spin) {
        bool ok = true;
#pragma unroll
        for (int i = 0; i < 4; ++i) e[i] = LD1(WS_H4 + part * 262144 + rho * 4096 + i * 1024 + lane * 16);
        asm volatile("" ::: "memory");
#pragma unroll
        for (int i = 0; i < 4; ++i) ok = ok && e[i].y == tag && e[i].w == tag;
        if (__all(ok)) break;
      }
      float s0 = (f_(e[0].x) + f_(e[1].x)) + (f_(e[2].x) + f_(e[3].x)), s1 = (f_(e[0].z) + f_(e[1].z)) + (f_(e[2].z) + f_(e[3].z));
      s0 += dpp<X8>(s0); s1 += dpp<X8>(s1);
      s0 = sum32(sum16(s0)); s1 = sum32(sum16(s1));
      if (lane < 8) *(GAS u32*)(outp + (rho & 1) * 1024 + part * 512 + (rho >> 1) * 16 + 2 * lane) = pk(s0, s1);
      if (gid == 0 && lane == 0) __builtin_amdgcn_raw_buffer_store_b32(tag, rs, WS_EPOCH, 0, 16);
      STAMP(13);
    }
  } else {
    if (wave < 4) {
      if (wave == 0) {
        STUB(2);
        SETWS(kb[0], kb[1]);
        __builtin_amdgcn_s_setprio(1);
        {
          const u32 uo = q4 * 1048576 + ht * 256 + g * 8192;
          u32 uv[4];
#pragma unroll
          for (int i = 0; i < 4; ++i) uv[i] = uo + ((n ^ (g | (i << 2))) * 16) + (i & 1) * 63488 + (i >> 1) * 131072;
          cptr uk = mkptr(kb[4], kb[5]);
#pragma unroll 1
          for (u32 H = 0; H < 4; ++H) {
            dma4r<32>(uk + H * 262144, uv[0], uv[0] + 31744, uv[1], uv[1] + 31744, L_UK + H * 8192);
            dma4r<32>(uk + H * 262144, uv[2], uv[2] + 31744, uv[3], uv[3] + 31744, L_UK + H * 8192 + 4096);
          }
        }
        vmwait<32>(); sig(S_WQ, lane);
        STAMP(14);
        vmwait<0>(); sig(S_UK, lane);
        STAMP(5);
        tag = __builtin_amdgcn_readfirstlane(TAGLOAD()) + 1;
        sig(S_TAG, lane);
        STUB(3);
        waitw<false>(S_PUB, 5);
        while (__builtin_amdgcn_s_memrealtime() - t0 < KSTART) __builtin_amdgcn_s_sleep(1);
        STAMP(13);
        const size_t base = (((size_t)(l16 >> 3) * 8 + x) * 8192 + 6144 + (l16 & 7) * (KVMIX ? 16 : 256)) * 256;
        constexpr u32 GS = KVMIX ? 32768 : 4096;
        cptr ks = mkptr(kb[6], kb[7]) + base, vs = mkptr(kb[8], kb[9]) + base;
        static_assert(WINB % 4 == 0 && NB0 <= 3 + WINB / 4, "block signals");
        {
          u32 kv[4];
#pragma unroll
          for (int i = 0; i < 4; ++i) kv[i] = g * 256 + ((n ^ (4 * i + g)) * 16);
#pragma unroll 1
          for (u32 G = 0; G < NB0; ++G) dma4r<WINB0>(ks + G * GS, kv[0], kv[1], kv[2], kv[3], L_K + G * 4096);
#pragma unroll 1
          for (u32 G = NB0; G < 16; ++G) {
            dma4r<WINB>(ks + G * GS, kv[0], kv[1], kv[2], kv[3], L_K + G * 4096);
            if (((G + 1 - WINB / 4) & 3) == 0 && G >= 3 + WINB / 4) { vmwait<WINB>(); sig(S_LAND, lane); }
          }
        }
        STAMP(1);
        {
          const u32 v0 = g * 256 + ((n ^ (g << 1)) * 16), v1 = g * 256 + ((n ^ ((4 + g) << 1)) * 16);
#pragma unroll 1
          for (u32 G = 0; G < 16; ++G) {
            if (ABL != 3) dma4r<WINB>(vs + G * GS, v0, v1, v0, v1, L_V + G * 4096);
            if (((G + 1 - WINB / 4) & 3) == 0 && G + 16 >= 3 + WINB / 4) { vmwait<WINB>(); sig(S_LAND, lane); }
          }
        }
        STAMP(2);
        waitw<false>(S_KRD, 4);
        {
          const u32 vo = (ht * 128 + g) * 1024 + q4 * 256, w0 = vo + ((n ^ (g << 1)) * 16), w1 = vo + ((n ^ ((g | 4) << 1)) * 16) + 6144;
#pragma unroll 1
          for (u32 G = 0; G < 16; ++G) {
            cptr wp = (G < 8 ? mkptr(kb[14], kb[15]) : mkptr(kb[12], kb[13])) + (G & 7) * 16384;
            if (ABL != 3) dma4r<WINB>(wp, w0, w0 + 3072, w1, w1 + 3072, L_W1 + G * 4096);
            if (((G + 1 - WINB / 4) & 3) == 0) { vmwait<WINB>(); sig(S_LAND, lane); }
          }
        }
        STAMP(3);
        vmwait<32>(); sig(S_W1, lane);
        vmwait<0>(); sig(S_W2, lane);
        STAMP(4);
      } else if (wave == 1) {
        START(3);
        const u64 t1 = __builtin_amdgcn_s_memrealtime();
        __builtin_amdgcn_s_setprio(2);
        asm volatile("" :: "v"(warm));
        TOUCH(4); TOUCH(3); TOUCH(5); TOUCH(6); TOUCH(7); TOUCH(8); TOUCH(9); TOUCH(10);
        if (lane < 6) { u32 t = __builtin_amdgcn_raw_buffer_load_b32(rs, lane < 2 ? lane * 1048576 + 262144 - 64 : WS_APART + (lane - 1) * APART_BLK - 128, 0, 16); asm volatile("" :: "v"(t)); }
        tag = __builtin_amdgcn_readfirstlane(TAGLOAD()) + 1;
        waitw<false>(S_TAG, 3);
        waitw<false>(S_PUB, 4);
        {
          const v4u pv = lds128(L_QX + (lane & 31) * 16);
          if (lane < 32) ST1(pv, WS_QABS + (lane >> 4) * 32768 + ht * 1024 + q4 * 256 + n * 16);
          u32 cs = (pv.x + pv.y) + (pv.z + pv.w);
          cs += dppu<X1>(cs); cs += dppu<X2>(cs); cs += dppu<X7>(cs); cs += dppu<X8>(cs);
          auto t = __builtin_amdgcn_permlane16_swap(cs, cs, false, false);
          if (lane == 0) ST1((v4u{t[0] + tag, t[1] + tag, tag, tag}), WS_HINT + (x * 16 + l16) * 128);
          if (l16 == GATEL && lane == 0) __builtin_amdgcn_raw_buffer_store_b32(tag, rs, WS_GATE + x * 128, 0, 16);
        }
        sig(S_PUB, lane);
        STAMP(2);
        while (__builtin_amdgcn_s_memrealtime() - t1 < QBSTART) __builtin_amdgcn_s_sleep(1);
        v4u e[16];
        for (int spin = 0; spin < SPIN_CAP; ++spin) {
          bool ok = true;
#pragma unroll
          for (int i = 0; i < 16; ++i) e[i] = LD1(WS_QB + x * 32768 + (l16 >> 3) * 16384 + i * 1024 + lane * 16);
          asm volatile("" ::: "memory");
#pragma unroll
          for (int i = 0; i < 16; ++i) ok = ok && e[i].y == tag && e[i].w == tag;
          if (__all(ok)) break;
        }
#pragma unroll
        for (int i = 0; i < 4; ++i)
          *lp<u32>(L_QBL + i * 256 + lane * 4) = pk((f_(e[i].x) + f_(e[4 + i].x)) + (f_(e[8 + i].x) + f_(e[12 + i].x)), (f_(e[i].z) + f_(e[4 + i].z)) + (f_(e[8 + i].z) + f_(e[12 + i].z)));
        sig(S_QBRDY, lane);
        STAMP(3);
      } else {
        START(4);
        asm volatile("" :: "v"(warm));
        tag = __builtin_amdgcn_readfirstlane(TAGLOAD()) + 1;
        sig(S_TAG, lane);
      }
      STUB(10);
      waitw<true>(S_W2, 1);
      v4u a2[8];
#pragma unroll
      for (int s = 0; s < 8; ++s) { const u32 a = L_W2 + (ab ^ (32 * (s >> 2))) + (s & 3) * 8192; a2[s] = ldstr(a, a + 1024); }
      if (SOON) {
        if (w == 2) {
          for (int spin = 0; spin < SPIN_CAP; ++spin) {
            const u32 f = __builtin_amdgcn_raw_buffer_load_b32(rs, WS_SOON + ht * 128, 0, 16);
            asm volatile("" ::: "memory");
            if (__builtin_amdgcn_readfirstlane(f) == tag) break;
          }
          const u64 ts = __builtin_amdgcn_s_memrealtime();
          while (__builtin_amdgcn_s_memrealtime() - ts < SOON) { }
          sig(S_QST, lane);
        }
        waitw<false>(S_QST, 1);
      }
      v4u e;
      for (int spin = 0; spin < SPIN_CAP; ++spin) {
        e = LD1(WS_H3B + ht * 4096 + w * 1024 + lane * 16);
        asm volatile("" ::: "memory");
        if (__all(e.y == tag && e.w == tag)) break;
      }
      lds64w(L_B2 + ((lane >> 5) * 4 + w) * 272 + (lane & 31) * 8, e.x, e.z);
      sig(S_TA, lane);
      waitw<false>(S_TA, 4);
      STAMP(11);
      v4u bq[4];
#pragma unroll
      for (int s = 0; s < 4; ++s) bq[s] = lds128(L_B2 + (n & 7) * 272 + g * 16 + s * 64);
      SB();
      f32x4 d0 = {0.f, 0.f, 0.f, 0.f}, d1 = d0;
#pragma unroll
      for (int s = 0; s < 4; ++s) { d0 = mfma16(a2[s], bq[s], d0); d1 = mfma16(a2[4 + s], bq[s], d1); }
      f32x4 d = (n & 8) ? d1 : d0;
#pragma unroll
      for (int k = 0; k < 4; ++k) { float t = d[k]; t += dpp<X1>(t); t += dpp<X2>(t); d[k] = t; }
      const bool k1 = (n & 1) != 0;
      if ((n & 2) == 0) ST1((v4u{u_(k1 ? d[2] : d[0]), tag, u_(k1 ? d[3] : d[1]), tag}),
                            WS_H4 + (((q4 * 8 + 2 * w + (n >> 3)) * 2 + ((n >> 2) & 1)) * 32 + ht) * 128 + g * 32 + (n & 1) * 16);
      STAMP(12);
    } else {
      START(5);
      tag = __builtin_amdgcn_readfirstlane(TAGLOAD()) + 1;
      __builtin_amdgcn_s_setprio(2);
      {
        waitw<false>(S_WQ, 1);
        STAMP(10);
        PROJ((void)0, WS_H1 + (4 * x + (((l16 >> 2) + 1) & 3)) * 8192 + q4 * 2048 + (n & 1) * 1024);
        STAMP(11);
        STUB(4);
        waitw<false>(S_UK, 1);
        v4u uk[8];
#pragma unroll
        for (int t = 0; t < 2; ++t)
#pragma unroll
          for (int s = 0; s < 4; ++s) uk[t * 4 + s] = lds128(L_UK + (32 * w + 8 * (n >> 2) + 4 * t + (n & 3)) * 256 + 16 * ((4 * s + g) ^ n));
        v4u e0, e1;
#define H1_LD() do { e0 = LD1(WS_H1 + ht * 8192 + g * 2048 + w * 256 + n * 16); e1 = LD1(WS_H1 + ht * 8192 + g * 2048 + 1024 + w * 256 + n * 16); \
                     asm volatile("" ::: "memory"); } while (0)
        H1_LD();
        for (int spin = 0; spin < SPIN_CAP; ++spin) {
          if (__all(e0.y == tag && e0.w == tag && e1.y == tag && e1.w == tag)) break;
          H1_LD();
        }
        STAMP(14);
        {
          auto p = __builtin_amdgcn_permlane32_swap(e0.x, e1.x, false, false), q = __builtin_amdgcn_permlane32_swap(e0.z, e1.z, false, false);
          const float a = sum16(f_(p[0]) + f_(p[1])), c = sum16(f_(q[0]) + f_(q[1]));
          if ((g & 1) == 0) *lp<u32>(L_QL + (g >> 1) * 256 + w * 64 + n * 4) = pk(a, c);
        }
        sig(S_H1, lane);
        waitw<false>(S_H1, 4);
        v4u qb[4];
#pragma unroll
        for (int s = 0; s < 4; ++s) qb[s] = lds128(L_QL + (lane & 1) * 256 + g * 16 + s * 64);
        SB();
        f32x4 u0 = {0.f, 0.f, 0.f, 0.f}, u1 = u0;
#pragma unroll
        for (int s = 0; s < 4; ++s) { u0 = mfma16(uk[s], qb[s], u0); u1 = mfma16(uk[4 + s], qb[s], u1); }
        const v4u pv = {pk(u0[0], u0[1]), pk(u0[2], u0[3]), pk(u1[0], u1[1]), pk(u1[2], u1[3])};
        if (n < 2) lds128w(L_QX + n * 256 + w * 64 + g * 16, pv);
        sig(S_PUB, lane);
        STAMP(15);
      }
      STUB(5);
      waitw<true>(S_LAND, w + 1);
      f32x4 sc[4];
      {
        v4u kf[16], qf[4];
        const u32 ka = L_K + (64 * w + n) * 256 + 16 * (g ^ n);
#pragma unroll
        for (int i = 0; i < 16; ++i) kf[i] = lds128((ka ^ (64 * (i & 3))) + (i >> 2) * 4096);
        sig(S_KRD, lane);
        SB();
        waitw<false>(S_QBRDY, 1);
        STAMP(3);
#pragma unroll
        for (int s = 0; s < 4; ++s) qf[s] = lds128(L_QBL + (lane & 3) * 256 + g * 16 + s * 64);
        SB();
#pragma unroll
        for (int t = 0; t < 4; ++t) {
          sc[t] = f32x4{0.f, 0.f, 0.f, 0.f};
#pragma unroll
          for (int s = 0; s < 4; ++s) sc[t] = mfma16(kf[4 * t + s], qf[s], sc[t]);
        }
      }
      float m = sc[0][0];
#pragma unroll
      for (int i = 1; i < 16; ++i) m = fmaxf(m, sc[i >> 2][i & 3]);
      m = max32(max16(m));
      if (lane < 4) *lp<float>(L_MAXX + lane * 16 + w * 4) = m;
      sig(S_MAX, lane);
      STUB(6);
      waitw<false>(S_MAX, 4);
      const v4u mm = lds128(L_MAXX + (lane & 3) * 16);
      const float mk = fmaxf(fmaxf(f_(mm.x), f_(mm.y)), fmaxf(f_(mm.z), f_(mm.w))) * KSC;
      float ls = 0.f;
#pragma unroll
      for (int i = 0; i < 16; ++i) { const float p = __builtin_amdgcn_exp2f(fmaf(sc[i >> 2][i & 3], KSC, -mk)); sc[i >> 2][i & 3] = p; ls += p; }
      ls = sum32(sum16(ls));
      if (n < 4) {
#pragma unroll
        for (int u = 0; u < 2; ++u)
          lds128w(L_PB + (2 * w + u) * 256 + g * 64 + n * 16,
                  v4u{pk(sc[2 * u][0], sc[2 * u][1]), pk(sc[2 * u][2], sc[2 * u][3]), pk(sc[2 * u + 1][0], sc[2 * u + 1][1]), pk(sc[2 * u + 1][2], sc[2 * u + 1][3])});
      }
      if (lane < 4) *lp<float>(L_LSUM + lane * 16 + w * 4) = ls;
      sig(S_P, lane);
      STUB(7);
      waitw<false>(S_P, 4);
      STAMP(5);
      v4u pf[8];
#pragma unroll
      for (int k = 0; k < 8; ++k) pf[k] = lds128(L_PB + k * 256 + g * 64 + (lane & 3) * 16);
      if (w == 0) {
        const v4u lv = lds128(L_LSUM + (lane & 3) * 16);
        const float lsum = (f_(lv.x) + f_(lv.y)) + (f_(lv.z) + f_(lv.w));
        if (lane < 4) ST1((v4u{u_(mk), tag, u_(lsum), tag}), WS_BIN + (4 * x + lane) * 9216 + 8192 + l16 * 16);
      }
      f32x4 o0 = {0.f, 0.f, 0.f, 0.f}, o1 = o0;
      const u32 va = L_V + (4 * g + tq) * 256 + 16 * ((4 * w + (tp >> 1)) ^ ((4 * (g & 1) + tq) << 1)) + 8 * (tp & 1);
#pragma unroll
      for (int k2 = 0; k2 < 4; ++k2) {
        waitw<false>(S_LAND, k2 + 5);
        v4u vf[4];
#pragma unroll
        for (int i = 0; i < 4; ++i) { const u32 a = (va ^ (32 * (i & 1))) + (2 * k2 + (i >> 1)) * 8192; vf[i] = ldstr(a, a + 4096); }
        SB();
        o0 = mfma16(vf[0], pf[2 * k2], o0); o1 = mfma16(vf[1], pf[2 * k2], o1);
        o0 = mfma16(vf[2], pf[2 * k2 + 1], o0); o1 = mfma16(vf[3], pf[2 * k2 + 1], o1);
      }
      STAMP(6);
      {
        const f32x4 o = (n & 4) ? o1 : o0;
        if (n < 8) ST1((v4u{pk(o[0], o[1]), tag, pk(o[2], o[3]), tag}), WS_BIN + (4 * x + (n & 3)) * 9216 + l16 * 512 + (4 * (2 * w + (n >> 2)) + g) * 16);
      }
      STAMP(7);
      STUB(8);
      waitw<false>(S_W1, 1);
      STAMP(8);
      v4u a1[8];
#pragma unroll
      for (int s = 0; s < 8; ++s) { const u32 a = L_W1 + (ab ^ (32 * (s >> 2))) + (s & 3) * 8192; a1[s] = ldstr(a, a + 1024); }
      v4u e0, e1, st;
      for (int spin = 0; spin < SPIN_CAP; ++spin) {
        e0 = LD1(WS_BIN + ht * 9216 + w * 2048 + lane * 16);
        e1 = LD1(WS_BIN + ht * 9216 + w * 2048 + 1024 + lane * 16);
        st = LD1(WS_BIN + ht * 9216 + 8192 + n * 16);
        asm volatile("" ::: "memory");
        if (__all(e0.y == tag && e0.w == tag && e1.y == tag && e1.w == tag && st.y == tag && st.w == tag)) break;
      }
      STAMP(9);
      lds64w(L_BB + (4 * w + (lane >> 5)) * 272 + (lane & 31) * 8, e0.x, e0.z);
      lds64w(L_BB + (4 * w + 2 + (lane >> 5)) * 272 + (lane & 31) * 8, e1.x, e1.z);
      sig(S_TB, lane);
      float M = f_(st.x);
      M = fmaxf(M, dpp<X1>(M)); M = fmaxf(M, dpp<X2>(M)); M = fmaxf(M, dpp<X7>(M));
      const float ee = __builtin_amdgcn_exp2f(f_(st.x) - M);
      float S = ee * f_(st.z);
      S += dpp<X1>(S); S += dpp<X2>(S); S += dpp<X7>(S);
      const float wt = ee / S;
      STUB(9);
      waitw<false>(S_TB, 4);
      v4u bq[4];
#pragma unroll
      for (int s = 0; s < 4; ++s) bq[s] = lds128(L_BB + n * 272 + g * 16 + s * 64);
      SB();
      f32x4 d0 = {0.f, 0.f, 0.f, 0.f}, d1 = d0;
#pragma unroll
      for (int s = 0; s < 4; ++s) { d0 = mfma16(a1[s], bq[s], d0); d1 = mfma16(a1[4 + s], bq[s], d1); }
#pragma unroll
      for (int k = 0; k < 4; ++k) {
        float t = d0[k] * wt, u = d1[k] * wt;
        t += dpp<X1>(t); u += dpp<X1>(u); t += dpp<X2>(t); u += dpp<X2>(u); t += dpp<X7>(t); u += dpp<X7>(u);
        d0[k] = (n & 2) ? u : t;
      }
      const bool k1 = (n & 1) != 0;
      if ((n & 4) == 0) ST1((v4u{u_(k1 ? d0[2] : d0[0]), tag, u_(k1 ? d0[3] : d0[1]), tag}),
                            WS_H4 + 262144 + (((q4 * 8 + 2 * w + ((n >> 1) & 1)) * 2 + (n >> 3)) * 32 + ht) * 128 + g * 32 + (n & 1) * 16);
      STAMP(12);
    }
  }
}

extern "C" void kernel_launch(void* const* d_in, const int* in_sizes, int n_in, void* d_out, int out_size, void* d_ws, size_t ws_size,
                              hipStream_t stream) {
  (void)hipFuncSetAttribute((const void*)fused_decode, hipFuncAttributeMaxDynamicSharedMemorySize, 163840);
  fused_decode<<<256, 512, 163840, stream>>>((const u16*)d_in[0], (const u16*)d_in[2], (const u16*)d_in[6], (const u16*)d_in[9],
                                             (unsigned char*)d_ws, (const u16*)d_in[1], (const u16*)d_in[3], (const u16*)d_in[7],
                                             (const u16*)d_in[8], (const u16*)d_in[4], (const u16*)d_in[5], (const u16*)d_in[10],
                                             (u16*)d_out);
}
```

```cpp
#include <hip/hip_runtime.h>
#include <hip/hip_bf16.h>

typedef unsigned u32;
typedef unsigned short u16;
typedef unsigned long long u64;
typedef u32 v4u __attribute__((ext_vector_type(4)));
typedef u32 v2u __attribute__((ext_vector_type(2)));
typedef short s16x4 __attribute__((ext_vector_type(4)));
typedef short bf16x8 __attribute__((ext_vector_type(8)));
typedef __bf16 bf16x2 __attribute__((ext_vector_type(2)));
typedef float f32x4 __attribute__((ext_vector_type(4)));
typedef float f32x16 __attribute__((ext_vector_type(16)));
#define GAS __attribute__((address_space(1)))
#define LAS __attribute__((address_space(3)))
#define DI __device__ __forceinline__
#define SB() __builtin_amdgcn_sched_barrier(0)

constexpr int WINA = 32;
constexpr int WINB = 24;
constexpr u32 THOLD = 45;
constexpr u32 KSTART = 450;
constexpr u32 QBSTART = 350;
constexpr u32 PDELAY = 70;
constexpr u32 FDELAY = 160;
constexpr u32 GATEL = 5;
constexpr u32 CODE_BYTES = 23552;

constexpr u32 WS_EPOCH = 0;
constexpr u32 WS_GATE = 4096;
constexpr u32 WS_HINT = 16384;
constexpr u32 WS_QABS = 65536;
constexpr u32 WS_ASTAT = 262144;
constexpr u32 WS_H3B = 524288;
constexpr u32 WS_BIN = 786432;
constexpr u32 WS_H4 = 1310720;
constexpr u32 WS_H1 = 1835008;
constexpr u32 WS_APART = 2097152;
constexpr u32 APART_BLK = 1048576 + 256;
constexpr u32 WS_QB = 6291456 + 4096;
constexpr u32 WS_BYTES = 6291456 + 4096 + 262144;

constexpr u32 L_C = 0, L_K = 0, L_V = 65536, L_UK = 32768, L_WQ = 98304;
constexpr u32 L_Q = 131072;
constexpr u32 L_X = L_Q + 16384, L_QL = L_Q + 18432, L_QX = L_Q + 20480, L_QBL = L_Q + 22528, L_PB = L_Q + 24576;
constexpr u32 L_P = L_Q, L_SCR = L_Q + 8192, L_LSUM = L_Q + 28672, L_EXP = L_Q + 29696, L_GOT = L_Q + 29824;
constexpr u32 L_MAXX = L_Q + 31744, L_SYNC = L_MAXX + 512, L_PTR = L_SYNC + 256;
constexpr u32 L_W1 = 0, L_W2 = 32768, L_WGT = 65536, L_CK = 66048, L_BAD = 66560, L_B1 = 67584;
constexpr u32 L_BB = L_Q, L_B2 = L_Q + 8192;
enum { S_WQ = 0, S_UK, S_PUB, S_TAG, S_H1, S_QBRDY, S_XRD, S_CISS, S_LAND, S_MAX, S_P, S_KRD, S_W1, S_W2, S_TB, S_TA, S_N };

constexpr float KSC = 0.08838834764831845f * 1.4426950408889634f;
constexpr int SPIN_CAP = 1 << 15;
constexpr u32 QTRIES = 1024;

template <u32 LDS> DI void dma1(const void* sb, u32 v) {
  asm volatile("s_mov_b32 m0, %2\n\ts_nop 0\n\tglobal_load_lds_dwordx4 %0, %1" :: "v"(v), "s"(sb), "i"(LDS) : "memory");
}
DI void dma4s(const void* sb, u32 v0, u32 v1, u32 v2, u32 v3, u32 lds) {
  asm volatile("s_mov_b32 m0, %5\n\ts_nop 0\n\tglobal_load_lds_dwordx4 %0, %4 sc1\n\tglobal_load_lds_dwordx4 %1, %4 offset:1024 sc1\n\t"
               "global_load_lds_dwordx4 %2, %4 offset:2048 sc1\n\tglobal_load_lds_dwordx4 %3, %4 offset:3072 sc1"
               :: "v"(v0), "v"(v1), "v"(v2), "v"(v3), "s"(sb), "s"(lds) : "memory");
}
template <int W> DI void dma4r(const void* sb, u32 v0, u32 v1, u32 v2, u32 v3, u32 lds) {
  asm volatile("s_mov_b32 m0, %5\n\ts_waitcnt vmcnt(%6)\n\tglobal_load_lds_dwordx4 %0, %4 nt\n\tglobal_load_lds_dwordx4 %1, %4 offset:1024 nt\n\t"
               "s_waitcnt vmcnt(%6)\n\tglobal_load_lds_dwordx4 %2, %4 offset:2048 nt\n\tglobal_load_lds_dwordx4 %3, %4 offset:3072 nt"
               :: "v"(v0), "v"(v1), "v"(v2), "v"(v3), "s"(sb), "s"(lds), "i"(W) : "memory");
}
typedef const unsigned char* cptr;
DI cptr mkptr(u32 lo, u32 hi) { return (cptr)((u64)hi << 32 | lo); }
template <int N> DI void vmwait() { asm volatile("s_waitcnt vmcnt(%0)" :: "i"(N) : "memory"); }
DI u32 pk(float a, float b) { bf16x2 v = {(__bf16)a, (__bf16)b}; return __builtin_bit_cast(u32, v); }
DI float blo(u32 x) { return __builtin_bit_cast(float, x << 16); }
DI float bhi(u32 x) { return __builtin_bit_cast(float, x & 0xffff0000u); }
template <class T> DI LAS T* lp(u32 a) { return (LAS T*)(size_t)a; }
DI v4u lds128(u32 a) { return *lp<v4u>(a); }
DI void lds128w(u32 a, v4u v) { *lp<v4u>(a) = v; }
DI void lds64w(u32 a, u32 x, u32 y) { *lp<v2u>(a) = v2u{x, y}; }
DI v4u ldstr(u32 a0, u32 a1) {
  s16x4 lo = __builtin_amdgcn_ds_read_tr16_b64_v4i16(lp<s16x4>(a0));
  s16x4 hi = __builtin_amdgcn_ds_read_tr16_b64_v4i16(lp<s16x4>(a1));
  v2u l = __builtin_bit_cast(v2u, lo), h = __builtin_bit_cast(v2u, hi);
  return v4u{l.x, l.y, h.x, h.y};
}
DI f32x4 mfma16(v4u a, v4u b, f32x4 c) { return __builtin_amdgcn_mfma_f32_16x16x32_bf16(__builtin_bit_cast(bf16x8, a), __builtin_bit_cast(bf16x8, b), c, 0, 0, 0); }
DI f32x16 mfma32(v4u a, v4u b, f32x16 c) { return __builtin_amdgcn_mfma_f32_32x32x16_bf16(__builtin_bit_cast(bf16x8, a), __builtin_bit_cast(bf16x8, b), c, 0, 0, 0); }
template <int CTRL> DI u32 dppu(u32 x) { return (u32)__builtin_amdgcn_mov_dpp((int)x, CTRL, 0xf, 0xf, true); }
DI float f_(u32 x) { return __builtin_bit_cast(float, x); }
DI u32 u_(float x) { return __builtin_bit_cast(u32, x); }
template <int CTRL> DI float dpp(float x) { return f_(dppu<CTRL>(u_(x))); }
constexpr int X1 = 0xB1, X2 = 0x4E, X7 = 0x141, X8 = 0x128;
DI float max3(float a, float b, float c) { float r; asm("v_max3_f32 %0, %1, %2, %3" : "=v"(r) : "v"(a), "v"(b), "v"(c)); return r; }
DI float sum32(float x) { auto t = __builtin_amdgcn_permlane32_swap(u_(x), u_(x), false, false); return f_(t[0]) + f_(t[1]); }
DI float max32(float x) { auto t = __builtin_amdgcn_permlane32_swap(u_(x), u_(x), false, false); return fmaxf(f_(t[0]), f_(t[1])); }
DI float sum16(float x) { auto t = __builtin_amdgcn_permlane16_swap(u_(x), u_(x), false, false); return f_(t[0]) + f_(t[1]); }
DI float max16(float x) { auto t = __builtin_amdgcn_permlane16_swap(u_(x), u_(x), false, false); return fmaxf(f_(t[0]), f_(t[1])); }
DI void sig(int word, int) {
  asm volatile("s_mov_b64 exec, 1\n\tds_add_u32 %0, %1\n\ts_mov_b64 exec, -1" :: "v"(L_SYNC + 4 * word), "v"(1u) : "memory");
}
template <bool SLEEP> DI void waitw(int word, u32 n) {
  u32 v, t, c = 1u << 20;
  asm volatile("1:\n\tds_read_b32 %0, %3\n\ts_waitcnt lgkmcnt(0)\n\tv_readfirstlane_b32 %1, %0\n\ts_cmp_ge_u32 %1, %4\n\ts_cbranch_scc1 2f\n\t"
               "s_sleep %5\n\ts_sub_u32 %2, %2, 1\n\ts_cbranch_scc0 1b\n2:"
               : "=&v"(v), "=&s"(t), "+s"(c) : "v"(L_SYNC + 4 * word), "s"(n), "i"(SLEEP ? 2 : 0) : "memory", "scc");
}
#define STUB(n) asm volatile("s_branch .Lsk" #n "\n.Lstub" #n ":\n\ts_setpc_b64 vcc\n.Lsk" #n ":")
#define TOUCH(n) asm volatile("s_call_b64 vcc, .Lstub" #n ::: "vcc")
#define BAR0(id) asm volatile("s_waitcnt lgkmcnt(0)\n\ts_barrier ; start " #id ::: "memory")
DI bool qbad(int lane) { return __any(*lp<u32>(L_EXP + (lane & 31) * 4) != *lp<u32>(L_GOT + (lane & 31) * 4)); }
DI void bar() { asm volatile("s_waitcnt lgkmcnt(0)\n\ts_barrier" ::: "memory"); }
#define LD1(off) __builtin_amdgcn_raw_buffer_load_b128(rs, (off), 0, 16)
#define ST1(v, off) __builtin_amdgcn_raw_buffer_store_b128((v), rs, (off), 0, 16)
#define TAGLOAD() __builtin_amdgcn_raw_buffer_load_b32(rs, WS_EPOCH, 0, 16)
DI v4u ldnt(const void* p, u32 off) { return __builtin_nontemporal_load((const GAS v4u*)((const GAS unsigned char*)p + off)); }
DI void stage_w(u32 base, const v4u (&park)[32], u32 g, u32 n) {
#pragma unroll
  for (int i = 0; i < 32; ++i) lds128w(base + g * 256 + i * 1024 + 16 * (n ^ ((g | (((i >> 1) & 1) << 2)) << 1)), park[i]);
}

__global__ void __launch_bounds__(512, 2)
fused_decode(const u16* __restrict__ xA, const u16* __restrict__ wqA, const u16* __restrict__ xB, const u16* __restrict__ wqB,
             unsigned char* ws_, const u16* __restrict__ ckv, const u16* __restrict__ wuk, const u16* __restrict__ kc,
             const u16* __restrict__ vc, const u16* __restrict__ wuv, const u16* __restrict__ woA, const u16* __restrict__ woB,
             u16* __restrict__ out) {
  typedef u32 s8u __attribute__((ext_vector_type(8)));
  typedef u32 s16u __attribute__((ext_vector_type(16)));
  const int tid = threadIdx.x, lane = tid & 63, wave = __builtin_amdgcn_readfirstlane(tid >> 6);
  s8u ka;
  s16u kb;
  if (wave == 0) asm volatile("s_load_dwordx8 %0, %2, 0x0\n\ts_load_dwordx16 %1, %2, 0x20" : "=&s"(ka), "=&s"(kb) : "s"(__builtin_amdgcn_kernarg_segment_ptr()));
  u64 pc0; asm volatile("s_getpc_b64 %0" : "=s"(pc0));
  const u32 gid = blockIdx.x, x = gid & 7, r = gid >> 3;
  const bool isA = (r & 4) == 0;
  const u32 l16 = (r & 3) | ((r >> 3) << 2);
  const u32 ht = 4 * x + (l16 >> 2), q4 = l16 & 3;
  const u32 g = lane >> 4, n = lane & 15, tq = n >> 2, tp = lane & 3;
  u32 warm = 0;
  u64 t0 = 0;
  unsigned char* ws;
  if (wave == 0) {
    const u32 vo = q4 * 1048576 + (((l16 >> 2) + 1) & 3) * 256 + x * 1024 + g * 8192;
    const u32 ce = vo + ((n ^ (g << 1)) * 16), cd = vo + ((n ^ ((g | 4) << 1)) * 16) + 63488;
    asm volatile("s_waitcnt lgkmcnt(0)" : "+s"(ka), "+s"(kb));
    asm volatile("s_memrealtime %0" : "=s"(t0));
    if (isA) {
      asm volatile("s_waitcnt lgkmcnt(0)" : "+s"(t0));
      while (__builtin_amdgcn_s_memrealtime() - t0 < THOLD) { }
    }
    dma1<L_X>(isA ? mkptr(ka[4], ka[5]) : mkptr(ka[0], ka[1]), (g & 1) * 1024 + q4 * 256 + n * 16);
    cptr wq = isA ? mkptr(ka[6], ka[7]) : mkptr(ka[2], ka[3]);
#pragma unroll 1
    for (u32 G = 0; G < 8; ++G) dma4r<63>(wq + G * 131072, ce, ce + 31744, cd, cd + 31744, L_WQ + G * 4096);
    if (lane < 2) *lp<u32>(L_PTR + 4 * lane) = lane ? kb[1] : kb[0];
    BAR0(0);
    asm volatile("s_waitcnt lgkmcnt(0)" : "+s"(t0));
  } else if (wave == 1) {
    const u32 ln = (r & 3) * 64 + lane;
    warm = ((const GAS u32*)(pc0 & ~127ull))[(ln < CODE_BYTES / 128 ? ln : 0u) * 32];
    SB();
  } else if (wave == 2) {
    if (lane < S_N) *lp<u32>(L_SYNC + 4 * lane) = 0;
  } else if (wave == 3) {
    s16u kc3;
    asm volatile("s_load_dwordx16 %0, %1, 0x20" : "=s"(kc3) : "s"(__builtin_amdgcn_kernarg_segment_ptr()));
    if (isA) TOUCH(1); else TOUCH(2);
    {
      asm volatile("s_waitcnt lgkmcnt(0)" : "+s"(kc3));
      const size_t kvb = (((size_t)(l16 >> 3) * 8 + x) * 8192 + 6144 + (l16 & 7) * 256) * 256;
      cptr p0 = isA ? mkptr(kc3[2], kc3[3]) + (size_t)(x * 16 + l16) * 131072 : mkptr(kc3[6], kc3[7]) + kvb;
      cptr p1 = isA ? mkptr(kc3[10], kc3[11]) + q4 * 1048576 + ht * 256 : mkptr(kc3[8], kc3[9]) + kvb;
      cptr p2 = isA ? p1 + 1040384 : mkptr(kc3[12], kc3[13]) + ht * 131072 + q4 * 256;
      cptr p3 = isA ? p0 + 130944 : mkptr(kc3[14], kc3[15]) + ht * 131072 + q4 * 256;
      cptr pp = (lane & 2) ? ((lane & 1) ? p3 : p2) : ((lane & 1) ? p1 : p0);
      if (isA && (lane & 3) == 0) pp = mkptr(kc3[2], kc3[3]) + (size_t)(x >> 2) * 8388608 + ((x & 3) * 16 + l16) * 16384 + ((lane >> 2) & 7) * 1048576;
      warm = __builtin_nontemporal_load((const GAS u32*)pp);
      SB();
    }
  }
  __amdgpu_buffer_rsrc_t rs;
#define SETWS(lo, hi) do { ws = (unsigned char*)mkptr(lo, hi); rs = __builtin_amdgcn_make_buffer_rsrc((void*)ws, 0, WS_BYTES, 0x00020000); } while (0)
#define START(id) do { BAR0(id); const v2u p_ = *lp<v2u>(L_PTR); SETWS(__builtin_amdgcn_readfirstlane(p_.x), __builtin_amdgcn_readfirstlane(p_.y)); } while (0)
  u32 tag;
  const u32 w = wave & 3;
  const u32 ab = (8 * g + tq) * 256 + 16 * ((4 * w + (tp >> 1)) ^ ((tq | ((g & 1) << 2)) << 1)) + 8 * (tp & 1);

#define PROJ(BEFORE_STORE, DST) do { \
    v4u xf[4], af[8]; \
    _Pragma("unroll") for (int s = 0; s < 4; ++s) xf[s] = lds128(L_X + (lane & 1) * 256 + g * 16 + s * 64); \
    _Pragma("unroll") for (int s = 0; s < 8; ++s) { const u32 a = L_WQ + (ab ^ (32 * (s & 1))) + (s >> 1) * 8192; af[s] = ldstr(a, a + 1024); } \
    SB(); \
    f32x4 c0 = {0.f, 0.f, 0.f, 0.f}, c1 = c0; \
    _Pragma("unroll") for (int s = 0; s < 4; ++s) { c0 = mfma16(af[2 * s], xf[s], c0); c1 = mfma16(af[2 * s + 1], xf[s], c1); } \
    const f32x4 c = (n & 4) ? c1 : c0; \
    BEFORE_STORE; \
    if (n < 8) ST1((v4u{u_((n & 2) ? c[2] : c[0]), tag, u_((n & 2) ? c[3] : c[1]), tag}), (DST) + (16 * w + 8 * (n >> 2) + 2 * g + ((n >> 1) & 1)) * 16); \
  } while (0)

  if (isA) {
    if (wave == 0) {
      STUB(1);
      SETWS(kb[0], kb[1]);
      __builtin_amdgcn_s_setprio(1);
      cptr src = mkptr(kb[2], kb[3]) + (size_t)(x >> 2) * 8388608 + ((x & 3) * 16 + l16) * 16384;
      u32 cv[16];
#pragma unroll
      for (int i = 0; i < 16; ++i) cv[i] = (lane ^ (((i & 3) << 2) | (i >> 2))) * 16;
#pragma unroll 1
      for (u32 H = 0; H < 8; ++H) {
        if (H == 6) { waitw<false>(S_XRD, 6); sig(S_CISS, lane); }
#pragma unroll
        for (int G = 0; G < 4; ++G) {
          dma4r<WINA>(src + H * 1048576 + G * 4096, cv[4 * G], cv[4 * G + 1], cv[4 * G + 2], cv[4 * G + 3], L_C + H * 16384 + G * 4096);
          constexpr int WQAT = WINA / 4 - 1;
          if (G == (WQAT & 3) && H == WQAT / 4) { vmwait<WINA>(); sig(S_WQ, lane); }
        }
      }
      sig(S_CISS, lane);
      vmwait<0>();
      bar();
      v4u park[32];
      const u32 vo = (128 * q4 + g) * 8192 + ht * 256 + n * 16;
#pragma unroll
      for (int i = 0; i < 32; ++i) park[i] = ldnt(mkptr(kb[10], kb[11]), vo + i * 32768);
      const u32 tv = TAGLOAD();
      SB();
      for (u32 it = 1; it <= QTRIES; ++it) {
        bar(); bar();
        if (!qbad(lane) || it == QTRIES) break;
        bar(); bar();
      }
      bar(); bar();
      stage_w(L_W1, park, g, n);
      tag = __builtin_amdgcn_readfirstlane(tv) + 1;
    } else if (wave < 4) {
      START(1);
      if (wave == 1) {
        asm volatile("" :: "v"(warm));
        TOUCH(11); TOUCH(12); TOUCH(13); TOUCH(14); TOUCH(15); TOUCH(16); TOUCH(17); TOUCH(18); TOUCH(19);
        if (lane < 6) { u32 t = __builtin_amdgcn_raw_buffer_load_b32(rs, lane < 2 ? lane * 1048576 + 262144 - 64 : WS_APART + (lane - 1) * APART_BLK - 128, 0, 16); asm volatile("" :: "v"(t)); }
      }
      if (wave != 2) sig(S_XRD, lane);
      asm volatile("" :: "v"(warm));
      tag = __builtin_amdgcn_readfirstlane(TAGLOAD()) + 1;
      v4u h0, h1;
      cptr qs = ws + WS_QABS + (x >> 2) * 32768;
#define Q_ISSUE() _Pragma("unroll 1") for (u32 G = 0; G < 8; ++G) { const u32 l0 = (lane ^ (G & 3)) * 16; \
          dma4s(qs + G * 4096, l0, l0 ^ 64, l0 ^ 128, l0 ^ 192, L_Q + G * 4096); }
#define HINT_LD(p, q) do { p = LD1(WS_HINT + lane * 128); q = LD1(WS_HINT + 8192 + lane * 128); asm volatile("" ::: "memory"); } while (0)
      if (wave == 2) {
        __builtin_amdgcn_s_setprio(2);
        waitw<true>(S_CISS, 1);
#pragma unroll 1
        for (int spin = 0; spin < 64; ++spin) {
          const u32 gt = __builtin_amdgcn_raw_buffer_load_b32(rs, WS_GATE + (lane & 7) * 128, 0, 16);
          asm volatile("" ::: "memory");
          const bool ok = __all(gt == tag);
          if (spin == 0) waitw<false>(S_CISS, 2);
          if (ok) break;
        }
        {
          asm volatile("global_load_dwordx4 %0, %2, %4 sc1\n\tglobal_load_dwordx4 %1, %3, %4 sc1"
                       : "=&v"(h0), "=&v"(h1) : "v"(WS_HINT + lane * 128), "v"(WS_HINT + 8192 + lane * 128), "s"(ws) : "memory");
          Q_ISSUE()
          asm volatile("s_waitcnt vmcnt(32)" : "+v"(h0), "+v"(h1) :: "memory");
        }
        if (!__all(h0.z == tag && h1.z == tag)) {
#pragma unroll 1
          for (int spin = 0; spin < SPIN_CAP; ++spin) {
            if (__all(h0.z == tag && h1.z == tag)) break;
            HINT_LD(h0, h1);
          }
          Q_ISSUE()
        }
        vmwait<0>();
      }
      bar();
      for (u32 it = 1; it <= QTRIES; ++it) {
        bar();
        if (wave == 2) {
          u32 e0 = (x & 4 ? h0.y : h0.x) - tag, e1 = (x & 4 ? h1.y : h1.x) - tag;
          e0 += dppu<X1>(e0); e1 += dppu<X1>(e1); e0 += dppu<X2>(e0); e1 += dppu<X2>(e1);
          if (tp < 2) *lp<u32>(L_EXP + (lane >> 2) * 4 + tp * 64) = tp ? e1 : e0;
        }
        bar();
        if (!qbad(lane) || it == QTRIES) break;
        bar();
        if (wave == 2) { HINT_LD(h0, h1); Q_ISSUE() vmwait<0>(); }
        bar();
      }
      bar(); bar();
      if (wave == 1) *lp<u32>(L_BAD) = 0;
    } else {
      START(2);
      tag = __builtin_amdgcn_readfirstlane(TAGLOAD()) + 1;
      const u32 bs = x * 16 + l16, b = bs >> 6, hh = lane >> 5, hd = lane & 31;
      __builtin_amdgcn_s_setprio(2);
      waitw<false>(S_WQ, 1);
      PROJ(sig(S_XRD, lane), WS_QB + x * 32768 + (n & 1) * 16384 + (q4 * 4 + (((l16 >> 2) + 1) & 3)) * 1024);
      STUB(11);
      f32x16 s;
      v4u mm;
      for (u32 it = 1; it <= QTRIES; ++it) {
        u32 cs = 0;
        bar();
#pragma unroll
        for (int i = 0; i < 16; ++i) s[i] = 0.f;
        {
          const u32 fr = ((hd & 3) << 2) | ((hd >> 2) & 3);
          const u32 qa = L_Q + hd * 1024 + 16 * (hh ^ fr), ca = qa - L_Q + L_C + w * 32768;
          v4u af[32], bf[32];
          constexpr int PD = 6;
#define S_RD(k) do { bf[k] = lds128((qa ^ (32 * ((k) & 7))) + 256 * ((k) >> 3)); af[k] = lds128((ca ^ (32 * ((k) & 7))) + 256 * ((k) >> 3)); } while (0)
#pragma unroll
          for (int k = 0; k < PD; ++k) S_RD(k);
          SB();
#pragma unroll
          for (int k = 0; k < 32; ++k) {
            if (k + PD < 32) S_RD(k + PD);
            s = mfma32(af[k], bf[k], s);
            cs = cs + bf[k].x + bf[k].y; cs = cs + bf[k].z + bf[k].w;
            if (k == 4) STUB(12);
            if (k == 26) { STUB(13); }
            if (k == 29) bar();
            SB();
          }
        }
        { auto t = __builtin_amdgcn_permlane32_swap(cs, cs, false, false); cs = t[0] + t[1]; }
        asm volatile("s_nop 11" : "+v"(s));
        float m = max3(max3(max3(s[0], s[1], s[2]), max3(s[3], s[4], s[5]), max3(s[6], s[7], s[8])),
                       max3(max3(s[9], s[10], s[11]), max3(s[12], s[13], s[14]), s[15]), s[15]);
        m = max32(m);
        if (hh == 0) *lp<float>(L_MAXX + hd * 16 + w * 4) = m;
        if (w == 0 && hh == 0) *lp<u32>(L_GOT + hd * 4) = cs;
        bar();
        mm = lds128(L_MAXX + hd * 16);
        if (!qbad(lane) || it == QTRIES) break;
        bar();
      }
      const float mk = fmaxf(fmaxf(f_(mm.x), f_(mm.y)), fmaxf(f_(mm.z), f_(mm.w))) * KSC;
      float ls = 0.f;
#pragma unroll
      for (int i = 0; i < 16; ++i) { s[i] = __builtin_amdgcn_exp2f(fmaf(s[i], KSC, -mk)); ls += s[i]; }
      lds128w(L_P + (2 * w) * 1024 + lane * 16, v4u{pk(s[0], s[1]), pk(s[2], s[3]), pk(s[4], s[5]), pk(s[6], s[7])});
      lds128w(L_P + (2 * w + 1) * 1024 + lane * 16, v4u{pk(s[8], s[9]), pk(s[10], s[11]), pk(s[12], s[13]), pk(s[14], s[15])});
      ls = sum32(ls);
      if (hh == 0) *lp<float>(L_LSUM + hd * 16 + w * 4) = ls;
      const u32 blk = (lane >> 4) & 1;
      u32 ta[2];
#pragma unroll
      for (int t = 0; t < 2; ++t)
        ta[t] = L_C + (4 * hh + 8 * t + tq) * 1024 + 256 * w + 16 * ((2 * blk + (tp >> 1)) ^ ((tq << 2) | (hh + 2 * t))) + 8 * (tp & 1);
      v4u cf[32];
      constexpr int PV_PD = 5;
#define PV_RD(kk) cf[kk] = ldstr((ta[0] ^ (64 * ((kk) >> 3))) + ((kk) & 7) * 16384, (ta[1] ^ (64 * ((kk) >> 3))) + ((kk) & 7) * 16384)
#pragma unroll
      for (int k = 0; k < PV_PD; ++k) PV_RD(k);
      SB();
      STUB(14);
      bar();
      v4u pf[8];
#pragma unroll
      for (int k = 0; k < 8; ++k) pf[k] = lds128(L_P + k * 1024 + lane * 16);
      const v4u lv = lds128(L_LSUM + hd * 16);
      SB();
      const u32 scw = L_SCR + w * 4096 + hd * 128 + 8 * hh + 16 * (hd & 7);
      const u32 scr = L_SCR + w * 4096 + (lane >> 3) * 128 + 16 * ((lane & 7) ^ (lane >> 3));
      const u32 sto = WS_APART + (((lane >> 3) * 4 + w) * 128 + bs) * 256 + 16 * (lane & 7);
      f32x16 o[4];
      v4u ov[4];
      u32 cs = tag;
#define EPI_W(c, gg) do { \
        const u32 p0 = pk(o[c][4 * (gg)], o[c][4 * (gg) + 1]), p1 = pk(o[c][4 * (gg) + 2], o[c][4 * (gg) + 3]); \
        lds64w(scw ^ (64 * ((c) & 1) + 16 * (gg)), p0, p1); cs += p0 + p1; asm volatile("" : "+v"(cs)); } while (0)
#define EPI_R() _Pragma("unroll") for (int i = 0; i < 4; ++i) ov[i] = lds128(scr + i * 1024);
#define EPI_S(p, i) ST1(ov[i], sto + 128 * (p) + (i) * APART_BLK)
#pragma unroll
      for (int kk = 0; kk < 32; ++kk) {
        const int c = kk >> 3, k = kk & 7;
        if (kk + PV_PD < 32) PV_RD(kk + PV_PD);
        if (k == 0) {
          f32x16 z;
#pragma unroll
          for (int i = 0; i < 16; ++i) z[i] = 0.f;
          o[c] = mfma32(cf[kk], pf[k], z);
        } else o[c] = mfma32(cf[kk], pf[k], o[c]);
        if (c > 0 && c != 2 && (k & 1) == 1) EPI_W(c > 0 ? c - 1 : 0, (k >> 1) & 3);
        if (c == 2 && k >= 1 && k <= 4) EPI_W(1, (k - 1) & 3);
        if (kk == 20) { EPI_R() }
        if (kk == 23) EPI_S(0, 0);
        if (kk == 24) EPI_S(0, 1);
        if (kk == 26) EPI_S(0, 2);
        if (kk == 28) EPI_S(0, 3);
        if (kk == 13) STUB(15);
        if (kk == 22) STUB(16);
        SB();
      }
      EPI_W(3, 0); EPI_W(3, 1); EPI_W(3, 2); EPI_W(3, 3); EPI_R()
      const float lsum = (f_(lv.x) + f_(lv.y)) + (f_(lv.z) + f_(lv.w));
      { auto t = __builtin_amdgcn_permlane32_swap(cs, cs, false, false); cs = t[0] + t[1] - tag + u_(mk) + u_(lsum); }
      EPI_S(1, 0); EPI_S(1, 1); EPI_S(1, 2); EPI_S(1, 3);
      if (hh == 0) ST1((v4u{u_(mk), u_(lsum), cs, tag}), WS_ASTAT + bs * 2048 + w * 512 + hd * 16);
      bar();
    }
    const u32 v = wave;
    v4u a1[8], bq[4];
    STUB(17);
    const u64 tpv = __builtin_amdgcn_s_memrealtime();
    bar();
    if (wave >= 4) {
#pragma unroll
      for (int s = 0; s < 8; ++s) { const u32 a = L_W1 + (ab ^ (32 * (s >> 2))) + (s & 3) * 8192; a1[s] = ldstr(a, a + 1024); }
    }
    if (wave == 3) {
      while (__builtin_amdgcn_s_memrealtime() - tpv < PDELAY) { }
    }
    for (u32 it = 1; it <= QTRIES; ++it) {
      v4u st0, st1;
      if (wave == 3) {
        for (int spin = 0; spin < SPIN_CAP; ++spin) {
          st0 = LD1(WS_ASTAT + lane * 2048 + q4 * 512 + ht * 16);
          st1 = LD1(WS_ASTAT + 131072 + lane * 2048 + q4 * 512 + ht * 16);
          asm volatile("" ::: "memory");
          if (__all(st0.w == tag && st1.w == tag)) break;
        }
      }
      bar();
      v4u pd[4];
#pragma unroll
      for (int i = 0; i < 4; ++i) pd[i] = LD1(WS_APART + (ht >> 3) * APART_BLK + ((ht & 7) * 4 + q4) * 32768 + v * 4096 + i * 1024 + lane * 16);
      SB();
      if (wave == 3) {
#pragma unroll
        for (int i = 0; i < 2; ++i) {
          const v4u st = i ? st1 : st0;
          float M = f_(st.x);
          M = fmaxf(M, dpp<X1>(M)); M = fmaxf(M, dpp<X2>(M)); M = fmaxf(M, dpp<X7>(M)); M = fmaxf(M, dpp<X8>(M)); M = max16(M); M = max32(M);
          const float e = __builtin_amdgcn_exp2f(f_(st.x) - M);
          float S = e * f_(st.y);
          S += dpp<X1>(S); S += dpp<X2>(S); S += dpp<X7>(S); S += dpp<X8>(S); S = sum16(S); S = sum32(S);
          *lp<float>(L_WGT + i * 256 + lane * 4) = e / S;
          *lp<u32>(L_CK + i * 256 + lane * 4) = st.z - tag - st.x - st.y;
        }
      }
      STUB(18);
      bar();
      float acc[8], wt[4];
#pragma unroll
      for (int i = 0; i < 4; ++i) wt[i] = *lp<float>(L_WGT + (v * 16 + 4 * i + g) * 4);
      const u32 ck = *lp<u32>(L_CK + (v * 16 + 4 * (lane & 3) + g) * 4);
#pragma unroll
      for (int k = 0; k < 4; ++k) { acc[2 * k] = wt[0] * blo(pd[0][k]); acc[2 * k + 1] = wt[0] * bhi(pd[0][k]); }
#pragma unroll
      for (int i = 1; i < 4; ++i)
#pragma unroll
        for (int k = 0; k < 4; ++k) { acc[2 * k] = fmaf(wt[i], blo(pd[i][k]), acc[2 * k]); acc[2 * k + 1] = fmaf(wt[i], bhi(pd[i][k]), acc[2 * k + 1]); }
      {
        float y[4];
#pragma unroll
        for (int k = 0; k < 4; ++k) { auto t = __builtin_amdgcn_permlane32_swap(u_(acc[k]), u_(acc[k + 4]), false, false); y[k] = f_(t[0]) + f_(t[1]); }
        float z[2];
#pragma unroll
        for (int k = 0; k < 2; ++k) { auto t = __builtin_amdgcn_permlane16_swap(u_(y[k]), u_(y[k + 2]), false, false); z[k] = f_(t[0]) + f_(t[1]); }
        const u32 hi = pk(z[0], z[1]), lo = pk(z[0] - blo(hi), z[1] - bhi(hi));
        *lp<u32>(L_B1 + v * 272 + (8 * n + 2 * g) * 2) = hi;
        *lp<u32>(L_B1 + (8 + v) * 272 + (8 * n + 2 * g) * 2) = lo;
        u32 c4[4], c2[2];
#pragma unroll
        for (int i = 0; i < 4; ++i) c4[i] = (pd[i].x + pd[i].y) + (pd[i].z + pd[i].w);
#pragma unroll
        for (int i = 0; i < 2; ++i) { const u32 p = c4[i] + dppu<X2>(c4[i]), q = c4[i + 2] + dppu<X2>(c4[i + 2]); c2[i] = (lane & 2) ? q : p; }
        const u32 p1 = c2[0] + dppu<X1>(c2[0]), q1 = c2[1] + dppu<X1>(c2[1]);
        u32 c1 = (lane & 1) ? q1 : p1;
        c1 += dppu<X8>(c1);
        c1 += dppu<0x124>(c1);
        if (c1 != ck) *lp<u32>(L_BAD) = it;
      }
      STUB(19);
      bar();
      const u32 bad = *lp<u32>(L_BAD);
#pragma unroll
      for (int s = 0; s < 4; ++s) bq[s] = lds128(L_B1 + n * 272 + g * 16 + s * 64);
      SB();
      if (__builtin_amdgcn_readfirstlane(bad) != it) break;
    }
    if (wave >= 4) {
      f32x4 d0 = {0.f, 0.f, 0.f, 0.f}, d1 = d0;
#pragma unroll
      for (int s = 0; s < 4; ++s) { d0 = mfma16(a1[s], bq[s], d0); d1 = mfma16(a1[4 + s], bq[s], d1); }
#pragma unroll
      for (int k = 0; k < 4; ++k) {
        const float t = d0[k] + dpp<X1>(d0[k]), u = d1[k] + dpp<X1>(d1[k]);
        float z = (n & 1) ? u : t;
        z += dpp<X2>(z); z += dpp<X8>(z); d0[k] = z;
      }
      if ((n & 10) == 0) ST1((v4u{pk(d0[0], d0[1]), tag, pk(d0[2], d0[3]), tag}), WS_H3B + ht * 4096 + q4 * 1024 + (n >> 2) * 512 + (8 * w + 4 * (n & 1) + g) * 16);
    }
    if (wave == 0) {
      const u32 rho = x * 8 + (l16 >> 1), part = l16 & 1;
      u16* outp = out; asm volatile("" : "+s"(outp));
      { const u64 t0 = __builtin_amdgcn_s_memrealtime(); while (__builtin_amdgcn_s_memrealtime() - t0 < (part ? 0 : FDELAY)) { } }
      v4u e[4];
      for (int spin = 0; spin < SPIN_CAP; ++spin) {
        bool ok = true;
#pragma unroll
        for (int i = 0; i < 4; ++i) e[i] = LD1(WS_H4 + part * 262144 + rho * 4096 + i * 1024 + lane * 16);
        asm volatile("" ::: "memory");
#pragma unroll
        for (int i = 0; i < 4; ++i) ok = ok && e[i].y == tag && e[i].w == tag;
        if (__all(ok)) break;
      }
      float s0 = (f_(e[0].x) + f_(e[1].x)) + (f_(e[2].x) + f_(e[3].x)), s1 = (f_(e[0].z) + f_(e[1].z)) + (f_(e[2].z) + f_(e[3].z));
      s0 += dpp<X8>(s0); s1 += dpp<X8>(s1);
      s0 = sum32(sum16(s0)); s1 = sum32(sum16(s1));
      if (lane < 8) __hip_atomic_store((GAS u32*)(outp + (rho & 1) * 1024 + part * 512 + (rho >> 1) * 16 + 2 * lane), pk(s0, s1), __ATOMIC_RELAXED, __HIP_MEMORY_SCOPE_AGENT);
      if (gid == 0 && lane == 0) __builtin_amdgcn_raw_buffer_store_b32(tag, rs, WS_EPOCH, 0, 16);
    }
  } else {
    if (wave < 4) {
      if (wave == 0) {
        STUB(2);
        SETWS(kb[0], kb[1]);
        __builtin_amdgcn_s_setprio(1);
        {
          const u32 uo = q4 * 1048576 + ht * 256 + g * 8192;
          u32 uv[4];
#pragma unroll
          for (int i = 0; i < 4; ++i) uv[i] = uo + ((n ^ (g | (i << 2))) * 16) + (i & 1) * 63488 + (i >> 1) * 131072;
          cptr uk = mkptr(kb[4], kb[5]);
#pragma unroll 1
          for (u32 H = 0; H < 4; ++H) {
            dma4r<32>(uk + H * 262144, uv[0], uv[0] + 31744, uv[1], uv[1] + 31744, L_UK + H * 8192);
            dma4r<32>(uk + H * 262144, uv[2], uv[2] + 31744, uv[3], uv[3] + 31744, L_UK + H * 8192 + 4096);
          }
        }
        vmwait<32>(); sig(S_WQ, lane);
        vmwait<0>(); sig(S_UK, lane);
        tag = __builtin_amdgcn_readfirstlane(TAGLOAD()) + 1;
        sig(S_TAG, lane);
        STUB(3);
        waitw<false>(S_PUB, 5);
        while (__builtin_amdgcn_s_memrealtime() - t0 < KSTART) __builtin_amdgcn_s_sleep(1);
        const size_t base = (((size_t)(l16 >> 3) * 8 + x) * 8192 + 6144 + (l16 & 7) * 16) * 256;
        cptr ks = mkptr(kb[6], kb[7]) + base, vs = mkptr(kb[8], kb[9]) + base;
        static_assert(WINB % 4 == 0 && WINB <= 32, "block signals");
        {
          u32 kv[4];
#pragma unroll
          for (int i = 0; i < 4; ++i) kv[i] = g * 256 + ((n ^ (4 * i + g)) * 16);
#pragma unroll 1
          for (u32 G = 0; G < 16; ++G) {
            dma4r<WINB>(ks + G * 32768, kv[0], kv[1], kv[2], kv[3], L_K + G * 4096);
            if (((G + 1 - WINB / 4) & 3) == 0 && G >= 3 + WINB / 4) { vmwait<WINB>(); sig(S_LAND, lane); }
          }
        }
        {
          const u32 v0 = g * 256 + ((n ^ (g << 1)) * 16), v1 = g * 256 + ((n ^ ((4 + g) << 1)) * 16);
#pragma unroll 1
          for (u32 G = 0; G < 16; ++G) {
            dma4r<WINB>(vs + G * 32768, v0, v1, v0, v1, L_V + G * 4096);
            if (((G + 1 - WINB / 4) & 3) == 0 && G + 16 >= 3 + WINB / 4) { vmwait<WINB>(); sig(S_LAND, lane); }
          }
        }
        waitw<false>(S_KRD, 4);
        {
          const u32 vo = (ht * 128 + g) * 1024 + q4 * 256, w0 = vo + ((n ^ (g << 1)) * 16), w1 = vo + ((n ^ ((g | 4) << 1)) * 16) + 6144;
#pragma unroll 1
          for (u32 G = 0; G < 16; ++G) {
            cptr wp = (G < 8 ? mkptr(kb[14], kb[15]) : mkptr(kb[12], kb[13])) + (G & 7) * 16384;
            dma4r<WINB>(wp, w0, w0 + 3072, w1, w1 + 3072, L_W1 + G * 4096);
            if (((G + 1 - WINB / 4) & 3) == 0) { vmwait<WINB>(); sig(S_LAND, lane); }
          }
        }
        vmwait<32>(); sig(S_W1, lane);
        vmwait<0>(); sig(S_W2, lane);
      } else if (wave == 1) {
        START(3);
        const u64 t1 = __builtin_amdgcn_s_memrealtime();
        __builtin_amdgcn_s_setprio(2);
        asm volatile("" :: "v"(warm));
        TOUCH(4); TOUCH(3); TOUCH(5); TOUCH(6); TOUCH(7); TOUCH(8); TOUCH(9); TOUCH(10);
        if (lane < 6) { u32 t = __builtin_amdgcn_raw_buffer_load_b32(rs, lane < 2 ? lane * 1048576 + 262144 - 64 : WS_APART + (lane - 1) * APART_BLK - 128, 0, 16); asm volatile("" :: "v"(t)); }
        tag = __builtin_amdgcn_readfirstlane(TAGLOAD()) + 1;
        waitw<false>(S_TAG, 3);
        waitw<false>(S_PUB, 4);
        {
          const v4u pv = lds128(L_QX + (lane & 31) * 16);
          if (lane < 32) ST1(pv, WS_QABS + (lane >> 4) * 32768 + ht * 1024 + q4 * 256 + n * 16);
          u32 cs = (pv.x + pv.y) + (pv.z + pv.w);
          cs += dppu<X1>(cs); cs += dppu<X2>(cs); cs += dppu<X7>(cs); cs += dppu<X8>(cs);
          auto t = __builtin_amdgcn_permlane16_swap(cs, cs, false, false);
          if (lane == 0) ST1((v4u{t[0] + tag, t[1] + tag, tag, tag}), WS_HINT + (x * 16 + l16) * 128);
          if (l16 == GATEL && lane == 0) __builtin_amdgcn_raw_buffer_store_b32(tag, rs, WS_GATE + x * 128, 0, 16);
        }
        sig(S_PUB, lane);
        while (__builtin_amdgcn_s_memrealtime() - t1 < QBSTART) __builtin_amdgcn_s_sleep(1);
        v4u e[16];
        for (int spin = 0; spin < SPIN_CAP; ++spin) {
          bool ok = true;
#pragma unroll
          for (int i = 0; i < 16; ++i) e[i] = LD1(WS_QB + x * 32768 + (l16 >> 3) * 16384 + i * 1024 + lane * 16);
          asm volatile("" ::: "memory");
#pragma unroll
          for (int i = 0; i < 16; ++i) ok = ok && e[i].y == tag && e[i].w == tag;
          if (__all(ok)) break;
        }
#pragma unroll
        for (int i = 0; i < 4; ++i)
          *lp<u32>(L_QBL + i * 256 + lane * 4) = pk((f_(e[i].x) + f_(e[4 + i].x)) + (f_(e[8 + i].x) + f_(e[12 + i].x)), (f_(e[i].z) + f_(e[4 + i].z)) + (f_(e[8 + i].z) + f_(e[12 + i].z)));
        sig(S_QBRDY, lane);
      } else {
        START(4);
        asm volatile("" :: "v"(warm));
        tag = __builtin_amdgcn_readfirstlane(TAGLOAD()) + 1;
        sig(S_TAG, lane);
      }
      STUB(10);
      waitw<true>(S_W2, 1);
      v4u a2[8];
#pragma unroll
      for (int s = 0; s < 8; ++s) { const u32 a = L_W2 + (ab ^ (32 * (s >> 2))) + (s & 3) * 8192; a2[s] = ldstr(a, a + 1024); }
      v4u e;
      for (int spin = 0; spin < SPIN_CAP; ++spin) {
        e = LD1(WS_H3B + ht * 4096 + w * 1024 + lane * 16);
        asm volatile("" ::: "memory");
        if (__all(e.y == tag && e.w == tag)) break;
      }
      lds64w(L_B2 + ((lane >> 5) * 4 + w) * 272 + (lane & 31) * 8, e.x, e.z);
      sig(S_TA, lane);
      waitw<false>(S_TA, 4);
      v4u bq[4];
#pragma unroll
      for (int s = 0; s < 4; ++s) bq[s] = lds128(L_B2 + (n & 7) * 272 + g * 16 + s * 64);
      SB();
      f32x4 d0 = {0.f, 0.f, 0.f, 0.f}, d1 = d0;
#pragma unroll
      for (int s = 0; s < 4; ++s) { d0 = mfma16(a2[s], bq[s], d0); d1 = mfma16(a2[4 + s], bq[s], d1); }
      f32x4 d = (n & 8) ? d1 : d0;
#pragma unroll
      for (int k = 0; k < 4; ++k) { float t = d[k]; t += dpp<X1>(t); t += dpp<X2>(t); d[k] = t; }
      const bool k1 = (n & 1) != 0;
      if ((n & 2) == 0) ST1((v4u{u_(k1 ? d[2] : d[0]), tag, u_(k1 ? d[3] : d[1]), tag}),
                            WS_H4 + (((q4 * 8 + 2 * w + (n >> 3)) * 2 + ((n >> 2) & 1)) * 32 + ht) * 128 + g * 32 + (n & 1) * 16);
    } else {
      START(5);
      tag = __builtin_amdgcn_readfirstlane(TAGLOAD()) + 1;
      __builtin_amdgcn_s_setprio(2);
      {
        waitw<false>(S_WQ, 1);
        PROJ((void)0, WS_H1 + (4 * x + (((l16 >> 2) + 1) & 3)) * 8192 + q4 * 2048 + (n & 1) * 1024);
        STUB(4);
        waitw<false>(S_UK, 1);
        v4u uk[8];
#pragma unroll
        for (int t = 0; t < 2; ++t)
#pragma unroll
          for (int s = 0; s < 4; ++s) uk[t * 4 + s] = lds128(L_UK + (32 * w + 8 * (n >> 2) + 4 * t + (n & 3)) * 256 + 16 * ((4 * s + g) ^ n));
        v4u e0, e1;
#define H1_LD() do { e0 = LD1(WS_H1 + ht * 8192 + g * 2048 + w * 256 + n * 16); e1 = LD1(WS_H1 + ht * 8192 + g * 2048 + 1024 + w * 256 + n * 16); \
                     asm volatile("" ::: "memory"); } while (0)
        H1_LD();
        for (int spin = 0; spin < SPIN_CAP; ++spin) {
          if (__all(e0.y == tag && e0.w == tag && e1.y == tag && e1.w == tag)) break;
          H1_LD();
        }
        {
          auto p = __builtin_amdgcn_permlane32_swap(e0.x, e1.x, false, false), q = __builtin_amdgcn_permlane32_swap(e0.z, e1.z, false, false);
          const float a = sum16(f_(p[0]) + f_(p[1])), c = sum16(f_(q[0]) + f_(q[1]));
          if ((g & 1) == 0) *lp<u32>(L_QL + (g >> 1) * 256 + w * 64 + n * 4) = pk(a, c);
        }
        sig(S_H1, lane);
        waitw<false>(S_H1, 4);
        v4u qb[4];
#pragma unroll
        for (int s = 0; s < 4; ++s) qb[s] = lds128(L_QL + (lane & 1) * 256 + g * 16 + s * 64);
        SB();
        f32x4 u0 = {0.f, 0.f, 0.f, 0.f}, u1 = u0;
#pragma unroll
        for (int s = 0; s < 4; ++s) { u0 = mfma16(uk[s], qb[s], u0); u1 = mfma16(uk[4 + s], qb[s], u1); }
        const v4u pv = {pk(u0[0], u0[1]), pk(u0[2], u0[3]), pk(u1[0], u1[1]), pk(u1[2], u1[3])};
        if (n < 2) lds128w(L_QX + n * 256 + w * 64 + g * 16, pv);
        sig(S_PUB, lane);
      }
      STUB(5);
      waitw<true>(S_LAND, w + 1);
      f32x4 sc[4];
      {
        v4u kf[16], qf[4];
        const u32 ka = L_K + (64 * w + n) * 256 + 16 * (g ^ n);
#pragma unroll
        for (int i = 0; i < 16; ++i) kf[i] = lds128((ka ^ (64 * (i & 3))) + (i >> 2) * 4096);
        sig(S_KRD, lane);
        SB();
        waitw<false>(S_QBRDY, 1);
#pragma unroll
        for (int s = 0; s < 4; ++s) qf[s] = lds128(L_QBL + (lane & 3) * 256 + g * 16 + s * 64);
        SB();
#pragma unroll
        for (int t = 0; t < 4; ++t) {
          sc[t] = f32x4{0.f, 0.f, 0.f, 0.f};
#pragma unroll
          for (int s = 0; s < 4; ++s) sc[t] = mfma16(kf[4 * t + s], qf[s], sc[t]);
        }
      }
      float m = sc[0][0];
#pragma unroll
      for (int i = 1; i < 16; ++i) m = fmaxf(m, sc[i >> 2][i & 3]);
      m = max32(max16(m));
      if (lane < 4) *lp<float>(L_MAXX + lane * 16 + w * 4) = m;
      sig(S_MAX, lane);
      STUB(6);
      waitw<false>(S_MAX, 4);
      const v4u mm = lds128(L_MAXX + (lane & 3) * 16);
      const float mk = fmaxf(fmaxf(f_(mm.x), f_(mm.y)), fmaxf(f_(mm.z), f_(mm.w))) * KSC;
      float ls = 0.f;
#pragma unroll
      for (int i = 0; i < 16; ++i) { const float p = __builtin_amdgcn_exp2f(fmaf(sc[i >> 2][i & 3], KSC, -mk)); sc[i >> 2][i & 3] = p; ls += p; }
      ls = sum32(sum16(ls));
      if (n < 4) {
#pragma unroll
        for (int u = 0; u < 2; ++u)
          lds128w(L_PB + (2 * w + u) * 256 + g * 64 + n * 16,
                  v4u{pk(sc[2 * u][0], sc[2 * u][1]), pk(sc[2 * u][2], sc[2 * u][3]), pk(sc[2 * u + 1][0], sc[2 * u + 1][1]), pk(sc[2 * u + 1][2], sc[2 * u + 1][3])});
      }
      if (lane < 4) *lp<float>(L_LSUM + lane * 16 + w * 4) = ls;
      sig(S_P, lane);
      STUB(7);
      waitw<false>(S_P, 4);
      v4u pf[8];
#pragma unroll
      for (int k = 0; k < 8; ++k) pf[k] = lds128(L_PB + k * 256 + g * 64 + (lane & 3) * 16);
      if (w == 0) {
        const v4u lv = lds128(L_LSUM + (lane & 3) * 16);
        const float lsum = (f_(lv.x) + f_(lv.y)) + (f_(lv.z) + f_(lv.w));
        if (lane < 4) ST1((v4u{u_(mk), tag, u_(lsum), tag}), WS_BIN + (4 * x + lane) * 9216 + 8192 + l16 * 16);
      }
      f32x4 o0 = {0.f, 0.f, 0.f, 0.f}, o1 = o0;
      const u32 va = L_V + (4 * g + tq) * 256 + 16 * ((4 * w + (tp >> 1)) ^ ((4 * (g & 1) + tq) << 1)) + 8 * (tp & 1);
#pragma unroll
      for (int k2 = 0; k2 < 4; ++k2) {
        waitw<false>(S_LAND, k2 + 5);
        v4u vf[4];
#pragma unroll
        for (int i = 0; i < 4; ++i) { const u32 a = (va ^ (32 * (i & 1))) + (2 * k2 + (i >> 1)) * 8192; vf[i] = ldstr(a, a + 4096); }
        SB();
        o0 = mfma16(vf[0], pf[2 * k2], o0); o1 = mfma16(vf[1], pf[2 * k2], o1);
        o0 = mfma16(vf[2], pf[2 * k2 + 1], o0); o1 = mfma16(vf[3], pf[2 * k2 + 1], o1);
      }
      {
        const f32x4 o = (n & 4) ? o1 : o0;
        if (n < 8) ST1((v4u{pk(o[0], o[1]), tag, pk(o[2], o[3]), tag}), WS_BIN + (4 * x + (n & 3)) * 9216 + l16 * 512 + (4 * (2 * w + (n >> 2)) + g) * 16);
      }
      STUB(8);
      waitw<false>(S_W1, 1);
      v4u a1[8];
#pragma unroll
      for (int s = 0; s < 8; ++s) { const u32 a = L_W1 + (ab ^ (32 * (s >> 2))) + (s & 3) * 8192; a1[s] = ldstr(a, a + 1024); }
      v4u e0, e1, st;
      for (int spin = 0; spin < SPIN_CAP; ++spin) {
        e0 = LD1(WS_BIN + ht * 9216 + w * 2048 + lane * 16);
        e1 = LD1(WS_BIN + ht * 9216 + w * 2048 + 1024 + lane * 16);
        st = LD1(WS_BIN + ht * 9216 + 8192 + n * 16);
        asm volatile("" ::: "memory");
        if (__all(e0.y == tag && e0.w == tag && e1.y == tag && e1.w == tag && st.y == tag && st.w == tag)) break;
      }
      lds64w(L_BB + (4 * w + (lane >> 5)) * 272 + (lane & 31) * 8, e0.x, e0.z);
      lds64w(L_BB + (4 * w + 2 + (lane >> 5)) * 272 + (lane & 31) * 8, e1.x, e1.z);
      sig(S_TB, lane);
      float M = f_(st.x);
      M = fmaxf(M, dpp<X1>(M)); M = fmaxf(M, dpp<X2>(M)); M = fmaxf(M, dpp<X7>(M));
      const float ee = __builtin_amdgcn_exp2f(f_(st.x) - M);
      float S = ee * f_(st.z);
      S += dpp<X1>(S); S += dpp<X2>(S); S += dpp<X7>(S);
      const float wt = ee / S;
      STUB(9);
      waitw<false>(S_TB, 4);
      v4u bq[4];
#pragma unroll
      for (int s = 0; s < 4; ++s) bq[s] = lds128(L_BB + n * 272 + g * 16 + s * 64);
      SB();
      f32x4 d0 = {0.f, 0.f, 0.f, 0.f}, d1 = d0;
#pragma unroll
      for (int s = 0; s < 4; ++s) { d0 = mfma16(a1[s], bq[s], d0); d1 = mfma16(a1[4 + s], bq[s], d1); }
#pragma unroll
      for (int k = 0; k < 4; ++k) {
        float t = d0[k] * wt, u = d1[k] * wt;
        t += dpp<X1>(t); u += dpp<X1>(u); t += dpp<X2>(t); u += dpp<X2>(u); t += dpp<X7>(t); u += dpp<X7>(u);
        d0[k] = (n & 2) ? u : t;
      }
      const bool k1 = (n & 1) != 0;
      if ((n & 4) == 0) ST1((v4u{u_(k1 ? d0[2] : d0[0]), tag, u_(k1 ? d0[3] : d0[1]), tag}),
                            WS_H4 + 262144 + (((q4 * 8 + 2 * w + ((n >> 1) & 1)) * 2 + (n >> 3)) * 32 + ht) * 128 + g * 32 + (n & 1) * 16);
    }
  }
}

extern "C" void kernel_launch(void* const* d_in, const int* in_sizes, int n_in, void* d_out, int out_size, void* d_ws, size_t ws_size,
                              hipStream_t stream) {
  (void)hipFuncSetAttribute((const void*)fused_decode, hipFuncAttributeMaxDynamicSharedMemorySize, 163840);
  fused_decode<<<256, 512, 163840, stream>>>((const u16*)d_in[0], (const u16*)d_in[2], (const u16*)d_in[6], (const u16*)d_in[9],
                                             (unsigned char*)d_ws, (const u16*)d_in[1], (const u16*)d_in[3], (const u16*)d_in[7],
                                             (const u16*)d_in[8], (const u16*)d_in[4], (const u16*)d_in[5], (const u16*)d_in[10],
                                             (u16*)d_out);
}
```
